```python
import torch

def _softmax(x, axis=-1):
    return torch.softmax(x, dim=axis)
def _gelu(x):
    return torch.nn.functional.gelu(x, approximate="tanh")
def _silu(x):
    return torch.nn.functional.silu(x)
def _sigmoid(x):
    return torch.sigmoid(x)
def _layernorm(x, axis=-1, eps=1e-5):
    m = x.mean(dim=axis, keepdim=True)
    v = x.var(dim=axis, keepdim=True, unbiased=False)
    return (x - m) / torch.sqrt(v + eps)
def _scatter_add(tgt, idx, src, axis=0):
    return tgt.clone().index_add_(axis, idx, src)
def _segment_sum(src, seg, n):
    out = torch.zeros((n,) + src.shape[1:], dtype=src.dtype)
    return out.index_add_(0, seg, src)
def _pad(x, pw):
    flat = tuple(v for pair in reversed(pw) for v in pair)
    return torch.nn.functional.pad(x, flat)

kernel_name = 'sequential_preLN__spectral_gfnet__dense_activation_silu__prompt'

def setup_inputs():

    _w = (lambda a: (lambda t: t.to(torch.bfloat16) if t.is_floating_point() else t)(torch.as_tensor(a)))
    def _A_setup():
        B, S, D = 1, 2048, 640
        Df = D // 2 + 1
        return {
            "x": _w((lambda *s: torch.randn(*s, dtype=torch.bfloat16) * 0.05)(B, S, D)),
            "w_r": _w((lambda *s: torch.randn(*s, dtype=torch.bfloat16) * 0.4)(S, Df) * 0.02),
            "w_i": _w((lambda *s: torch.randn(*s, dtype=torch.bfloat16) * 0.4)(S, Df) * 0.02),
        }
    _A_in = _A_setup()

    def _B_setup():
        B, S, D, F = 1, 2048, 640, 1728
        return {
            "x": _w((lambda *s: torch.randn(*s, dtype=torch.bfloat16) * 0.05)(B, S, D)),
            "w1": _w((lambda *s: torch.randn(*s, dtype=torch.bfloat16) * 0.8)(D, F) * (D ** -0.5)),
            "w2": _w((lambda *s: torch.randn(*s, dtype=torch.bfloat16) * 0.1)(F, D) * (F ** -0.5)),
        }
    _B_in = _B_setup()

    return {
        "A_x": _A_in["x"],
        "A_w_r": _A_in["w_r"],
        "A_w_i": _A_in["w_i"],
        "B_x": _B_in["x"],
        "B_w1": _B_in["w1"],
        "B_w2": _B_in["w2"],
    }

def reference(A_x, A_w_r, A_w_i, B_x, B_w1, B_w2):
    def _A_ref(x, w_r, w_i):
        B, S, D = x.shape
        X = (lambda x, axes=(-2,-1): torch.fft.rfft2(x.float(), dim=axes))(x, axes=(-2, -1))
        W = w_r + 1j * w_i
        X = X * W
        y = (lambda x, s, axes=(-2,-1): torch.fft.irfft2(x, s=s, dim=axes).to(torch.bfloat16))(X, (S, D), axes=(-2, -1))
        return y

    def _B_ref(x, w1, w2):
        h = torch.einsum("bsd,df->bsf", x, w1)
        h = _silu(h)
        out = torch.einsum("bsf,fd->bsd", h, w2)
        return out

    x = A_x
    h = x + _A_ref(_layernorm(x, axis=-1), A_w_r, A_w_i)
    h2 = h + _B_ref(_layernorm(h, axis=-1), B_w1, B_w2)
    return h2
```

```cpp
#include <hip/hip_runtime.h>
#include <stdint.h>

typedef unsigned int u32;
typedef unsigned short u16;
typedef unsigned long long u64;
typedef __attribute__((ext_vector_type(2))) u32 u32x2;
typedef __attribute__((ext_vector_type(4))) u32 u32x4;
typedef __attribute__((ext_vector_type(2))) float f32x2;
typedef __attribute__((ext_vector_type(4))) float f32x4;
typedef __attribute__((ext_vector_type(16))) float f32x16;
typedef __attribute__((ext_vector_type(8))) __bf16 bf16x8;
typedef __attribute__((ext_vector_type(2))) __bf16 bf16x2;
typedef __attribute__((ext_vector_type(4))) short s16x4;
typedef __amdgpu_buffer_rsrc_t rsrc_t;
#define DI __device__ __forceinline__
#define LAS __attribute__((address_space(3)))

constexpr int SEQ = 2048, DM = 640, FF = 1728, DF = 321;
constexpr u32 SZ = 2621440u;
constexpr u32 OFF_U = 0, OFF_WT = SZ, OFF_B1 = 2 * SZ, OFF_H = 3 * SZ, OFF_HB = 4 * SZ, OFF_S = 5 * SZ;
constexpr u32 OFF_STD = OFF_S + 128u * 54u * 1024u, OFF_MEAN = OFF_STD + 8192u, WS_USED = OFF_MEAN + 8192u;
constexpr float LN_EPS = 1e-5f;
#define ST_AUX 16

constexpr double c_cos_rev(int num, int den) {
  num %= den;
  if (2 * num > den) num = den - num;
  bool neg = false;
  if (4 * num > den) { neg = true; num = den - 2 * num; den = 2 * den; }
  if (4 * num == den) return 0.0;
  const double x = 6.283185307179586476925 * num / den, x2 = x * x;
  double term = 1.0, sum = 1.0;
  for (int i = 1; i <= 12; ++i) { term *= -x2 / ((2 * i - 1) * (2 * i)); sum += term; }
  return neg ? -sum : sum;
}
template <int N> struct CosTab { double c[N]; };
template <int N> constexpr CosTab<N> make_cos() { CosTab<N> t{}; for (int i = 0; i < N; ++i) t.c[i] = c_cos_rev(i, N); return t; }
constexpr CosTab<640> C640 = make_cos<640>();
constexpr CosTab<2048> C2048 = make_cos<2048>();
constexpr double cos640(int n) { return C640.c[n % 640]; }
constexpr double sin640(int n) { return C640.c[(n + 480) % 640]; }
constexpr double cos2048(int n) { return C2048.c[n % 2048]; }
constexpr double sin2048(int n) { return C2048.c[(n + 1536) % 2048]; }
constexpr u16 c_bf16(double v) { u32 u = __builtin_bit_cast(u32, (float)v); u += 0x7fffu + ((u >> 16) & 1u); return (u16)(u >> 16); }
constexpr double SC2 = 0.055901699437494742410, SC1 = 0.039528470752104741650;

__host__ __device__ constexpr int slot_k1(int sg) { return sg < 10 ? 2 * sg : 2 * (sg - 10) + 1; }
__host__ __device__ constexpr int col_k(int sg, int c) { return sg == 0 ? (c == 0 ? 320 : (c < 8 ? 40 * c : 40 * c - 300)) : slot_k1(sg) + 40 * c; }
__host__ __device__ constexpr int col_kd(int sg, int c) { return col_k(sg, c) > 320 ? 640 - col_k(sg, c) : col_k(sg, c); }

struct alignas(16) F40Tab { u16 v[4][3][64][8]; };
constexpr F40Tab make_f40() {
  F40Tab t{};
  for (int w = 0; w < 4; ++w) for (int st = 0; st < 3; ++st) for (int l = 0; l < 64; ++l) {
    const int q = w & 1, m = w >> 1, r = l & 31, h = l >> 5;
    const int reg = (r & 3) + 4 * (r >> 3), comp = (r >> 2) & 1;
    if (reg >= 10) continue;
    const int sg = 10 * m + (reg < 5 ? 5 * q + reg : 5 * (1 - q) + reg - 5);
    for (int j = 0; j < (st < 2 ? 8 : 4); ++j) {
      const int lam = 8 * st + j, a = 8 * (lam >> 2) + 4 * h + ((lam & 3) ^ (2 * m));
      const double v = sg == 0 ? (comp == 0 ? 1.0 : ((a & 1) ? -1.0 : 1.0))
                               : (comp == 0 ? cos640(16 * a * slot_k1(sg)) : -sin640(16 * a * slot_k1(sg)));
      t.v[w][st][l][j] = c_bf16(v);
    }
  }
  return t;
}
struct alignas(16) TTab { u16 v[20][2][64][8]; };
constexpr TTab make_tt() {
  TTab t{};
  for (int sg = 0; sg < 20; ++sg) for (int q = 0; q < 2; ++q) for (int l = 0; l < 64; ++l) {
    const int kc = l & 31, h = l >> 5, c = kc >> 1, e = kc & 1, k = col_k(sg, c);
    for (int j = 0; j < 8; ++j) {
      const int b = 8 * q + j;
      double v = 0.0;
      if (sg == 0) {
        if (c == 0) v = (e == 0 && h == 0) ? SC1 * cos640(b * k) : 0.0;
        else if (h == (c >= 8 ? 1 : 0)) v = e == 0 ? SC2 * cos640(b * k) : -SC2 * sin640(b * k);
      } else {
        v = e == 0 ? (h == 0 ? cos640(b * k) : sin640(b * k)) : (h == 0 ? -sin640(b * k) : cos640(b * k));
        v *= (e == 1 && k > 320) ? -SC2 : SC2;
      }
      t.v[sg][q][l][j] = c_bf16(v);
    }
  }
  return t;
}
struct alignas(16) K2Tab { u16 f64[4][2][2][64][8]; u16 f32[2][2][64][8]; float tw1[4][2][2][64][4]; float tw2[4][2][2][64][4]; };
constexpr K2Tab make_k2() {
  K2Tab t{};
  for (int l = 0; l < 64; ++l) {
    const int r = l & 15, g = l >> 4;
    for (int T = 0; T < 2; ++T) {
      for (int j = 0; j < 8; ++j) {
        const int o = 16 * (j >> 2) + 4 * g + (j & 3);
        for (int w = 0; w < 4; ++w) {
          t.f64[w][T][0][l][j] = c_bf16(cos2048(32 * (32 * T + o) * (16 * w + r)));
          t.f64[w][T][1][l][j] = c_bf16(sin2048(32 * (32 * T + o) * (16 * w + r)));
        }
        t.f32[T][0][l][j] = c_bf16(cos2048(64 * o * (16 * T + r)));
        t.f32[T][1][l][j] = c_bf16(sin2048(64 * o * (16 * T + r)));
      }
      for (int w = 0; w < 4; ++w) for (int i = 0; i < 4; ++i) {
        t.tw1[w][T][0][l][i] = (float)cos2048((16 * w + r) * (16 * T + 4 * g + i));
        t.tw1[w][T][1][l][i] = (float)sin2048((16 * w + r) * (16 * T + 4 * g + i));
        t.tw2[w][T][0][l][i] = (float)(cos2048((16 * w + 4 * g + i) * (16 * T + r)) / 2048.0);
        t.tw2[w][T][1][l][i] = (float)(sin2048((16 * w + 4 * g + i) * (16 * T + r)) / 2048.0);
      }
    }
  }
  return t;
}
static __device__ const F40Tab g_f40 = make_f40();
static __device__ const TTab g_tt = make_tt();
static __device__ const K2Tab g_k2 = make_k2();

DI rsrc_t mk_rsrc(const void* p, u32 bytes) { return __builtin_amdgcn_make_buffer_rsrc((void*)p, (short)0, (int)bytes, 0x00020000); }
DI u32 rfl(u32 v) { return __builtin_amdgcn_readfirstlane(v); }
DI u32x4 ld128(rsrc_t r, u32 voff, u32 soff = 0) { return __builtin_amdgcn_raw_buffer_load_b128(r, voff, soff, 0); }
DI u32x2 ld64(rsrc_t r, u32 voff, u32 soff = 0) { return __builtin_amdgcn_raw_buffer_load_b64(r, voff, soff, 0); }
DI void st128(u32x4 v, rsrc_t r, u32 voff, u32 soff = 0) { __builtin_amdgcn_raw_buffer_store_b128(v, r, voff, soff, ST_AUX); }
DI void dma16(rsrc_t r, u32 voff, u32 soff, u32 lds) {
  asm volatile("s_mov_b32 m0, %0\n\ts_nop 0\n\tbuffer_load_dwordx4 %1, %2, %3 offen lds" :: "s"(lds), "v"(voff), "s"(r), "s"(soff) : "memory");
}
#define WAIT_VM(n) asm volatile("s_waitcnt vmcnt(" #n ")" ::: "memory")
#define BAR() do { asm volatile("s_waitcnt lgkmcnt(0)" ::: "memory"); __builtin_amdgcn_s_barrier(); asm volatile("" ::: "memory"); } while (0)
DI u32 pk(float a, float b) { f32x2 v = {a, b}; return __builtin_bit_cast(u32, __builtin_convertvector(v, bf16x2)); }
DI float asf(u32 v) { return __builtin_bit_cast(float, v); }
DI u32 asu(float v) { return __builtin_bit_cast(u32, v); }
DI float bf_lo(u32 v) { return __builtin_bit_cast(float, v << 16); }
DI float bf_hi(u32 v) { return __builtin_bit_cast(float, v & 0xffff0000u); }
DI f32x16 mfma32(u32x4 a, u32x4 b, f32x16 c) { return __builtin_amdgcn_mfma_f32_32x32x16_bf16(__builtin_bit_cast(bf16x8, a), __builtin_bit_cast(bf16x8, b), c, 0, 0, 0); }
DI f32x16 mfma32h(u32x2 a, u32x2 b, f32x16 c) { return __builtin_amdgcn_mfma_f32_32x32x8bf16_1k(__builtin_bit_cast(s16x4, a), __builtin_bit_cast(s16x4, b), c, 0, 0, 0); }
DI f32x4 mfma16(u32x4 a, u32x4 b, f32x4 c) { return __builtin_amdgcn_mfma_f32_16x16x32_bf16(__builtin_bit_cast(bf16x8, a), __builtin_bit_cast(bf16x8, b), c, 0, 0, 0); }
DI float dot2(u32 a, u32 b, float c) { return __builtin_amdgcn_fdot2_f32_bf16(__builtin_bit_cast(bf16x2, a), __builtin_bit_cast(bf16x2, b), c, false); }
DI u32x4 lds128(u32 a) { return *(LAS u32x4*)(size_t)a; }
DI u32x2 lds64(u32 a) { return *(LAS u32x2*)(size_t)a; }
DI void lds128w(u32 a, u32x4 v) { *(LAS u32x4*)(size_t)a = v; }
DI void lds64w(u32 a, u32x2 v) { *(LAS u32x2*)(size_t)a = v; }
DI u32x2 ldstr(u32 a) { return __builtin_bit_cast(u32x2, __builtin_amdgcn_ds_read_tr16_b64_v4i16((LAS s16x4*)(size_t)a)); }
DI void swap16(u32& a, u32& b) { auto r = __builtin_amdgcn_permlane16_swap(a, b, false, false); a = r[0]; b = r[1]; }
DI float swap32_add(float v) { u32 u = __builtin_bit_cast(u32, v); auto r = __builtin_amdgcn_permlane32_swap(u, u, false, false);
  return __builtin_bit_cast(float, (u32)r[0]) + __builtin_bit_cast(float, (u32)r[1]); }
DI u32x4 neg8(u32x4 v) { return v ^ 0x80008000u; }
extern __shared__ __attribute__((aligned(16))) char smem[];
DI u32 lds_base() { return (u32)(size_t)(LAS char*)smem; }

#define STAMP(k, i)

template <bool IS_X>
DI void dfft32(int unit, const void* src, char* ws) {
  const u32 tid = threadIdx.x, lane = tid & 63, w = rfl(tid >> 6), q = w & 1, m = w >> 1;
  const u32 rho = lane & 31, h = lane >> 5, L0 = lds_base();
  const rsrc_t rw = mk_rsrc(ws, WS_USED);
  const rsrc_t rf = mk_rsrc(&g_f40, sizeof(F40Tab)), rt = mk_rsrc(&g_tt, sizeof(TTab));
  const u32x4 F0 = ld128(rf, lane * 16, w * 3072), F1 = ld128(rf, lane * 16 + 1024, w * 3072);
  const u32x2 F2 = ld64(rf, lane * 16 + 2048, w * 3072);
  if constexpr (IS_X) {
    const rsrc_t rx = mk_rsrc(src, SEQ * DM * 2);
    const u32 T = unit >> 5, n2 = unit & 31;
#pragma unroll
    for (int i = 0; i < 10; ++i) {
      const u32 ci = i * 64 + lane, rl = ci / 80, c = ci - rl * 80, row = w * 8 + rl;
      const u32 nu = (row & 7) | ((row & 8) << 1) | ((row & 16) >> 1);
      dma16(rx, ((32 * T + nu) * 32 + n2) * 1280 + ((c ^ (row & 15)) << 4), 0, L0 + w * 10240 + i * 1024);
    }
  } else {
    const rsrc_t rx = mk_rsrc(src, DM * FF * 2);
#pragma unroll
    for (int i = 0; i < 10; ++i) {
      const u32 a = i * 4 + w;
      dma16(rx, ((lane >> 2) ^ w) * (FF * 2) + (lane & 3) * 16, a * 16 * (FF * 2) + unit * 64, L0 + a * 1024);
    }
  }
  u32x4 TA[5], TB[5];
#pragma unroll
  for (int i = 0; i < 5; ++i) {
    TA[i] = ld128(rt, lane * 16, ((10 * m + 5 * q + i) * 2 + q) * 1024);
    TB[i] = ld128(rt, lane * 16, ((10 * m + 5 * q + i) * 2 + (q ^ 1)) * 1024);
  }
  __builtin_amdgcn_sched_barrier(0);
  WAIT_VM(10);
  BAR();
  f32x16 acc[8];
  const f32x16 zero = {};
  float rstd = 1.f;
  constexpr u32 XCH = 40960, PST = XCH + 20480;
  if constexpr (IS_X) {
    u32x4 Ld[20];
#pragma unroll
    for (int l = 0; l < 20; ++l) Ld[l] = lds128(L0 + rho * 1280 + (((8 * h + 4 * m + q) ^ (rho & 15) ^ (2 * (l & 3))) << 4) + (l >> 2) * 256);
    float s1 = 0.f, s2 = 0.f;
#pragma unroll
    for (int l = 0; l < 20; ++l) if ((l & 2) == 0) {
#pragma unroll
      for (int i = 0; i < 4; ++i) { s1 = dot2(Ld[l][i], 0x3f803f80u, s1); s2 = dot2(Ld[l][i], Ld[l][i], s2); }
    }
    lds64w(L0 + PST + (w * 64 + lane) * 8, u32x2{__builtin_bit_cast(u32, s1), __builtin_bit_cast(u32, s2)});
#pragma unroll
    for (int b = 0; b < 8; ++b) {
      u32 f[10];
#pragma unroll
      for (int k = 0; k < 10; ++k) f[k] = __builtin_amdgcn_perm(Ld[2 * k + 1][b >> 1], Ld[2 * k][b >> 1], (b & 1) ? 0x07060302u : 0x05040100u);
      acc[b] = mfma32(F0, u32x4{f[0], f[1], f[2], f[3]}, zero);
      acc[b] = mfma32(F1, u32x4{f[4], f[5], f[6], f[7]}, acc[b]);
      acc[b] = mfma32h(F2, u32x2{f[8], f[9]}, acc[b]);
    }
    BAR();
    float t1 = 0.f, t2 = 0.f;
#pragma unroll
    for (int ww = 0; ww < 4; ++ww) { const u32x2 p = lds64(L0 + PST + (ww * 64 + lane) * 8); t1 += asf(p[0]); t2 += asf(p[1]); }
    t1 = swap32_add(t1); t2 = swap32_add(t2);
    const float mean = t1 * (1.f / DM), var = fmaxf(t2 * (1.f / DM) - mean * mean, 0.f) + LN_EPS;
    rstd = __builtin_amdgcn_rsqf(var);
    if (w == 0) {
      const u32 T = unit >> 5, n2 = unit & 31, nu = (rho & 7) | ((rho & 8) << 1) | ((rho & 16) >> 1);
      const u32 n1 = 32 * T + nu;
      const u32 idx = (((n1 >> 4) * 2 + (n2 >> 4)) * 64 + ((n1 >> 2) & 3) * 16 + (n2 & 15)) * 4 + (n1 & 3);
      const float v = h ? mean * 25.298221281347035f : var * rstd;
      __builtin_amdgcn_raw_buffer_store_b32(__builtin_bit_cast(u32, v), rw, OFF_STD + h * 8192 + idx * 4, 0, ST_AUX);
    }
  } else {
    const u32 qq = (lane & 15) >> 2, p = lane & 3, blk = (lane >> 4) & 1, al = qq ^ (2 * m);
#pragma unroll
    for (int b = 0; b < 8; ++b) {
      const u32 ad = L0 + (4 * h + al) * 1024 + q * 512 + (((b & 3) ^ al) << 6) + (b >> 2) * 256 + blk * 32 + p * 8;
      const u32x2 f0 = ldstr(ad), f1 = ldstr(ad + 8192), f2 = ldstr(ad + 16384), f3 = ldstr(ad + 24576), f4 = ldstr(ad + 32768);
      acc[b] = mfma32(F0, u32x4{f0[0], f0[1], f1[0], f1[1]}, zero);
      acc[b] = mfma32(F1, u32x4{f2[0], f2[1], f3[0], f3[1]}, acc[b]);
      acc[b] = mfma32h(F2, f4, acc[b]);
    }
  }
  u32x4 P[10];
#pragma unroll
  for (int i = 0; i < 10; ++i)
#pragma unroll
    for (int k = 0; k < 4; ++k) P[i][k] = IS_X ? pk(acc[2 * k][i] * rstd, acc[2 * k + 1][i] * rstd) : pk(acc[2 * k][i], acc[2 * k + 1][i]);
#pragma unroll
  for (int i = 0; i < 5; ++i) lds128w(L0 + XCH + (w * 5 + i) * 1024 + lane * 16, P[5 + i]);
  BAR();
#pragma unroll
  for (int i = 0; i < 5; ++i) {
    const u32x4 R = lds128(L0 + XCH + ((w ^ 1) * 5 + i) * 1024 + lane * 16);
    f32x16 o;
    if constexpr (IS_X) { o = mfma32(P[i], TA[i], zero); o = mfma32(R, TB[i], o); }
    else { o = mfma32(TA[i], P[i], zero); o = mfma32(TB[i], R, o); }
    const u32 sg = 10 * m + 5 * q + i;
#pragma unroll
    for (int hf = 0; hf < 2; ++hf) {
      const u32x4 v = {pk(o[8 * hf], o[8 * hf + 1]), pk(o[8 * hf + 2], o[8 * hf + 3]), pk(o[8 * hf + 4], o[8 * hf + 5]), pk(o[8 * hf + 6], o[8 * hf + 7])};
      if constexpr (IS_X) {
        const u32 r = unit & 15, sw = (r & 10) | ((r & 1) << 2) | ((r >> 2) & 1);
        st128(v, rw, (rho >> 2) * 16384 + (((8 * hf + 4 * h + (rho & 3)) ^ sw) << 4), OFF_U + sg * 8 * 16384 + unit * 256);
      } else {
        st128(v, rw, lane * 16, OFF_B1 + (unit * 40 + sg * 2 + hf) * 1024);
      }
    }
  }
}

DI void gen_g(int chi, char* ws) {
  const u32 tid = threadIdx.x, lane = tid & 63, w = rfl(tid >> 6), r = lane & 31, h = lane >> 5;
  const rsrc_t rw = mk_rsrc(ws, WS_USED);
  const int d = 32 * chi + ((r & 19) | ((r & 8) >> 1) | ((r & 4) << 1));
  for (int i = 0; i < 10; ++i) {
    const int ks = w * 10 + i, sg = ks >> 1, hf = ks & 1;
    u32x4 v;
#pragma unroll
    for (int j = 0; j < 4; ++j) {
      const int c = 8 * hf + 4 * (j >> 1) + 2 * h + (j & 1);
      const int kd = col_kd(sg, c);
      const float ang = (float)((kd * d) % 640) * (1.f / 640.f);
      float cs = __builtin_amdgcn_cosf(ang) * (float)SC2, sn = __builtin_amdgcn_sinf(ang) * -(float)SC2;
      if (kd == 320) { cs = (d & 1) ? -(float)SC1 : (float)SC1; sn = (float)SC1; }
      v[j] = pk(cs, sn);
    }
    st128(v, rw, lane * 16, OFF_B1 + ((54 + chi) * 40 + ks) * 1024);
  }
}

DI void wtrans(int beta, const void* wr, const void* wi, char* ws) {
  const u32 tid = threadIdx.x, lane = tid & 63, w = rfl(tid >> 6), L0 = lds_base();
  const u32 kp = beta >> 3, o = beta & 7;
  const rsrc_t rs = mk_rsrc((w & 2) ? wi : wr, SEQ * DF * 2), rw = mk_rsrc(ws, WS_USED);
  const u32 row0 = 8 * o + 64 * (2 * kp + (w & 1));
#pragma unroll
  for (int i = 0; i < 6; ++i) dma16(rs, lane * 16, row0 * (DF * 2) + i * 1024, L0 + w * 6144 + i * 1024);
  WAIT_VM(0);
  BAR();
  const u32 dst = (((o >> 1) * 2 + (kp >> 3)) * 2 + (kp & 1)) * 1024 + (((kp >> 1) & 3) * 16 + 8 * (o & 1) + (tid & 7)) * 16;
#pragma unroll
  for (int it = 0; it < 5; ++it) {
    const int pi = (tid >> 3) + 32 * it, sg = pi >> 3, c = (pi & 7) * 2;
    u32x4 v;
#pragma unroll
    for (int col = 0; col < 2; ++col) {
      const u32 ad = L0 + (tid & 7) * (DF * 2) + col_kd(sg, c + col) * 2;
#pragma unroll
      for (int k = 0; k < 2; ++k)
        v[col * 2 + k] = (u32) * (LAS u16*)(size_t)(ad + k * 6144) | ((u32) * (LAS u16*)(size_t)(ad + k * 6144 + 12288) << 16);
    }
    st128(v, rw, pi * 16384 + dst, OFF_WT);
  }
}

__global__ __launch_bounds__(256, 2) void k1(const void* x, const void* wr, const void* wi, const void* w1, char* ws) {
  STAMP(0, 0);
  const int b = blockIdx.x;
  if (b < 64) dfft32<true>(b, x, ws);
  else if (b < 128) {
    const int g = b - 64, chunk = (g & 48) | ((g & 7) << 1) | ((g >> 3) & 1);
    if (chunk < 54) dfft32<false>(chunk, w1, ws);
    else gen_g(chunk - 54, ws);
  } else wtrans(b - 128, wr, wi, ws);
  STAMP(0, 1);
}

__global__ __launch_bounds__(256, 2) void k2(char* ws) {
  STAMP(1, 0);
  const u32 tid = threadIdx.x, lane = tid & 63, w = rfl(tid >> 6), r = lane & 15, g = lane >> 4, L0 = lds_base(), pi = blockIdx.x;
  const rsrc_t rw = mk_rsrc(ws, WS_USED), rk = mk_rsrc(&g_k2, sizeof(K2Tab));
#pragma unroll
  for (int i = 0; i < 4; ++i) dma16(rw, lane * 16, OFF_U + pi * 16384 + (w * 4 + i) * 1024, L0 + (w * 4 + i) * 1024);
  u32x4 fc[2], fs[2], fn[2], gc[2], gs[2], gn[2], Wt[2][2];
  f32x4 c1[2], s1[2], c2[2], s2[2], sd[2];
#pragma unroll
  for (int t = 0; t < 2; ++t) { fc[t] = ld128(rk, lane * 16, (w * 4 + t * 2) * 1024); fs[t] = ld128(rk, lane * 16, (w * 4 + t * 2 + 1) * 1024); }
#pragma unroll
  for (int t = 0; t < 2; ++t) {
    c1[t] = __builtin_bit_cast(f32x4, ld128(rk, lane * 16, 20480 + (w * 4 + t * 2) * 1024));
    s1[t] = __builtin_bit_cast(f32x4, ld128(rk, lane * 16, 20480 + (w * 4 + t * 2 + 1) * 1024));
    gc[t] = ld128(rk, lane * 16, 16384 + (t * 2) * 1024); gs[t] = ld128(rk, lane * 16, 16384 + (t * 2 + 1) * 1024);
  }
#pragma unroll
  for (int t = 0; t < 2; ++t)
#pragma unroll
    for (int ip = 0; ip < 2; ++ip) Wt[t][ip] = ld128(rw, lane * 16, OFF_WT + pi * 16384 + ((w * 2 + t) * 2 + ip) * 1024);
#pragma unroll
  for (int t = 0; t < 2; ++t) {
    c2[t] = __builtin_bit_cast(f32x4, ld128(rk, lane * 16, 36864 + (w * 4 + t * 2) * 1024));
    s2[t] = __builtin_bit_cast(f32x4, ld128(rk, lane * 16, 36864 + (w * 4 + t * 2 + 1) * 1024));
    sd[t] = __builtin_bit_cast(f32x4, ld128(rw, lane * 16, OFF_STD + (w * 2 + t) * 1024));
  }
  __builtin_amdgcn_sched_barrier(0);
  WAIT_VM(22);
  BAR();
#pragma unroll
  for (int t = 0; t < 2; ++t) { fn[t] = neg8(fs[t]); gn[t] = neg8(gs[t]); }
  const u32 sw = (r & 10) | ((r & 1) << 2) | ((r >> 2) & 1);
  const f32x4 z4 = {};
  constexpr u32 XCH = 16384;
#pragma unroll
  for (int col = 0; col < 2; ++col) {
    f32x4 re[2], im[2];
#pragma unroll
    for (int t = 0; t < 2; ++t) {
      re[t] = z4; im[t] = z4;
#pragma unroll
      for (int T = 0; T < 2; ++T) {
        const u32x4 ur = lds128(L0 + (T * 32 + t * 16 + r) * 256 + (((4 * g + 2 * col) ^ sw) << 4));
        const u32x4 ui = lds128(L0 + (T * 32 + t * 16 + r) * 256 + (((4 * g + 2 * col + 1) ^ sw) << 4));
        re[t] = mfma16(ur, fc[T], re[t]); re[t] = mfma16(ui, fs[T], re[t]);
        im[t] = mfma16(ui, fc[T], im[t]); im[t] = mfma16(ur, fn[T], im[t]);
      }
    }
    u32x4 br, bi;
#pragma unroll
    for (int t = 0; t < 2; ++t) {
      const f32x4 a = re[t] * c1[t] + im[t] * s1[t], b = im[t] * c1[t] - re[t] * s1[t];
      br[2 * t] = pk(a[0], a[1]); br[2 * t + 1] = pk(a[2], a[3]); bi[2 * t] = pk(b[0], b[1]); bi[2 * t + 1] = pk(b[2], b[3]);
    }
#pragma unroll
    for (int t = 0; t < 2; ++t) {
      re[t] = mfma16(gc[t], br, z4); re[t] = mfma16(gs[t], bi, re[t]);
      im[t] = mfma16(gc[t], bi, z4); im[t] = mfma16(gn[t], br, im[t]);
    }
#pragma unroll
    for (int t = 0; t < 2; ++t) {
      const f32x4 wrv = {bf_lo(Wt[t][0][col * 2]), bf_lo(Wt[t][0][col * 2 + 1]), bf_lo(Wt[t][1][col * 2]), bf_lo(Wt[t][1][col * 2 + 1])};
      const f32x4 wiv = {bf_hi(Wt[t][0][col * 2]), bf_hi(Wt[t][0][col * 2 + 1]), bf_hi(Wt[t][1][col * 2]), bf_hi(Wt[t][1][col * 2 + 1])};
      const f32x4 a = re[t] * wrv - im[t] * wiv, b = re[t] * wiv + im[t] * wrv;
      br[2 * t] = pk(a[0], a[1]); br[2 * t + 1] = pk(a[2], a[3]); bi[2 * t] = pk(b[0], b[1]); bi[2 * t + 1] = pk(b[2], b[3]);
    }
#pragma unroll
    for (int t = 0; t < 2; ++t) {
      re[t] = mfma16(br, gc[t], z4); re[t] = mfma16(bi, gn[t], re[t]);
      im[t] = mfma16(bi, gc[t], z4); im[t] = mfma16(br, gs[t], im[t]);
    }
#pragma unroll
    for (int t = 0; t < 2; ++t) {
      const f32x4 a = re[t] * c2[t] - im[t] * s2[t], b = im[t] * c2[t] + re[t] * s2[t];
      const u32 ad = L0 + XCH + ((col * 2 + t) * 4 + (w >> 1)) * 1024 + lane * 16 + (w & 1) * 8;
      lds64w(ad, u32x2{pk(a[0], a[1]), pk(a[2], a[3])});
      lds64w(ad + 2048, u32x2{pk(b[0], b[1]), pk(b[2], b[3])});
    }
  }
  BAR();
#pragma unroll
  for (int t = 0; t < 2; ++t) {
    f32x4 zr[2], zi[2];
#pragma unroll
    for (int col = 0; col < 2; ++col) {
      const u32 ua = L0 + ((w >> 1) * 32 + t * 16 + r) * 256 + (w & 1) * 8;
      const u32x2 ur = lds64(ua + (((4 * g + 2 * col) ^ sw) << 4)), ui = lds64(ua + (((4 * g + 2 * col + 1) ^ sw) << 4));
      zr[col] = f32x4{bf_lo(ur[0]), bf_hi(ur[0]), bf_lo(ur[1]), bf_hi(ur[1])} * sd[t];
      zi[col] = f32x4{bf_lo(ui[0]), bf_hi(ui[0]), bf_lo(ui[1]), bf_hi(ui[1])} * sd[t];
#pragma unroll
      for (int T = 0; T < 2; ++T) {
        const u32 ad = L0 + XCH + ((col * 2 + t) * 4 + T) * 1024 + lane * 16;
        const u32x4 cr = lds128(ad), ci = lds128(ad + 2048);
        zr[col] = mfma16(fc[T], cr, zr[col]); zr[col] = mfma16(fn[T], ci, zr[col]);
        zi[col] = mfma16(fc[T], ci, zi[col]); zi[col] = mfma16(fs[T], cr, zi[col]);
      }
    }
    if (pi == 0) zi[0] = __builtin_bit_cast(f32x4, ld128(rw, lane * 16, OFF_MEAN + (w * 2 + t) * 1024));
#pragma unroll
    for (int ip = 0; ip < 2; ++ip) {
      const u32x4 v = {pk(zr[0][2 * ip], zi[0][2 * ip]), pk(zr[1][2 * ip], zi[1][2 * ip]), pk(zr[0][2 * ip + 1], zi[0][2 * ip + 1]), pk(zr[1][2 * ip + 1], zi[1][2 * ip + 1])};
      st128(v, rw, lane * 16, OFF_H + pi * 16384 + ((w * 2 + t) * 2 + ip) * 1024);
    }
  }
  STAMP(1, 1);
}

__global__ __launch_bounds__(256, 2) void k3(char* ws) {
  STAMP(2, 0);
  const u32 tid = threadIdx.x, lane = tid & 63, w = rfl(tid >> 6), h = lane >> 5, L0 = lds_base(), b = blockIdx.x;
  const u32 P = 2 * (b & 7) + ((b >> 3) & 1), C = b >> 4;
  const rsrc_t rw = mk_rsrc(ws, WS_USED);
#pragma unroll
  for (int i = 0; i < 40; ++i) dma16(rw, lane * 16, OFF_B1 + C * 163840 + (i * 4 + w) * 1024, L0 + (i * 4 + w) * 1024);
  u32x4 A[40];
  const u32 gam = lane >> 4, av = ((gam >> 1) + 2 * (gam & 1)) * 16384 + (lane & 15) * 16;
#pragma unroll
  for (int k = 0; k < 40; ++k) A[k] = ld128(rw, av, OFF_H + k * 65536 + P * 1024 + w * 256);
  __builtin_amdgcn_sched_barrier(0);
  WAIT_VM(0);
  BAR();
  float ss = 0.f;
#pragma unroll
  for (int k = 0; k < 40; ++k) {
    u32 a0 = A[k][0], a1 = A[k][1], a2 = A[k][2], a3 = A[k][3];
    swap16(a0, a2); swap16(a1, a3);
    A[k] = u32x4{a0, a1, a2, a3};
    if (k == 0) a0 &= h ? 0xffffffffu : 0x0000ffffu;
    ss = dot2(a0, a0, ss); ss = dot2(a1, a1, ss); ss = dot2(a2, a2, ss); ss = dot2(a3, a3, ss);
  }
  ss = swap32_add(ss);
  const float rstd = __builtin_amdgcn_rsqf(ss * (1.f / DM) + LN_EPS);
  const u32 rt = P * 8 + w * 2 + ((lane >> 4) & 1);
  for (int c = 0; c < 4; ++c) {
    f32x16 e = {}, o = {};
#pragma unroll
    for (int k = 0; k < 20; ++k) e = mfma32(lds128(L0 + (c * 40 + k) * 1024 + lane * 16), A[k], e);
#pragma unroll
    for (int k = 20; k < 40; ++k) o = mfma32(lds128(L0 + (c * 40 + k) * 1024 + lane * 16), A[k], o);
    const u32 chunk = C * 4 + c;
    if (chunk < 54) {
      float s[16];
#pragma unroll
      for (int i = 0; i < 16; ++i) {
        const float v = (e[i] + o[i]) * rstd;
        s[i] = v * __builtin_amdgcn_rcpf(1.f + __builtin_amdgcn_exp2f(v * -1.4426950408889634f));
      }
#pragma unroll
      for (int hf = 0; hf < 2; ++hf)
        st128(u32x4{pk(s[8 * hf], s[8 * hf + 1]), pk(s[8 * hf + 2], s[8 * hf + 3]), pk(s[8 * hf + 4], s[8 * hf + 5]), pk(s[8 * hf + 6], s[8 * hf + 7])},
              rw, rt * 54 * 1024 + (h * 16 + (lane & 15)) * 16, OFF_S + chunk * 1024 + hf * 512);
    } else {
      const f32x16 y1 = e + o, y2 = e - o;
#pragma unroll
      for (int hf = 0; hf < 2; ++hf) {
        const u32 vo = (rt * 80 + h) * 256 + (lane & 15) * 16, so = OFF_HB + (4 * (chunk - 54) + 2 * hf) * 256;
        st128(u32x4{pk(y1[8 * hf], y1[8 * hf + 1]), pk(y1[8 * hf + 2], y1[8 * hf + 3]), pk(y1[8 * hf + 4], y1[8 * hf + 5]), pk(y1[8 * hf + 6], y1[8 * hf + 7])}, rw, vo, so);
        st128(u32x4{pk(y2[8 * hf], y2[8 * hf + 1]), pk(y2[8 * hf + 2], y2[8 * hf + 3]), pk(y2[8 * hf + 4], y2[8 * hf + 5]), pk(y2[8 * hf + 6], y2[8 * hf + 7])}, rw, vo, so + 40 * 256);
      }
    }
  }
  STAMP(2, 1);
}

DI void ld128_asm(u32x4& d, rsrc_t r, u32 voff, u32 soff) { asm volatile("buffer_load_dwordx4 %0, %1, %2, %3 offen" : "=v"(d) : "v"(voff), "s"(r), "s"(soff) : "memory"); }
template <int N> DI void wait_vm4(u32x4 (&a)[4]) { asm volatile("s_waitcnt vmcnt(%4)" : "+v"(a[0]), "+v"(a[1]), "+v"(a[2]), "+v"(a[3]) : "i"(N) : "memory"); }
__global__ __launch_bounds__(256, 2) void k4(const void* w2, char* ws, void* out) {
  STAMP(3, 0);
  const u32 tid = threadIdx.x, lane = tid & 63, w = rfl(tid >> 6), r = lane & 15, g = lane >> 4, L0 = lds_base(), b = blockIdx.x;
  const u32 P = 2 * (b & 7) + ((b >> 3) & 1), C = b >> 4, hp = C >> 3, ct = C & 7, rt0 = P * 8 + hp * 4;
  const rsrc_t rw = mk_rsrc(ws, WS_USED), r2 = mk_rsrc(w2, FF * DM * 2), ro = mk_rsrc(out, SEQ * DM * 2);
  constexpr int DEPTH = 6;
  u32 dv[5];
#pragma unroll
  for (int i = 0; i < 5; ++i) { const u32 ci = i * 64 + lane, row = ci / 10; dv[i] = row * 1280 + (ci - row * 10) * 16; }
  const u32 ho = ((rt0 + w) * 80 + ct * 10) * 256;
  u32x4 hA = ld128(rw, (2 * (g & 1) + (g >> 1)) * 256 + r * 16, OFF_HB + ho), hB = ld128(rw, (4 + 2 * (g & 1) + (g >> 1)) * 256 + r * 16, OFF_HB + ho);
  const u32x2 hC = ld64(rw, (8 + (g >> 1)) * 256 + r * 16 + (g & 1) * 8, OFF_HB + ho);
  asm volatile("" : "+v"(hA), "+v"(hB));
  __builtin_amdgcn_sched_barrier(0);
  u32x4 A[DEPTH][4];
  const u32 Lw = L0 + w * DEPTH * 5120;
  auto issue = [&](int st) {
    const u32 ch = 4 * st + w;
#pragma unroll
    for (int i = 0; i < 5; ++i) dma16(r2, dv[i], ch * 32 * 1280 + ct * 160, Lw + (st % DEPTH) * 5120 + i * 1024);
    const u32 chs = ch < 54 ? ch : 53;
#pragma unroll
    for (int i = 0; i < 4; ++i) ld128_asm(A[st % DEPTH][i], rw, lane * 16, OFF_S + ((rt0 + i) * 54 + chs) * 1024);
  };
#pragma unroll
  for (int st = 0; st < DEPTH; ++st) issue(st);
  f32x4 acc[4][5] = {};
  const u32 ta = (16 * (g >> 1) + 4 * (g & 1) + (r >> 2)) * 160 + (r & 3) * 8;
#pragma unroll
  for (int st = 0; st < 14; ++st) {
    constexpr int NI = 14;
    if (st + DEPTH <= NI) wait_vm4<9 * (DEPTH - 1)>(A[st % DEPTH]);
    else if (st == 9) wait_vm4<36>(A[st % DEPTH]);
    else if (st == 10) wait_vm4<27>(A[st % DEPTH]);
    else if (st == 11) wait_vm4<18>(A[st % DEPTH]);
    else if (st == 12) wait_vm4<9>(A[st % DEPTH]);
    else wait_vm4<0>(A[st % DEPTH]);
    u32x4 B[5];
#pragma unroll
    for (int t = 0; t < 5; ++t) {
      const u32x2 lo = ldstr(Lw + (st % DEPTH) * 5120 + ta + t * 32), hi = ldstr(Lw + (st % DEPTH) * 5120 + ta + t * 32 + 8 * 160);
      B[t] = u32x4{lo[0], lo[1], hi[0], hi[1]};
    }
#pragma unroll
    for (int t = 0; t < 5; ++t)
#pragma unroll
      for (int i = 0; i < 4; ++i) acc[i][t] = mfma16(B[t], A[st % DEPTH][i], acc[i][t]);
    __builtin_amdgcn_sched_barrier(0);
    if (st + DEPTH < NI) issue(st + DEPTH);
    __builtin_amdgcn_sched_barrier(0);
  }
  BAR();
#pragma unroll
  for (int i = 0; i < 4; ++i)
#pragma unroll
    for (int t = 0; t < 5; ++t) lds128w(L0 + ((i * 4 + w) * 5 + t) * 1024 + lane * 16, __builtin_bit_cast(u32x4, acc[i][t]));
  BAR();
  u32 h0 = hA[0], h1 = hA[1], h2 = hA[2], h3 = hA[3], h4 = hB[0], h5 = hB[1], h6 = hB[2], h7 = hB[3];
  swap16(h0, h2); swap16(h1, h3); swap16(h4, h6); swap16(h5, h7);
  const u32 hq[5][2] = {{h0, h1}, {h2, h3}, {h4, h5}, {h6, h7}, {hC[0], hC[1]}};
  u32 o[5][2];
#pragma unroll
  for (int t = 0; t < 5; ++t) {
    f32x4 s = {bf_lo(hq[t][0]), bf_hi(hq[t][0]), bf_lo(hq[t][1]), bf_hi(hq[t][1])};
#pragma unroll
    for (int ww = 0; ww < 4; ++ww) s += __builtin_bit_cast(f32x4, lds128(L0 + ((w * 4 + ww) * 5 + t) * 1024 + lane * 16));
    o[t][0] = pk(s[0], s[1]); o[t][1] = pk(s[2], s[3]);
  }
  swap16(o[0][0], o[1][0]); swap16(o[0][1], o[1][1]); swap16(o[2][0], o[3][0]); swap16(o[2][1], o[3][1]);
  const u32 rt = rt0 + w, om = (rt >> 1) & 3, par = rt & 1;
  const u32 s0 = 32 * (16 * (P >> 2) + 4 * om + 2 * (P & 1) + par) + 16 * ((P >> 1) & 1);
  const u32 so = s0 * 1280 + ct * 160, vo = r * 1280 + (2 * (g & 1) + (g >> 1)) * 16;
  __builtin_amdgcn_raw_buffer_store_b128(u32x4{o[0][0], o[0][1], o[1][0], o[1][1]}, ro, vo, so, 0);
  __builtin_amdgcn_raw_buffer_store_b128(u32x4{o[2][0], o[2][1], o[3][0], o[3][1]}, ro, vo + 64, so, 0);
  __builtin_amdgcn_raw_buffer_store_b64(u32x2{o[4][0], o[4][1]}, ro, r * 1280 + 128 + g * 8, so, 0);
  STAMP(3, 1);
}

extern "C" void kernel_launch(void* const* d_in, const int* in_sizes, int n_in, void* d_out, int out_size, void* d_ws, size_t ws_size,
                              hipStream_t stream) {
  char* ws = (char*)d_ws;
  (void)hipFuncSetAttribute((const void*)k3, hipFuncAttributeMaxDynamicSharedMemorySize, 163840);
  (void)hipFuncSetAttribute((const void*)k4, hipFuncAttributeMaxDynamicSharedMemorySize, 122880);
  k1<<<256, 256, 63488, stream>>>(d_in[0], d_in[1], d_in[2], d_in[4], ws);
  k2<<<160, 256, 32768, stream>>>(ws);
  k3<<<256, 256, 163840, stream>>>(ws);
  k4<<<256, 256, 122880, stream>>>(d_in[5], ws, d_out);
}
```

```cpp
#include <hip/hip_runtime.h>
#define WS_OFF 0
#define OFF_TS (20987904 + WS_OFF)

__global__ __launch_bounds__(256, 1) __attribute__((aligned(4096))) void fused(const void* x, const void* wr, const void* wi, const void* w1, const void* w2, void* ws, void* out) {
  const void __attribute__((address_space(4)))* karg = (const void __attribute__((address_space(4)))*)__builtin_amdgcn_kernarg_segment_ptr();
  unsigned bid = blockIdx.x, tid = threadIdx.x;
  asm volatile(
    "s_load_dwordx2 s[12:13], s[0:1], 0x0\n"
    "s_load_dwordx4 s[36:39], s[0:1], 0x8\n"
    "s_load_dwordx2 s[16:17], s[0:1], 0x18\n"
    "s_load_dwordx2 s[4:5], s[0:1], 0x28\n"
    "s_load_dwordx2 s[8:9], s[0:1], 0x20\n"
    "s_load_dwordx2 s[20:21], s[0:1], 0x30\n"
    "v_lshrrev_b32 v1, 6, v0\n"
    "v_and_b32 v255, 63, v0\n"
    "v_readfirstlane_b32 s3, v1\n"
    "s_getpc_b64 s[24:25]\n"
    ".Lbegin:\n"
    "s_mov_b32 s26, .Lend-.Lbegin\n"
    "s_mov_b32 s27, 0x20000\n"
    "s_lshr_b32 s29, s2, 3\n"
    "s_lshl_b32 s34, s29, 11\n"
    "v_and_b32 v1, 0xc3, v0\n"
    "v_lshrrev_b32 v2, 4, v1\n"
    "v_or_b32 v1, v1, v2\n"
    "v_and_b32 v1, 15, v1\n"
    "v_lshlrev_b32 v1, 7, v1\n"
    "buffer_load_dword a255, v1, s[24:27], s34 offen\n"
    "s_and_b32 s28, s2, 7\n"
    "v_lshlrev_b32 v254, 4, v255\n"
    "v_and_b32 v251, 15, v255\n"
    "v_lshrrev_b32 v250, 4, v255\n"
    "s_mov_b32 s15, s27\n"
    "s_mov_b32 s19, s27\n"
    "s_mov_b32 s18, 2211840\n"
    "s_cmp_ge_u32 s29, 16\n"
    "s_cbranch_scc1 .Lpre_b\n"
    "s_lshl_b32 s40, s28, 4\n"
    "s_add_u32 s40, s40, s29\n"
    "s_lshr_b32 s41, s40, 5\n"
    "s_and_b32 s42, s40, 31\n"
    "s_lshl_b32 s34, s41, 9\n"
    "s_add_u32 s34, s34, s42\n"
    "s_mul_i32 s43, s34, 1280\n"
    "s_mov_b32 s14, 2621440\n"
    "s_mul_i32 s44, s3, 5120\n"
    "v_lshrrev_b32 v1, 4, v0\n"
    "v_xor_b32 v2, v1, v251\n"
    "v_mul_u32_u24 v1, 0xa000, v1\n"
    "v_lshl_add_u32 v1, v2, 4, v1\n"
    "s_add_u32 m0, s44, 114688\n"
    "s_waitcnt lgkmcnt(0)\n"
    "buffer_load_dwordx4 v1, s[12:15], s43 offen offset:0  lds\n"
    "s_add_u32 m0, s44, 115456\n"
    "s_nop 0\n"
    "buffer_load_dwordx4 v1, s[12:15], s43 offen offset:256  lds\n"
    "s_add_u32 m0, s44, 116224\n"
    "s_nop 0\n"
    "buffer_load_dwordx4 v1, s[12:15], s43 offen offset:512  lds\n"
    "s_add_u32 m0, s44, 116992\n"
    "s_nop 0\n"
    "buffer_load_dwordx4 v1, s[12:15], s43 offen offset:768  lds\n"
    "s_add_u32 m0, s44, 117760\n"
    "s_nop 0\n"
    "buffer_load_dwordx4 v1, s[12:15], s43 offen offset:1024  lds\n"
    "s_branch .Lrole_a\n"
    ".Lpre_b:\n"
    "s_cmp_ge_u32 s29, 24\n"
    "s_cbranch_scc1 .Lpre_c\n"
    "s_sub_u32 s40, s29, 16\n"
    "s_lshl_b32 s40, s40, 1\n"
    "s_lshr_b32 s34, s28, 2\n"
    "s_add_u32 s40, s40, s34\n"
    "s_and_b32 s43, s28, 3\n"
    "s_lshl_b32 s41, s40, 1\n"
    "s_and_b32 s44, s3, 1\n"
    "s_add_u32 s41, s41, s44\n"
    "s_lshl_b32 s41, s41, 2\n"
    "s_add_u32 s41, s41, s43\n"
    "s_mul_i32 s41, s41, 10272\n"
    "s_mul_i32 s42, s3, 11264\n"
    "s_mov_b32 s14, 1314816\n"
    "s_cmp_ge_u32 s3, 2\n"
    "s_waitcnt lgkmcnt(0)\n"
    "s_cselect_b32 s12, s38, s36\n"
    "s_cselect_b32 s13, s39, s37\n"
    "s_add_u32 m0, s42, 114688\n"
    "s_add_u32 s45, s41, 0\n"
    "buffer_load_dwordx4 v254, s[12:15], s45 offen offset:0  lds\n"
    "buffer_load_dwordx4 v254, s[12:15], s45 offen offset:1024  lds\n"
    "buffer_load_dwordx4 v254, s[12:15], s45 offen offset:2048  lds\n"
    "buffer_load_dwordx4 v254, s[12:15], s45 offen offset:3072  lds\n"
    "s_add_u32 m0, s42, 118784\n"
    "s_add_u32 s45, s41, 4096\n"
    "buffer_load_dwordx4 v254, s[12:15], s45 offen offset:0  lds\n"
    "buffer_load_dwordx4 v254, s[12:15], s45 offen offset:1024  lds\n"
    "buffer_load_dwordx4 v254, s[12:15], s45 offen offset:2048  lds\n"
    "buffer_load_dwordx4 v254, s[12:15], s45 offen offset:3072  lds\n"
    "s_branch .Lrole_b\n"
    ".Lpre_c:\n"
    "s_sub_u32 s34, s29, 24\n"
    "s_lshr_b32 s34, s34, 1\n"
    "s_lshl_b32 s34, s34, 3\n"
    "s_add_u32 s34, s34, s28\n"
    "s_lshl_b32 s34, s34, 1\n"
    "s_and_b32 s35, s29, 1\n"
    "s_add_u32 s43, s34, s35\n"
    "s_cmp_ge_u32 s43, 54\n"
    "s_cbranch_scc1 .Lrole_d\n"
    "s_mul_i32 s44, s3, 55296\n"
    "s_lshl_b32 s34, s43, 6\n"
    "s_add_u32 s44, s44, s34\n"
    "s_lshl_b32 s45, s3, 10\n"
    "v_lshrrev_b32 v10, 2, v255\n"
    "v_xor_b32 v10, s3, v10\n"
    "v_mul_u32_u24 v10, 0xd80, v10\n"
    "v_and_b32 v11, 48, v254\n"
    "v_add_u32 v11, v10, v11\n"
    "s_add_u32 m0, s45, 81920\n"
    "s_add_u32 s56, s44, 0\n"
    "s_waitcnt lgkmcnt(0)\n"
    "buffer_load_dwordx4 v11, s[16:19], s56 offen  lds\n"
    "s_add_u32 m0, s45, 86016\n"
    "s_add_u32 s56, s44, 221184\n"
    "buffer_load_dwordx4 v11, s[16:19], s56 offen  lds\n"
    "s_add_u32 m0, s45, 90112\n"
    "s_add_u32 s56, s44, 442368\n"
    "buffer_load_dwordx4 v11, s[16:19], s56 offen  lds\n"
    "s_add_u32 m0, s45, 94208\n"
    "s_add_u32 s56, s44, 663552\n"
    "buffer_load_dwordx4 v11, s[16:19], s56 offen  lds\n"
    "s_branch .Lrole_c\n"
    ".Lwarm0:\n"
    "s_mov_b64 exec, 0\n"
    "s_branch .Lpad0\n"
    ".Lwarm:\n"
    "s_mov_b64 exec, 0\n"
    "s_cmp_eq_u32 s3, 0\n"
    "s_cbranch_scc1 .Lpad1\n"
    "s_cmp_eq_u32 s3, 1\n"
    "s_cbranch_scc1 .Lpad2\n"
    "s_cmp_eq_u32 s3, 2\n"
    "s_cbranch_scc1 .Lpad3\n"
    "s_branch .Lpad5\n"
    ".Lw_ret:\n"
    "s_mov_b64 exec, -1\n"
    "s_setpc_b64 s[0:1]\n"
    ".Lrole_a:\n"
    "s_mov_b32 s6, 21544960\n"
    "s_mov_b32 s7, 0x20000\n"
    "s_mov_b32 s34, 20201472\n"
    "v_mov_b32 v5, 0\n"
    "buffer_load_dword v253, v5, s[4:7], s34 offen sc1\n"
    "s_lshl_b32 s34, s2, 2\n"
    "s_add_u32 s34, s34, s3\n"
    "s_cmp_lt_u32 s34, 1024\n"
    "s_cselect_b32 s35, 2, 0\n"
    "s_lshl_b32 s34, s34, 6\n"
    "s_add_u32 s34, s34, 20267040\n"
    "v_cmp_gt_u32 vcc, s35, v255\n"
    "v_mul_u32_u24 v6, 65536, v255\n"
    "v_mov_b32 v7, 0x7fffffff\n"
    "v_cndmask_b32 v6, v7, v6, vcc\n"
    "buffer_store_dword v5, v6, s[4:7], s34 offen sc1\n"
    "s_lshl_b32 s34, s29, 2\n"
    "s_add_u32 s34, s34, s3\n"
    "s_mul_i32 s34, s34, 34560\n"
    "s_lshl_b32 s35, s28, 7\n"
    "s_add_u32 s34, s34, s35\n"
    "s_cmp_lt_u32 s28, 3\n"
    "s_cselect_b32 s35, 4, 3\n"
    "v_mul_u32_u24 v0, 0xd80, v251\n"
    "v_lshl_add_u32 v0, v250, 10, v0\n"
    "v_cmp_gt_u32 vcc, 10, v251\n"
    "v_cmp_gt_u32 s[48:49], s35, v250\n"
    "s_nop 0\n"
    "s_and_b64 vcc, vcc, s[48:49]\n"
    "v_mov_b32 v1, 0x7fffffff\n"
    "v_cndmask_b32 v230, v1, v0, vcc\n"
    "s_mov_b32 s45, s34\n"
    "s_and_b32 s34, s29, 14\n"
    "s_lshr_b32 s35, s28, 2\n"
    "s_add_u32 s34, s34, s35\n"
    "s_lshl_b32 s34, s34, 1\n"
    "s_and_b32 s35, s3, 1\n"
    "s_add_u32 s34, s34, s35\n"
    "s_lshl_b32 s34, s34, 2\n"
    "s_and_b32 s35, s28, 3\n"
    "s_add_u32 s34, s34, s35\n"
    "s_mul_i32 s34, s34, 10272\n"
    "s_and_b32 s35, s29, 1\n"
    "s_mul_i32 s35, s35, 1280\n"
    "s_add_u32 s34, s34, s35\n"
    "s_add_u32 s34, s34, 7836\n"
    "s_cmp_ge_u32 s3, 2\n"
    "s_cselect_b32 s36, s38, s36\n"
    "s_cselect_b32 s37, s39, s37\n"
    "s_mov_b32 s38, 1314816\n"
    "s_mov_b32 s39, 0x20000\n"
    "v_min_u32 v0, 9, v255\n"
    "v_lshlrev_b32 v0, 7, v0\n"
    "buffer_load_dword a251, v0, s[36:39], s34 offen\n"
    "buffer_load_dword a254, v230, s[16:19], s45 offen\n"
    "s_nop 0\n"
    "v_bfe_u32 v0, v255, 3, 2\n"
    "v_and_b32 v1, 3, v255\n"
    "v_lshl_add_u32 v0, v0, 2, v1\n"
    "v_bfe_u32 v1, v255, 2, 1\n"
    "v_cvt_f32_u32 v2, v1\n"
    "v_mul_f32 v2, 0x3e800000, v2\n"
    "v_lshrrev_b32 v3, 5, v255\n"
    "v_cvt_f32_u32 v3, v3\n"
    "v_mul_f32 v3, 4.0, v3\n"
    "v_cvt_f32_u32 v4, v0\n"
    "v_fmaak_f32 v5, 2.0, v4, 0x3f800000\n"
    "v_mul_f32 v5, 0x3ccccccd, v5\n"
    "v_fma_f32 v6, v5, v3, v2\n"
    "v_mul_f32 v128, 0x41000000, v5\n"
    "v_fmamk_f32 v109, v5, 0x3f800000, v6\n"
    "v_fmamk_f32 v110, v5, 0x40000000, v6\n"
    "v_fmamk_f32 v111, v5, 0x40400000, v6\n"
    "v_fmamk_f32 v112, v5, 0x41000000, v6\n"
    "v_fmamk_f32 v113, v5, 0x41100000, v6\n"
    "v_fmamk_f32 v114, v5, 0x41200000, v6\n"
    "v_fmamk_f32 v115, v5, 0x41300000, v6\n"
    "v_cos_f32 v128, v128\n"
    "v_cos_f32 v108, v6\n"
    "v_cos_f32 v109, v109\n"
    "v_cos_f32 v110, v110\n"
    "v_cos_f32 v111, v111\n"
    "v_cos_f32 v112, v112\n"
    "v_cos_f32 v113, v113\n"
    "v_cos_f32 v114, v114\n"
    "v_cos_f32 v115, v115\n"
    "v_add_f32 v128, v128, v128\n"
    "v_pk_fma_f32 v[116:117], v[112:113], v[128:129], v[108:109] op_sel_hi:[1,0,1] neg_lo:[0,0,1] neg_hi:[0,0,1]\n"
    "v_pk_fma_f32 v[118:119], v[114:115], v[128:129], v[110:111] op_sel_hi:[1,0,1] neg_lo:[0,0,1] neg_hi:[0,0,1]\n"
    "v_pk_fma_f32 v[120:121], v[116:117], v[128:129], v[112:113] op_sel_hi:[1,0,1] neg_lo:[0,0,1] neg_hi:[0,0,1]\n"
    "v_pk_fma_f32 v[122:123], v[118:119], v[128:129], v[114:115] op_sel_hi:[1,0,1] neg_lo:[0,0,1] neg_hi:[0,0,1]\n"
    "v_pk_fma_f32 v[124:125], v[120:121], v[128:129], v[116:117] op_sel_hi:[1,0,1] neg_lo:[0,0,1] neg_hi:[0,0,1]\n"
    "v_pk_fma_f32 v[126:127], v[122:123], v[128:129], v[118:119] op_sel_hi:[1,0,1] neg_lo:[0,0,1] neg_hi:[0,0,1]\n"
    "v_cvt_pk_bf16_f32 v18, v108, v109\n"
    "v_cvt_pk_bf16_f32 v19, v110, v111\n"
    "v_cvt_pk_bf16_f32 v20, v112, v113\n"
    "v_cvt_pk_bf16_f32 v21, v114, v115\n"
    "v_cvt_pk_bf16_f32 v22, v116, v117\n"
    "v_cvt_pk_bf16_f32 v23, v118, v119\n"
    "v_cvt_pk_bf16_f32 v24, v120, v121\n"
    "v_cvt_pk_bf16_f32 v25, v122, v123\n"
    "v_cvt_pk_bf16_f32 v26, v124, v125\n"
    "v_cvt_pk_bf16_f32 v27, v126, v127\n"
    "v_cmp_eq_u32 vcc, 0, v0\n"
    "v_mul_f32 v5, 0x3d4ccccd, v4\n"
    "v_add_f32 v6, v2, v2\n"
    "v_cndmask_b32 v5, v5, v6, vcc\n"
    "v_cndmask_b32_e64 v2, v2, 0, vcc\n"
    "v_cmp_gt_u32 vcc, 10, v0\n"
    "s_nop 1\n"
    "v_cndmask_b32 v5, 0, v5, vcc\n"
    "v_cndmask_b32 v2, 0, v2, vcc\n"
    "v_fma_f32 v6, v5, v3, v2\n"
    "v_mul_f32 v128, 0x41000000, v5\n"
    "v_fmamk_f32 v109, v5, 0x3f800000, v6\n"
    "v_fmamk_f32 v110, v5, 0x40000000, v6\n"
    "v_fmamk_f32 v111, v5, 0x40400000, v6\n"
    "v_fmamk_f32 v112, v5, 0x41000000, v6\n"
    "v_fmamk_f32 v113, v5, 0x41100000, v6\n"
    "v_fmamk_f32 v114, v5, 0x41200000, v6\n"
    "v_fmamk_f32 v115, v5, 0x41300000, v6\n"
    "v_cos_f32 v128, v128\n"
    "v_cos_f32 v108, v6\n"
    "v_cos_f32 v109, v109\n"
    "v_cos_f32 v110, v110\n"
    "v_cos_f32 v111, v111\n"
    "v_cos_f32 v112, v112\n"
    "v_cos_f32 v113, v113\n"
    "v_cos_f32 v114, v114\n"
    "v_cos_f32 v115, v115\n"
    "v_add_f32 v128, v128, v128\n"
    "v_pk_fma_f32 v[116:117], v[112:113], v[128:129], v[108:109] op_sel_hi:[1,0,1] neg_lo:[0,0,1] neg_hi:[0,0,1]\n"
    "v_pk_fma_f32 v[118:119], v[114:115], v[128:129], v[110:111] op_sel_hi:[1,0,1] neg_lo:[0,0,1] neg_hi:[0,0,1]\n"
    "v_pk_fma_f32 v[120:121], v[116:117], v[128:129], v[112:113] op_sel_hi:[1,0,1] neg_lo:[0,0,1] neg_hi:[0,0,1]\n"
    "v_pk_fma_f32 v[122:123], v[118:119], v[128:129], v[114:115] op_sel_hi:[1,0,1] neg_lo:[0,0,1] neg_hi:[0,0,1]\n"
    "v_pk_fma_f32 v[124:125], v[120:121], v[128:129], v[116:117] op_sel_hi:[1,0,1] neg_lo:[0,0,1] neg_hi:[0,0,1]\n"
    "v_pk_fma_f32 v[126:127], v[122:123], v[128:129], v[118:119] op_sel_hi:[1,0,1] neg_lo:[0,0,1] neg_hi:[0,0,1]\n"
    "v_cvt_pk_bf16_f32 v8, v108, v109\n"
    "v_cvt_pk_bf16_f32 v9, v110, v111\n"
    "v_cvt_pk_bf16_f32 v10, v112, v113\n"
    "v_cvt_pk_bf16_f32 v11, v114, v115\n"
    "v_cvt_pk_bf16_f32 v12, v116, v117\n"
    "v_cvt_pk_bf16_f32 v13, v118, v119\n"
    "v_cvt_pk_bf16_f32 v14, v120, v121\n"
    "v_cvt_pk_bf16_f32 v15, v122, v123\n"
    "v_cvt_pk_bf16_f32 v16, v124, v125\n"
    "v_cvt_pk_bf16_f32 v17, v126, v127\n"
    "v_cmp_gt_u32 vcc, 8, v251\n"
    "v_mov_b32 v0, 0x3acccccd\n"
    "s_mul_i32 s34, s3, 10\n"
    "v_cndmask_b32_e64 v0, -v0, v0, vcc\n"
    "s_cmp_ge_u32 s3, 2\n"
    "s_cselect_b32 s35, 19, 0\n"
    "s_sub_u32 s34, s34, s35\n"
    "v_mul_u32_u24 v1, 40, v251\n"
    "v_add_u32 v2, s34, v1\n"
    "v_cvt_f32_u32 v2, v2\n"
    "v_mul_f32 v2, v0, v2\n"
    "v_mul_f32 v4, 0xc2200000, v3\n"
    "v_mul_f32 v4, v0, v4\n"
    "v_and_b32 v5, 1, v250\n"
    "v_cvt_f32_u32 v5, v5\n"
    "v_mul_f32 v5, 0x41000000, v5\n"
    "v_add_f32 v0, v0, v0\n"
    "v_mov_b32 v236, v2\n"
    "v_mov_b32 v237, v4\n"
    "s_cmp_lg_u32 s3, 0\n"
    "s_cbranch_scc1 .La_t0\n"
    "v_subrev_u32 v6, 300, v1\n"
    "v_cndmask_b32 v6, v6, v1, vcc\n"
    "v_cvt_f32_u32 v6, v6\n"
    "v_mul_f32 v236, 0x3acccccd, v6\n"
    "v_mov_b32 v237, 0\n"
    ".La_t0:\n"
    "v_fma_f32 v6, v236, v5, v237\n"
    "v_cos_f32 v124, v236\n"
    "v_add_f32 v110, v6, v236\n"
    "v_sin_f32_e64 v111, -v110\n"
    "v_cos_f32 v110, v110\n"
    "v_cos_f32 v108, v6\n"
    "v_sin_f32_e64 v109, -v6\n"
    "v_add_f32 v124, v124, v124\n"
    "v_pk_fma_f32 v[112:113], v[110:111], v[124:125], v[108:109] op_sel_hi:[1,0,1] neg_lo:[0,0,1] neg_hi:[0,0,1]\n"
    "v_cvt_pk_bf16_f32 v28, v108, v110\n"
    "v_pk_fma_f32 v[114:115], v[112:113], v[124:125], v[110:111] op_sel_hi:[1,0,1] neg_lo:[0,0,1] neg_hi:[0,0,1]\n"
    "v_cvt_pk_bf16_f32 v32, v109, v111\n"
    "v_pk_fma_f32 v[116:117], v[114:115], v[124:125], v[112:113] op_sel_hi:[1,0,1] neg_lo:[0,0,1] neg_hi:[0,0,1]\n"
    "v_cvt_pk_bf16_f32 v29, v112, v114\n"
    "v_pk_fma_f32 v[118:119], v[116:117], v[124:125], v[114:115] op_sel_hi:[1,0,1] neg_lo:[0,0,1] neg_hi:[0,0,1]\n"
    "v_cvt_pk_bf16_f32 v33, v113, v115\n"
    "v_pk_fma_f32 v[120:121], v[118:119], v[124:125], v[116:117] op_sel_hi:[1,0,1] neg_lo:[0,0,1] neg_hi:[0,0,1]\n"
    "v_cvt_pk_bf16_f32 v30, v116, v118\n"
    "v_pk_fma_f32 v[122:123], v[120:121], v[124:125], v[118:119] op_sel_hi:[1,0,1] neg_lo:[0,0,1] neg_hi:[0,0,1]\n"
    "v_cvt_pk_bf16_f32 v34, v117, v119\n"
    "v_cvt_pk_bf16_f32 v31, v120, v122\n"
    "v_cvt_pk_bf16_f32 v35, v121, v123\n"
    "s_cbranch_scc1 .La_t1\n"
    "v_bfe_u32 v6, v255, 3, 1\n"
    "v_lshrrev_b32 v7, 5, v255\n"
    "v_cmp_eq_u32 vcc, v6, v7\n"
    "s_nop 1\n"
    "v_cndmask_b32 v28, 0, v28, vcc\n"
    "v_cndmask_b32 v29, 0, v29, vcc\n"
    "v_cndmask_b32 v30, 0, v30, vcc\n"
    "v_cndmask_b32 v31, 0, v31, vcc\n"
    "v_cndmask_b32 v32, 0, v32, vcc\n"
    "v_cndmask_b32 v33, 0, v33, vcc\n"
    "v_cndmask_b32 v34, 0, v34, vcc\n"
    "v_cndmask_b32 v35, 0, v35, vcc\n"
    "v_and_b32 v6, 47, v255\n"
    "v_cmp_eq_u32 vcc, 0, v6\n"
    "v_mov_b32 v6, 0xbf353f35\n"
    "s_nop 0\n"
    "v_cndmask_b32 v28, v28, v6, vcc\n"
    "v_cndmask_b32 v29, v29, v6, vcc\n"
    "v_cndmask_b32 v30, v30, v6, vcc\n"
    "v_cndmask_b32 v31, v31, v6, vcc\n"
    ".La_t1:\n"
    "v_add_f32 v2, v2, v0\n"
    "v_fma_f32 v6, v2, v5, v4\n"
    "v_cos_f32 v124, v2\n"
    "v_add_f32 v110, v6, v2\n"
    "v_sin_f32_e64 v111, -v110\n"
    "v_cos_f32 v110, v110\n"
    "v_cos_f32 v108, v6\n"
    "v_sin_f32_e64 v109, -v6\n"
    "v_add_f32 v124, v124, v124\n"
    "v_pk_fma_f32 v[112:113], v[110:111], v[124:125], v[108:109] op_sel_hi:[1,0,1] neg_lo:[0,0,1] neg_hi:[0,0,1]\n"
    "v_cvt_pk_bf16_f32 v36, v108, v110\n"
    "v_pk_fma_f32 v[114:115], v[112:113], v[124:125], v[110:111] op_sel_hi:[1,0,1] neg_lo:[0,0,1] neg_hi:[0,0,1]\n"
    "v_cvt_pk_bf16_f32 v40, v109, v111\n"
    "v_pk_fma_f32 v[116:117], v[114:115], v[124:125], v[112:113] op_sel_hi:[1,0,1] neg_lo:[0,0,1] neg_hi:[0,0,1]\n"
    "v_cvt_pk_bf16_f32 v37, v112, v114\n"
    "v_pk_fma_f32 v[118:119], v[116:117], v[124:125], v[114:115] op_sel_hi:[1,0,1] neg_lo:[0,0,1] neg_hi:[0,0,1]\n"
    "v_cvt_pk_bf16_f32 v41, v113, v115\n"
    "v_pk_fma_f32 v[120:121], v[118:119], v[124:125], v[116:117] op_sel_hi:[1,0,1] neg_lo:[0,0,1] neg_hi:[0,0,1]\n"
    "v_cvt_pk_bf16_f32 v38, v116, v118\n"
    "v_pk_fma_f32 v[122:123], v[120:121], v[124:125], v[118:119] op_sel_hi:[1,0,1] neg_lo:[0,0,1] neg_hi:[0,0,1]\n"
    "v_cvt_pk_bf16_f32 v42, v117, v119\n"
    "v_cvt_pk_bf16_f32 v39, v120, v122\n"
    "v_cvt_pk_bf16_f32 v43, v121, v123\n"
    "v_add_f32 v2, v2, v0\n"
    "v_fma_f32 v6, v2, v5, v4\n"
    "v_cos_f32 v124, v2\n"
    "v_add_f32 v110, v6, v2\n"
    "v_sin_f32_e64 v111, -v110\n"
    "v_cos_f32 v110, v110\n"
    "v_cos_f32 v108, v6\n"
    "v_sin_f32_e64 v109, -v6\n"
    "v_add_f32 v124, v124, v124\n"
    "v_pk_fma_f32 v[112:113], v[110:111], v[124:125], v[108:109] op_sel_hi:[1,0,1] neg_lo:[0,0,1] neg_hi:[0,0,1]\n"
    "v_cvt_pk_bf16_f32 v44, v108, v110\n"
    "v_pk_fma_f32 v[114:115], v[112:113], v[124:125], v[110:111] op_sel_hi:[1,0,1] neg_lo:[0,0,1] neg_hi:[0,0,1]\n"
    "v_cvt_pk_bf16_f32 v48, v109, v111\n"
    "v_pk_fma_f32 v[116:117], v[114:115], v[124:125], v[112:113] op_sel_hi:[1,0,1] neg_lo:[0,0,1] neg_hi:[0,0,1]\n"
    "v_cvt_pk_bf16_f32 v45, v112, v114\n"
    "v_pk_fma_f32 v[118:119], v[116:117], v[124:125], v[114:115] op_sel_hi:[1,0,1] neg_lo:[0,0,1] neg_hi:[0,0,1]\n"
    "v_cvt_pk_bf16_f32 v49, v113, v115\n"
    "v_pk_fma_f32 v[120:121], v[118:119], v[124:125], v[116:117] op_sel_hi:[1,0,1] neg_lo:[0,0,1] neg_hi:[0,0,1]\n"
    "v_cvt_pk_bf16_f32 v46, v116, v118\n"
    "v_pk_fma_f32 v[122:123], v[120:121], v[124:125], v[118:119] op_sel_hi:[1,0,1] neg_lo:[0,0,1] neg_hi:[0,0,1]\n"
    "v_cvt_pk_bf16_f32 v50, v117, v119\n"
    "v_cvt_pk_bf16_f32 v47, v120, v122\n"
    "v_cvt_pk_bf16_f32 v51, v121, v123\n"
    "v_add_f32 v2, v2, v0\n"
    "v_fma_f32 v6, v2, v5, v4\n"
    "v_cos_f32 v124, v2\n"
    "v_add_f32 v110, v6, v2\n"
    "v_sin_f32_e64 v111, -v110\n"
    "v_cos_f32 v110, v110\n"
    "v_cos_f32 v108, v6\n"
    "v_sin_f32_e64 v109, -v6\n"
    "v_add_f32 v124, v124, v124\n"
    "v_pk_fma_f32 v[112:113], v[110:111], v[124:125], v[108:109] op_sel_hi:[1,0,1] neg_lo:[0,0,1] neg_hi:[0,0,1]\n"
    "v_cvt_pk_bf16_f32 v52, v108, v110\n"
    "v_pk_fma_f32 v[114:115], v[112:113], v[124:125], v[110:111] op_sel_hi:[1,0,1] neg_lo:[0,0,1] neg_hi:[0,0,1]\n"
    "v_cvt_pk_bf16_f32 v56, v109, v111\n"
    "v_pk_fma_f32 v[116:117], v[114:115], v[124:125], v[112:113] op_sel_hi:[1,0,1] neg_lo:[0,0,1] neg_hi:[0,0,1]\n"
    "v_cvt_pk_bf16_f32 v53, v112, v114\n"
    "v_pk_fma_f32 v[118:119], v[116:117], v[124:125], v[114:115] op_sel_hi:[1,0,1] neg_lo:[0,0,1] neg_hi:[0,0,1]\n"
    "v_cvt_pk_bf16_f32 v57, v113, v115\n"
    "v_pk_fma_f32 v[120:121], v[118:119], v[124:125], v[116:117] op_sel_hi:[1,0,1] neg_lo:[0,0,1] neg_hi:[0,0,1]\n"
    "v_cvt_pk_bf16_f32 v54, v116, v118\n"
    "v_pk_fma_f32 v[122:123], v[120:121], v[124:125], v[118:119] op_sel_hi:[1,0,1] neg_lo:[0,0,1] neg_hi:[0,0,1]\n"
    "v_cvt_pk_bf16_f32 v58, v117, v119\n"
    "v_cvt_pk_bf16_f32 v55, v120, v122\n"
    "v_cvt_pk_bf16_f32 v59, v121, v123\n"
    "v_add_f32 v2, v2, v0\n"
    "v_fma_f32 v6, v2, v5, v4\n"
    "v_cos_f32 v124, v2\n"
    "v_add_f32 v110, v6, v2\n"
    "v_sin_f32_e64 v111, -v110\n"
    "v_cos_f32 v110, v110\n"
    "v_cos_f32 v108, v6\n"
    "v_sin_f32_e64 v109, -v6\n"
    "v_add_f32 v124, v124, v124\n"
    "v_pk_fma_f32 v[112:113], v[110:111], v[124:125], v[108:109] op_sel_hi:[1,0,1] neg_lo:[0,0,1] neg_hi:[0,0,1]\n"
    "v_cvt_pk_bf16_f32 v60, v108, v110\n"
    "v_pk_fma_f32 v[114:115], v[112:113], v[124:125], v[110:111] op_sel_hi:[1,0,1] neg_lo:[0,0,1] neg_hi:[0,0,1]\n"
    "v_cvt_pk_bf16_f32 v64, v109, v111\n"
    "v_pk_fma_f32 v[116:117], v[114:115], v[124:125], v[112:113] op_sel_hi:[1,0,1] neg_lo:[0,0,1] neg_hi:[0,0,1]\n"
    "v_cvt_pk_bf16_f32 v61, v112, v114\n"
    "v_pk_fma_f32 v[118:119], v[116:117], v[124:125], v[114:115] op_sel_hi:[1,0,1] neg_lo:[0,0,1] neg_hi:[0,0,1]\n"
    "v_cvt_pk_bf16_f32 v65, v113, v115\n"
    "v_pk_fma_f32 v[120:121], v[118:119], v[124:125], v[116:117] op_sel_hi:[1,0,1] neg_lo:[0,0,1] neg_hi:[0,0,1]\n"
    "v_cvt_pk_bf16_f32 v62, v116, v118\n"
    "v_pk_fma_f32 v[122:123], v[120:121], v[124:125], v[118:119] op_sel_hi:[1,0,1] neg_lo:[0,0,1] neg_hi:[0,0,1]\n"
    "v_cvt_pk_bf16_f32 v66, v117, v119\n"
    "v_cvt_pk_bf16_f32 v63, v120, v122\n"
    "v_cvt_pk_bf16_f32 v67, v121, v123\n"
    "v_bfe_u32 v232, v255, 3, 1\n"
    "v_xor_b32 v232, s3, v232\n"
    "v_lshrrev_b32 v233, 1, v232\n"
    "v_and_b32 v232, 1, v232\n"
    "v_and_b32 v5, 1, v250\n"
    "v_lshlrev_b32 v5, 2, v5\n"
    "v_lshl_add_u32 v234, v232, 3, v5\n"
    "v_lshrrev_b32 v235, 5, v255\n"
    "v_lshl_add_u32 v236, v235, 3, v233\n"
    "v_xor_b32 v236, v236, v251\n"
    "v_lshrrev_b32 v237, 2, v251\n"
    "v_mul_u32_u24 v237, 0x1000, v237\n"
    "v_lshl_add_u32 v237, v251, 8, v237\n"
    "v_add_u32 v237, 114688, v237\n"
    "v_add_u32 v237, v237, v234\n"
    "v_xor_b32 v239, 0, v236\n"
    "v_lshl_add_u32 v0, v239, 4, v237\n"
    "v_xor_b32 v239, 2, v236\n"
    "v_lshl_add_u32 v1, v239, 4, v237\n"
    "v_xor_b32 v239, 4, v236\n"
    "v_lshl_add_u32 v2, v239, 4, v237\n"
    "v_xor_b32 v239, 6, v236\n"
    "v_lshl_add_u32 v3, v239, 4, v237\n"
    "v_lshl_add_u32 v236, v235, 1, v233\n"
    "v_lshl_add_u32 v236, v236, 4, v251\n"
    "v_lshl_add_u32 v4, v236, 4, v234\n"
    "v_add_u32 v4, 135168, v4\n"
    "s_lshl_b32 s34, s3, 3\n"
    "s_add_u32 s34, s34, 155648\n"
    "v_lshl_add_u32 v5, v251, 5, v5\n"
    "v_add_u32 v5, s34, v5\n"
    "s_add_u32 s34, s44, 135168\n"
    "v_add_u32 v6, s34, v254\n"
    "v_and_b32 v232, 3, v255\n"
    "v_lshl_add_u32 v7, v250, 2, v232\n"
    "v_lshlrev_b32 v7, 5, v7\n"
    "v_add_u32 v7, 155648, v7\n"
    "v_and_b32 v236, 1, v255\n"
    "v_lshl_add_u32 v236, v236, 2, v250\n"
    "s_bfe_u32 s34, s42, 0x30001\n"
    "v_xor_b32 v236, s34, v236\n"
    "v_lshrrev_b32 v237, 1, v251\n"
    "v_lshlrev_b32 v237, 14, v237\n"
    "v_lshl_add_u32 v238, v236, 4, v237\n"
    "s_mul_i32 s46, s3, 655360\n"
    "s_lshl_b32 s34, s40, 7\n"
    "s_add_u32 s46, s46, s34\n"
    "s_cmp_eq_u32 s3, 0\n"
    "s_cselect_b32 s35, 8, 0\n"
    "v_cmp_gt_u32 vcc, s35, v251\n"
    "v_lshlrev_b32 v239, 4, v250\n"
    "v_lshl_add_u32 v239, v232, 2, v239\n"
    "v_bfe_u32 v232, v255, 2, 1\n"
    "v_lshl_add_u32 v239, v232, 6, v239\n"
    "v_mov_b32 v233, 0x7fffffff\n"
    "v_cndmask_b32 v239, v233, v239, vcc\n"
    "v_cmp_gt_u32 vcc, 4, v251\n"
    "s_lshl_b32 s47, s40, 7\n"
    "s_add_u32 s47, s47, 20185088\n"
    "s_mov_b32 s34, 0x05040100\n"
    "s_mov_b32 s35, 0x07060302\n"
    "v_mov_b32 v240, 0\n"
    "v_mov_b32 v241, 0\n"
    "s_mov_b32 s10, 2211840\n"
    "s_mov_b32 s11, 0x20000\n"
    "s_mov_b32 s22, 2621440\n"
    "s_mov_b32 s23, 0x20000\n"
    "s_lshr_b32 s41, s28, 1\n"
    "s_lshl_b32 s31, s41, 2\n"
    "s_lshl_b32 s33, s41, 3\n"
    "s_and_b32 s41, s28, 1\n"
    "s_lshl_b32 s32, s41, 3\n"
    "s_lshl_b32 s30, s41, 2\n"
    "s_and_b32 s41, s29, 3\n"
    "s_add_u32 s31, s31, s41\n"
    "s_add_u32 s30, s30, s41\n"
    "s_lshr_b32 s50, s29, 2\n"
    "s_add_u32 s32, s32, s50\n"
    "s_add_u32 s33, s33, s50\n"
    "s_add_u32 s50, s50, 7\n"
    "s_cmp_ge_u32 s50, 8\n"
    "s_cselect_b32 s41, 8, 0\n"
    "s_sub_u32 s50, s50, s41\n"
    "s_nop 0\n"
    "s_nop 0\n"
    "s_nop 0\n"
    "s_nop 0\n"
    "v_lshrrev_b32 v246, 3, v255\n"
    "v_and_b32 v247, 6, v246\n"
    "v_xor_b32 v247, v247, v255\n"
    "v_and_b32 v247, 7, v247\n"
    "v_mul_u32_u24 v246, 0x500, v246\n"
    "v_lshl_add_u32 v249, v247, 4, v246\n"
    "v_mul_u32_u24 v248, 128, v255\n"
    "s_mul_i32 s52, s3, 40960\n"
    "s_lshl_b32 s54, s30, 7\n"
    "s_add_u32 s52, s52, s54\n"
    "s_mul_i32 s53, s3, 4096\n"
    "s_mov_b32 s10, 2211840\n"
    "s_mov_b32 s11, 0x20000\n"
    "s_lshl_b32 s26, s29, 2\n"
    "s_add_u32 s26, s26, s3\n"
    "s_mul_i32 s26, s26, 14080\n"
    "s_and_b32 s27, s28, 1\n"
    "s_lshl_b32 s27, s27, 9\n"
    "s_add_u32 s26, s26, s27\n"
    "v_lshrrev_b32 v242, 2, v255\n"
    "v_min_u32 v242, 10, v242\n"
    "v_mul_u32_u24 v242, 0x500, v242\n"
    "v_and_b32 v243, 3, v255\n"
    "v_lshl_add_u32 v159, v243, 7, v242\n"
    "s_call_b64 s[0:1], .Lwarm0\n"
    "s_waitcnt vmcnt(4)\n"
    "buffer_load_dword a252, v159, s[8:11], s26 offen\n"
    "s_barrier\n"
    "ds_read2st64_b32 v[68:69], v0 offset0:0 offset1:4\n"
    "ds_read2st64_b32 v[70:71], v1 offset0:0 offset1:4\n"
    "ds_read2st64_b32 v[72:73], v2 offset0:0 offset1:4\n"
    "ds_read2st64_b32 v[74:75], v3 offset0:0 offset1:4\n"
    "ds_read2st64_b32 v[76:77], v0 offset0:8 offset1:12\n"
    "ds_read2st64_b32 v[78:79], v1 offset0:8 offset1:12\n"
    "ds_read2st64_b32 v[80:81], v2 offset0:8 offset1:12\n"
    "ds_read2st64_b32 v[82:83], v3 offset0:8 offset1:12\n"
    "ds_read_b32 v84, v0 offset:4096\n"
    "ds_read_b32 v85, v1 offset:4096\n"
    "ds_read_b32 v86, v2 offset:4096\n"
    "ds_read_b32 v87, v3 offset:4096\n"
    "s_waitcnt lgkmcnt(8)\n"
    "v_dot2c_f32_bf16 v240, v68, v68\n"
    "v_dot2c_f32_bf16 v241, v69, v69\n"
    "v_dot2c_f32_bf16 v240, v70, v70\n"
    "v_dot2c_f32_bf16 v241, v71, v71\n"
    "v_dot2c_f32_bf16 v240, v72, v72\n"
    "v_dot2c_f32_bf16 v241, v73, v73\n"
    "v_dot2c_f32_bf16 v240, v74, v74\n"
    "v_dot2c_f32_bf16 v241, v75, v75\n"
    "v_perm_b32 v88, v70, v68, s34\n"
    "v_perm_b32 v89, v74, v72, s34\n"
    "v_perm_b32 v90, v71, v69, s34\n"
    "v_perm_b32 v91, v75, v73, s34\n"
    "s_waitcnt lgkmcnt(4)\n"
    "v_dot2c_f32_bf16 v240, v76, v76\n"
    "v_dot2c_f32_bf16 v241, v77, v77\n"
    "v_dot2c_f32_bf16 v240, v78, v78\n"
    "v_dot2c_f32_bf16 v241, v79, v79\n"
    "v_dot2c_f32_bf16 v240, v80, v80\n"
    "v_dot2c_f32_bf16 v241, v81, v81\n"
    "v_dot2c_f32_bf16 v240, v82, v82\n"
    "v_dot2c_f32_bf16 v241, v83, v83\n"
    "s_waitcnt lgkmcnt(0)\n"
    "v_dot2c_f32_bf16 v240, v84, v84\n"
    "v_dot2c_f32_bf16 v241, v85, v85\n"
    "v_dot2c_f32_bf16 v240, v86, v86\n"
    "v_dot2c_f32_bf16 v241, v87, v87\n"
    "v_mfma_f32_32x32x16_bf16 v[108:123], v[8:11], v[88:91], 0\n"
    "v_perm_b32 v98, v70, v68, s35\n"
    "v_perm_b32 v99, v74, v72, s35\n"
    "v_perm_b32 v100, v71, v69, s35\n"
    "v_perm_b32 v101, v75, v73, s35\n"
    "v_perm_b32 v92, v78, v76, s34\n"
    "s_nop 0\n"
    "v_mfma_f32_32x32x16_bf16 v[124:139], v[8:11], v[98:101], 0\n"
    "v_perm_b32 v93, v82, v80, s34\n"
    "v_perm_b32 v94, v79, v77, s34\n"
    "v_perm_b32 v95, v83, v81, s34\n"
    "v_perm_b32 v102, v78, v76, s35\n"
    "v_perm_b32 v103, v82, v80, s35\n"
    "v_mfma_f32_32x32x16_bf16 v[108:123], v[12:15], v[92:95], v[108:123]\n"
    "v_perm_b32 v104, v79, v77, s35\n"
    "v_perm_b32 v105, v83, v81, s35\n"
    "v_perm_b32 v96, v85, v84, s34\n"
    "v_perm_b32 v97, v87, v86, s34\n"
    "v_perm_b32 v106, v85, v84, s35\n"
    "v_mfma_f32_32x32x16_bf16 v[124:139], v[12:15], v[102:105], v[124:139]\n"
    "v_perm_b32 v107, v87, v86, s35\n"
    "v_add_f32 v240, v240, v241\n"
    "v_mfma_f32_32x32x8_bf16 v[108:123], v[16:17], v[96:97], v[108:123]\n"
    "v_mfma_f32_32x32x8_bf16 v[124:139], v[16:17], v[106:107], v[124:139]\n"
    "v_mfma_f32_32x32x16_bf16 v[140:155], v[18:21], v[88:91], 0\n"
    "s_nop 6\n"
    "v_mfma_f32_32x32x16_bf16 v[156:171], v[18:21], v[98:101], 0\n"
    "s_nop 2\n"
    "v_add_f32 v241, v118, v134\n"
    "v_cvt_pk_bf16_f32 v172, v108, v124\n"
    "v_cvt_pk_bf16_f32 v173, v109, v125\n"
    "v_cvt_pk_bf16_f32 v174, v110, v126\n"
    "v_mfma_f32_32x32x16_bf16 v[140:155], v[22:25], v[92:95], v[140:155]\n"
    "v_cvt_pk_bf16_f32 v175, v111, v127\n"
    "v_cvt_pk_bf16_f32 v176, v112, v128\n"
    "v_cvt_pk_bf16_f32 v177, v113, v129\n"
    "v_cvt_pk_bf16_f32 v178, v114, v130\n"
    "v_permlane16_swap_b32 v241, v240\n"
    "v_mfma_f32_32x32x16_bf16 v[156:171], v[22:25], v[102:105], v[156:171]\n"
    "ds_write2st64_b32 v4, v172, v173 offset0:0 offset1:4\n"
    "ds_write2st64_b32 v4, v174, v175 offset0:8 offset1:12\n"
    "ds_write2st64_b32 v4, v176, v177 offset0:16 offset1:20\n"
    "v_cvt_pk_bf16_f32 v179, v115, v131\n"
    "v_cvt_pk_bf16_f32 v180, v116, v132\n"
    "v_cvt_pk_bf16_f32 v181, v117, v133\n"
    "v_mfma_f32_32x32x8_bf16 v[140:155], v[26:27], v[96:97], v[140:155]\n"
    "ds_write2st64_b32 v4, v178, v179 offset0:24 offset1:28\n"
    "ds_write2st64_b32 v4, v180, v181 offset0:32 offset1:36\n"
    "v_add_f32 v240, v240, v241\n"
    "v_mov_b32 v241, v240\n"
    "v_mfma_f32_32x32x8_bf16 v[156:171], v[26:27], v[106:107], v[156:171]\n"
    "s_nop 0\n"
    "v_permlane32_swap_b32 v240, v241\n"
    "v_add_f32 v240, v240, v241\n"
    "ds_write_b32 v5, v240\n"
    "s_nop 7\n"
    "v_cvt_pk_bf16_f32 v182, v140, v156\n"
    "v_cvt_pk_bf16_f32 v183, v141, v157\n"
    "v_cvt_pk_bf16_f32 v184, v142, v158\n"
    "v_cvt_pk_bf16_f32 v185, v143, v159\n"
    "v_cvt_pk_bf16_f32 v186, v144, v160\n"
    "v_cvt_pk_bf16_f32 v187, v145, v161\n"
    "v_cvt_pk_bf16_f32 v188, v146, v162\n"
    "v_cvt_pk_bf16_f32 v189, v147, v163\n"
    "v_cvt_pk_bf16_f32 v190, v148, v164\n"
    "v_cvt_pk_bf16_f32 v191, v149, v165\n"
    "ds_write2st64_b32 v4, v182, v183 offset0:40 offset1:44\n"
    "ds_write2st64_b32 v4, v184, v185 offset0:48 offset1:52\n"
    "ds_write2st64_b32 v4, v186, v187 offset0:56 offset1:60\n"
    "ds_write2st64_b32 v4, v188, v189 offset0:64 offset1:68\n"
    "ds_write2st64_b32 v4, v190, v191 offset0:72 offset1:76\n"
    "s_waitcnt lgkmcnt(0)\n"
    "s_barrier\n"
    "ds_read_b128 v[88:91], v6\n"
    "ds_read_b128 v[68:71], v7\n"
    "ds_read_b128 v[72:75], v7 offset:16\n"
    "ds_read_b128 v[92:95], v6 offset:1024\n"
    "ds_read_b128 v[96:99], v6 offset:2048\n"
    ".Lpad0:\n"
    "s_cbranch_execz .Lw_ret\n"
    "ds_read_b128 v[100:103], v6 offset:3072\n"
    "ds_read_b128 v[104:107], v6 offset:4096\n"
    "s_nop 0\n"
    "s_waitcnt lgkmcnt(6)\n"
    "v_mfma_f32_16x16x32_bf16 v[192:195], v[88:91], v[28:31], 0\n"
    "s_waitcnt lgkmcnt(5)\n"
    "v_add_f32 v68, v68, v70\n"
    "s_waitcnt lgkmcnt(4)\n"
    "v_add_f32 v72, v72, v74\n"
    "v_mfma_f32_16x16x32_bf16 v[196:199], v[88:91], v[32:35], 0\n"
    "s_waitcnt lgkmcnt(3)\n"
    "v_add_f32 v69, v69, v71\n"
    "v_add_f32 v73, v73, v75\n"
    "v_mfma_f32_16x16x32_bf16 v[200:203], v[92:95], v[36:39], 0\n"
    "v_add_f32 v68, v68, v72\n"
    "v_add_f32 v69, v69, v73\n"
    "v_mfma_f32_16x16x32_bf16 v[204:207], v[92:95], v[40:43], 0\n"
    "s_waitcnt lgkmcnt(2)\n"
    "v_mul_f32 v68, 0x3a4ccccd, v68\n"
    "v_mul_f32 v69, 0x3acccccd, v69\n"
    "v_mfma_f32_16x16x32_bf16 v[208:211], v[96:99], v[44:47], 0\n"
    "v_fma_f32 v69, -v68, v68, v69\n"
    "v_max_f32 v69, 0, v69\n"
    "v_mfma_f32_16x16x32_bf16 v[212:215], v[96:99], v[48:51], 0\n"
    "s_waitcnt lgkmcnt(1)\n"
    "v_add_f32 v69, 0x3727c5ac, v69\n"
    "v_mul_f32 v68, 0x32ca62c2, v68\n"
    "v_mfma_f32_16x16x32_bf16 v[216:219], v[100:103], v[52:55], 0\n"
    "v_rsq_f32 v70, v69\n"
    "v_mfma_f32_16x16x32_bf16 v[220:223], v[100:103], v[56:59], 0\n"
    "s_waitcnt lgkmcnt(0)\n"
    "v_mul_f32 v69, 0x0e800000, v69\n"
    "v_mul_f32 v71, 0x1f64f92e, v70\n"
    "v_mfma_f32_16x16x32_bf16 v[224:227], v[104:107], v[60:63], 0\n"
    "v_mul_f32 v69, v69, v70\n"
    "v_cndmask_b32 v68, v68, v69, vcc\n"
    "v_mfma_f32_16x16x32_bf16 v[228:231], v[104:107], v[64:67], 0\n"
    "v_mov_b32_dpp v72, v71 quad_perm:[0,0,0,0] row_mask:0xf bank_mask:0xf\n"
    "v_mov_b32_dpp v73, v71 quad_perm:[1,1,1,1] row_mask:0xf bank_mask:0xf\n"
    "v_mov_b32_dpp v74, v71 quad_perm:[2,2,2,2] row_mask:0xf bank_mask:0xf\n"
    "v_mov_b32_dpp v75, v71 quad_perm:[3,3,3,3] row_mask:0xf bank_mask:0xf\n"
    "v_pk_mul_f32 v[192:193], v[192:193], v[72:73]\n"
    "v_pk_mul_f32 v[194:195], v[194:195], v[74:75]\n"
    "v_pk_mul_f32 v[196:197], v[196:197], v[72:73]\n"
    "v_pk_mul_f32 v[198:199], v[198:199], v[74:75]\n"
    "v_cvt_pk_bf16_f32 v172, v192, v193\n"
    "v_cvt_pk_bf16_f32 v173, v194, v195\n"
    "v_cvt_pk_bf16_f32 v174, v196, v197\n"
    "v_cvt_pk_bf16_f32 v175, v198, v199\n"
    "v_or_b32 v172, 0x40004000, v172\n"
    "v_or_b32 v173, 0x40004000, v173\n"
    "v_or_b32 v174, 0x40004000, v174\n"
    "v_or_b32 v175, 0x40004000, v175\n"
    "buffer_store_dwordx4 v[172:175], v238, s[4:7], s46 offen sc1\n"
    "v_pk_mul_f32 v[200:201], v[200:201], v[72:73]\n"
    "v_pk_mul_f32 v[202:203], v[202:203], v[74:75]\n"
    "v_pk_mul_f32 v[204:205], v[204:205], v[72:73]\n"
    "v_pk_mul_f32 v[206:207], v[206:207], v[74:75]\n"
    "v_cvt_pk_bf16_f32 v176, v200, v201\n"
    "v_cvt_pk_bf16_f32 v177, v202, v203\n"
    "v_cvt_pk_bf16_f32 v178, v204, v205\n"
    "v_cvt_pk_bf16_f32 v179, v206, v207\n"
    "v_or_b32 v176, 0x40004000, v176\n"
    "v_or_b32 v177, 0x40004000, v177\n"
    "v_or_b32 v178, 0x40004000, v178\n"
    "v_or_b32 v179, 0x40004000, v179\n"
    "s_add_u32 s46, s46, 131072\n"
    "buffer_store_dwordx4 v[176:179], v238, s[4:7], s46 offen sc1\n"
    "v_pk_mul_f32 v[208:209], v[208:209], v[72:73]\n"
    "v_pk_mul_f32 v[210:211], v[210:211], v[74:75]\n"
    "v_pk_mul_f32 v[212:213], v[212:213], v[72:73]\n"
    "v_pk_mul_f32 v[214:215], v[214:215], v[74:75]\n"
    "v_cvt_pk_bf16_f32 v180, v208, v209\n"
    "v_cvt_pk_bf16_f32 v181, v210, v211\n"
    "v_cvt_pk_bf16_f32 v182, v212, v213\n"
    "v_cvt_pk_bf16_f32 v183, v214, v215\n"
    "v_or_b32 v180, 0x40004000, v180\n"
    "v_or_b32 v181, 0x40004000, v181\n"
    "v_or_b32 v182, 0x40004000, v182\n"
    "v_or_b32 v183, 0x40004000, v183\n"
    "s_add_u32 s46, s46, 131072\n"
    "buffer_store_dwordx4 v[180:183], v238, s[4:7], s46 offen sc1\n"
    "v_pk_mul_f32 v[216:217], v[216:217], v[72:73]\n"
    "v_pk_mul_f32 v[218:219], v[218:219], v[74:75]\n"
    "v_pk_mul_f32 v[220:221], v[220:221], v[72:73]\n"
    "v_pk_mul_f32 v[222:223], v[222:223], v[74:75]\n"
    "v_cvt_pk_bf16_f32 v184, v216, v217\n"
    "v_cvt_pk_bf16_f32 v185, v218, v219\n"
    "v_cvt_pk_bf16_f32 v186, v220, v221\n"
    "v_cvt_pk_bf16_f32 v187, v222, v223\n"
    "v_or_b32 v184, 0x40004000, v184\n"
    "v_or_b32 v185, 0x40004000, v185\n"
    "v_or_b32 v186, 0x40004000, v186\n"
    "v_or_b32 v187, 0x40004000, v187\n"
    "s_add_u32 s46, s46, 131072\n"
    "buffer_store_dwordx4 v[184:187], v238, s[4:7], s46 offen sc1\n"
    "v_pk_mul_f32 v[224:225], v[224:225], v[72:73]\n"
    "v_pk_mul_f32 v[226:227], v[226:227], v[74:75]\n"
    "v_pk_mul_f32 v[228:229], v[228:229], v[72:73]\n"
    "v_pk_mul_f32 v[230:231], v[230:231], v[74:75]\n"
    "v_cvt_pk_bf16_f32 v188, v224, v225\n"
    "v_cvt_pk_bf16_f32 v189, v226, v227\n"
    "v_cvt_pk_bf16_f32 v190, v228, v229\n"
    "v_cvt_pk_bf16_f32 v191, v230, v231\n"
    "v_or_b32 v188, 0x40004000, v188\n"
    "v_or_b32 v189, 0x40004000, v189\n"
    "v_or_b32 v190, 0x40004000, v190\n"
    "v_or_b32 v191, 0x40004000, v191\n"
    "s_add_u32 s46, s46, 131072\n"
    "buffer_store_dwordx4 v[188:191], v238, s[4:7], s46 offen sc1\n"
    "buffer_store_dword v68, v239, s[4:7], s47 offen sc1\n"
    "s_waitcnt vmcnt(10)\n"
    "v_add_u32 v253, 0x9e3779b9, v253\n"
    "s_add_u32 m0, s53, 0\n"
    "s_add_u32 s54, s52, 0\n"
    "buffer_load_dwordx4 v249, s[8:11], s54 offen lds\n"
    "s_lshr_b32 s55, s2, 2\n"
    "s_mul_i32 s55, s55, 128\n"
    "s_and_b32 s35, s2, 3\n"
    "s_lshl_b32 s35, s35, 2\n"
    "s_add_u32 s55, s55, s35\n"
    "s_mul_i32 s35, s3, 16384\n"
    "s_add_u32 s55, s55, s35\n"
    "s_add_u32 s55, s55, 20267008\n"
    "s_cmp_lt_u32 s3, 8\n"
    "s_cselect_b32 s35, 2, 0\n"
    "v_mul_u32_u24 v246, 8192, v255\n"
    "v_mov_b32 v247, 0x7fffffff\n"
    "v_cmp_gt_u32 vcc, s35, v255\n"
    "s_nop 1\n"
    "v_cndmask_b32 v252, v247, v246, vcc\n"
    "v_lshrrev_b32 v244, 2, v251\n"
    "v_lshl_add_u32 v244, v250, 2, v244\n"
    "v_bfe_u32 v245, v255, 1, 1\n"
    "v_and_b32 v246, 6, v244\n"
    "s_add_u32 m0, s53, 1024\n"
    "s_add_u32 s54, s52, 10240\n"
    "buffer_load_dwordx4 v249, s[8:11], s54 offen lds\n"
    "v_or_b32 v245, v245, v246\n"
    "v_and_b32 v246, 1, v255\n"
    "v_lshlrev_b32 v246, 3, v246\n"
    "v_lshl_add_u32 v246, v244, 7, v246\n"
    "v_add_u32 v246, s53, v246\n"
    "v_xor_b32 v247, 0, v245\n"
    "v_lshl_add_u32 v240, v247, 4, v246\n"
    "v_xor_b32 v247, 2, v245\n"
    "v_lshl_add_u32 v241, v247, 4, v246\n"
    "v_xor_b32 v247, 4, v245\n"
    "v_lshl_add_u32 v242, v247, 4, v246\n"
    "v_xor_b32 v247, 6, v245\n"
    "v_lshl_add_u32 v243, v247, 4, v246\n"
    "s_lshl_b32 s34, s3, 4\n"
    "v_add_u32 v0, s34, v251\n"
    "v_cvt_f32_u32 v0, v0\n"
    "v_lshlrev_b32 v1, 2, v250\n"
    "s_add_u32 m0, s53, 2048\n"
    "s_add_u32 s54, s52, 20480\n"
    "buffer_load_dwordx4 v249, s[8:11], s54 offen lds\n"
    "v_cvt_f32_u32 v2, v1\n"
    "v_cvt_f32_u32 v3, v251\n"
    "v_add_u32 v1, s34, v1\n"
    "v_cvt_f32_u32 v1, v1\n"
    "v_mul_f32 v4, 0x3c800000, v0\n"
    "v_mul_f32 v5, v4, v2\n"
    "v_and_b32 v12, 1, v255\n"
    "v_mul_u32_u24 v12, 0x8000, v12\n"
    "v_lshl_or_b32 v12, v12, 16, v12\n"
    "v_cos_f32 v8, v5\n"
    "v_sin_f32 v10, v5\n"
    "v_fmamk_f32 v6, v4, 0x3f800000, v5\n"
    "v_cos_f32 v9, v6\n"
    "v_sin_f32 v11, v6\n"
    "s_add_u32 m0, s53, 3072\n"
    "s_add_u32 s54, s52, 30720\n"
    "buffer_load_dwordx4 v249, s[8:11], s54 offen lds\n"
    "v_cvt_pk_bf16_f32 v162, v8, v9\n"
    "v_cvt_pk_bf16_f32 v164, v10, v11\n"
    "v_xor_b32 v160, 0x80008000, v164\n"
    "v_fmamk_f32 v6, v4, 0x40000000, v5\n"
    "v_cos_f32 v8, v6\n"
    "v_sin_f32 v10, v6\n"
    "v_fmamk_f32 v6, v4, 0x40400000, v5\n"
    "v_cos_f32 v9, v6\n"
    "v_sin_f32 v11, v6\n"
    "v_cvt_pk_bf16_f32 v163, v8, v9\n"
    "v_cvt_pk_bf16_f32 v165, v10, v11\n"
    "v_xor_b32 v161, 0x80008000, v165\n"
    "v_fmamk_f32 v6, v4, 0x41800000, v5\n"
    "v_cos_f32 v8, v6\n"
    "v_sin_f32 v10, v6\n"
    "v_fmamk_f32 v6, v4, 0x41880000, v5\n"
    "v_cos_f32 v9, v6\n"
    "v_sin_f32 v11, v6\n"
    "v_cvt_pk_bf16_f32 v168, v8, v9\n"
    "v_cvt_pk_bf16_f32 v170, v10, v11\n"
    "v_xor_b32 v166, 0x80008000, v170\n"
    "v_fmamk_f32 v6, v4, 0x41900000, v5\n"
    "v_cos_f32 v8, v6\n"
    "v_sin_f32 v10, v6\n"
    "v_fmamk_f32 v6, v4, 0x41980000, v5\n"
    "v_cos_f32 v9, v6\n"
    "v_sin_f32 v11, v6\n"
    "v_cvt_pk_bf16_f32 v169, v8, v9\n"
    "v_cvt_pk_bf16_f32 v171, v10, v11\n"
    "v_xor_b32 v167, 0x80008000, v171\n"
    "v_xor_b32 v172, v12, v160\n"
    "v_xor_b32 v173, v12, v161\n"
    "v_xor_b32 v174, v12, v162\n"
    "v_xor_b32 v175, v12, v163\n"
    "v_xor_b32 v176, v12, v164\n"
    "v_xor_b32 v177, v12, v165\n"
    "v_xor_b32 v178, v12, v166\n"
    "v_xor_b32 v179, v12, v167\n"
    "v_xor_b32 v180, v12, v168\n"
    "v_xor_b32 v181, v12, v169\n"
    "v_xor_b32 v182, v12, v170\n"
    "v_xor_b32 v183, v12, v171\n"
    "s_waitcnt vmcnt(4)\n"
    "s_nop 1\n"
    ".Larr:\n"
    "s_waitcnt lgkmcnt(0)\n"
    "s_barrier\n"
    "buffer_store_dword v253, v252, s[4:7], s55 offen sc1\n"
    "s_nop 1\n"
    ".Larr4:\n"
    ".Lpad1:\n"
    "s_cbranch_execz .Lpad4\n"
    "s_and_b32 s40, s28, 7\n"
    "s_mul_i32 s40, s40, 8192\n"
    "s_add_u32 s40, s40, 20267008\n"
    "s_cmp_ge_u32 s29, 20\n"
    "s_cbranch_scc1 .Lidle\n"
    "s_lshl_b32 s42, s2, 14\n"
    "s_lshl_b32 s43, s3, 12\n"
    "s_add_u32 s42, s42, s43\n"
    "s_add_u32 m0, s53, 16384\n"
    "s_add_u32 s54, s52, 163840\n"
    "buffer_load_dwordx4 v249, s[8:11], s54 offen lds\n"
    "s_lshl_b32 s34, s3, 4\n"
    "v_add_u32 v0, s34, v251\n"
    "v_cvt_f32_u32 v0, v0\n"
    "v_lshlrev_b32 v1, 2, v250\n"
    "v_cvt_f32_u32 v2, v1\n"
    "v_cvt_f32_u32 v3, v251\n"
    "v_add_u32 v1, s34, v1\n"
    "v_cvt_f32_u32 v1, v1\n"
    "v_mul_f32 v4, 0x3d000000, v3\n"
    "v_mul_f32 v5, v4, v2\n"
    "v_cos_f32 v8, v5\n"
    "v_sin_f32 v10, v5\n"
    "v_fmamk_f32 v6, v4, 0x3f800000, v5\n"
    "v_cos_f32 v9, v6\n"
    "v_sin_f32 v11, v6\n"
    "s_add_u32 m0, s53, 17408\n"
    "s_add_u32 s54, s52, 174080\n"
    "buffer_load_dwordx4 v249, s[8:11], s54 offen lds\n"
    "v_cvt_pk_bf16_f32 v184, v8, v9\n"
    "v_cvt_pk_bf16_f32 v192, v10, v11\n"
    "v_xor_b32 v188, 0x80000000, v184\n"
    "v_xor_b32 v196, 0x80000000, v192\n"
    "v_xor_b32 v200, 0x80008000, v192\n"
    "v_xor_b32 v204, 0x00008000, v192\n"
    "v_fmamk_f32 v6, v4, 0x40000000, v5\n"
    "v_cos_f32 v8, v6\n"
    "v_sin_f32 v10, v6\n"
    "v_fmamk_f32 v6, v4, 0x40400000, v5\n"
    "v_cos_f32 v9, v6\n"
    "v_sin_f32 v11, v6\n"
    "v_cvt_pk_bf16_f32 v185, v8, v9\n"
    "s_add_u32 m0, s53, 18432\n"
    "s_add_u32 s54, s52, 184320\n"
    "buffer_load_dwordx4 v249, s[8:11], s54 offen lds\n"
    "v_cvt_pk_bf16_f32 v193, v10, v11\n"
    "v_xor_b32 v189, 0x80000000, v185\n"
    "v_xor_b32 v197, 0x80000000, v193\n"
    "v_xor_b32 v201, 0x80008000, v193\n"
    "v_xor_b32 v205, 0x00008000, v193\n"
    "v_fmamk_f32 v6, v4, 0x41800000, v5\n"
    "v_cos_f32 v8, v6\n"
    "v_sin_f32 v10, v6\n"
    "v_fmamk_f32 v6, v4, 0x41880000, v5\n"
    "v_cos_f32 v9, v6\n"
    "v_sin_f32 v11, v6\n"
    "v_cvt_pk_bf16_f32 v186, v8, v9\n"
    "v_cvt_pk_bf16_f32 v194, v10, v11\n"
    "v_xor_b32 v190, 0x80000000, v186\n"
    "s_add_u32 m0, s53, 19456\n"
    "s_add_u32 s54, s52, 194560\n"
    "buffer_load_dwordx4 v249, s[8:11], s54 offen lds\n"
    "v_xor_b32 v198, 0x80000000, v194\n"
    "v_xor_b32 v202, 0x80008000, v194\n"
    "v_xor_b32 v206, 0x00008000, v194\n"
    "v_fmamk_f32 v6, v4, 0x41900000, v5\n"
    "v_cos_f32 v8, v6\n"
    "v_sin_f32 v10, v6\n"
    "v_fmamk_f32 v6, v4, 0x41980000, v5\n"
    "v_cos_f32 v9, v6\n"
    "v_sin_f32 v11, v6\n"
    "v_cvt_pk_bf16_f32 v187, v8, v9\n"
    "v_cvt_pk_bf16_f32 v195, v10, v11\n"
    "v_xor_b32 v191, 0x80000000, v187\n"
    "v_xor_b32 v199, 0x80000000, v195\n"
    "v_xor_b32 v203, 0x80008000, v195\n"
    "s_add_u32 m0, s53, 32768\n"
    "s_add_u32 s54, s52, 327680\n"
    "buffer_load_dwordx4 v249, s[8:11], s54 offen lds\n"
    "v_xor_b32 v207, 0x00008000, v195\n"
    "v_mul_f32 v4, 0x3a000000, v0\n"
    "v_mul_f32 v5, v4, v2\n"
    "v_cos_f32 v208, v5\n"
    "v_sin_f32 v216, v5\n"
    "v_fmamk_f32 v6, v4, 0x3f800000, v5\n"
    "v_cos_f32 v209, v6\n"
    "v_sin_f32 v217, v6\n"
    "v_fmamk_f32 v6, v4, 0x40000000, v5\n"
    "v_cos_f32 v210, v6\n"
    "v_sin_f32 v218, v6\n"
    "v_fmamk_f32 v6, v4, 0x40400000, v5\n"
    "s_add_u32 m0, s53, 33792\n"
    "s_add_u32 s54, s52, 337920\n"
    "buffer_load_dwordx4 v249, s[8:11], s54 offen lds\n"
    "v_cos_f32 v211, v6\n"
    "v_sin_f32 v219, v6\n"
    "v_fmamk_f32 v6, v4, 0x41800000, v5\n"
    "v_cos_f32 v212, v6\n"
    "v_sin_f32 v220, v6\n"
    "v_fmamk_f32 v6, v4, 0x41880000, v5\n"
    "v_cos_f32 v213, v6\n"
    "v_sin_f32 v221, v6\n"
    "v_fmamk_f32 v6, v4, 0x41900000, v5\n"
    "v_cos_f32 v214, v6\n"
    "v_sin_f32 v222, v6\n"
    "s_add_u32 m0, s53, 34816\n"
    "s_add_u32 s54, s52, 348160\n"
    "buffer_load_dwordx4 v249, s[8:11], s54 offen lds\n"
    "v_fmamk_f32 v6, v4, 0x41980000, v5\n"
    "v_cos_f32 v215, v6\n"
    "v_sin_f32 v223, v6\n"
    "v_mul_f32 v4, 0x3a000000, v3\n"
    "v_mul_f32 v5, v4, v1\n"
    "v_cos_f32 v224, v5\n"
    "v_sin_f32 v232, v5\n"
    "v_fmamk_f32 v6, v4, 0x3f800000, v5\n"
    "v_cos_f32 v225, v6\n"
    "v_sin_f32 v233, v6\n"
    "v_fmamk_f32 v6, v4, 0x40000000, v5\n"
    "s_add_u32 m0, s53, 35840\n"
    "s_add_u32 s54, s52, 358400\n"
    "buffer_load_dwordx4 v249, s[8:11], s54 offen lds\n"
    "v_cos_f32 v226, v6\n"
    "v_sin_f32 v234, v6\n"
    "v_fmamk_f32 v6, v4, 0x40400000, v5\n"
    "v_cos_f32 v227, v6\n"
    "v_sin_f32 v235, v6\n"
    "v_add_f32 v3, 0x41800000, v3\n"
    "v_mul_f32 v4, 0x3a000000, v3\n"
    "v_mul_f32 v5, v4, v1\n"
    "v_cos_f32 v228, v5\n"
    "v_sin_f32 v236, v5\n"
    "v_fmamk_f32 v6, v4, 0x3f800000, v5\n"
    "v_cos_f32 v229, v6\n"
    "s_add_u32 m0, s53, 49152\n"
    "s_add_u32 s54, s52, 491520\n"
    "buffer_load_dwordx4 v249, s[8:11], s54 offen lds\n"
    "v_sin_f32 v237, v6\n"
    "v_fmamk_f32 v6, v4, 0x40000000, v5\n"
    "v_cos_f32 v230, v6\n"
    "v_sin_f32 v238, v6\n"
    "v_fmamk_f32 v6, v4, 0x40400000, v5\n"
    "v_cos_f32 v231, v6\n"
    "v_sin_f32 v239, v6\n"
    "v_mov_b32 v4, 0x1d800000\n"
    "v_mov_b32 v5, 0x2b000000\n"
    "v_pk_mul_f32 v[208:209], v[208:209], v[4:5] op_sel:[0,0] op_sel_hi:[1,0]\n"
    "v_pk_mul_f32 v[210:211], v[210:211], v[4:5] op_sel:[0,0] op_sel_hi:[1,0]\n"
    "v_pk_mul_f32 v[212:213], v[212:213], v[4:5] op_sel:[0,0] op_sel_hi:[1,0]\n"
    "s_add_u32 m0, s53, 50176\n"
    "s_add_u32 s54, s52, 501760\n"
    "buffer_load_dwordx4 v249, s[8:11], s54 offen lds\n"
    "v_pk_mul_f32 v[214:215], v[214:215], v[4:5] op_sel:[0,0] op_sel_hi:[1,0]\n"
    "v_pk_mul_f32 v[216:217], v[216:217], v[4:5] op_sel:[0,0] op_sel_hi:[1,0]\n"
    "v_pk_mul_f32 v[218:219], v[218:219], v[4:5] op_sel:[0,0] op_sel_hi:[1,0]\n"
    "v_pk_mul_f32 v[220:221], v[220:221], v[4:5] op_sel:[0,0] op_sel_hi:[1,0]\n"
    "v_pk_mul_f32 v[222:223], v[222:223], v[4:5] op_sel:[0,0] op_sel_hi:[1,0]\n"
    "v_pk_mul_f32 v[224:225], v[224:225], v[4:5] op_sel:[0,1] op_sel_hi:[1,1]\n"
    "v_pk_mul_f32 v[226:227], v[226:227], v[4:5] op_sel:[0,1] op_sel_hi:[1,1]\n"
    "v_pk_mul_f32 v[228:229], v[228:229], v[4:5] op_sel:[0,1] op_sel_hi:[1,1]\n"
    "v_pk_mul_f32 v[230:231], v[230:231], v[4:5] op_sel:[0,1] op_sel_hi:[1,1]\n"
    "v_pk_mul_f32 v[232:233], v[232:233], v[4:5] op_sel:[0,1] op_sel_hi:[1,1]\n"
    "v_pk_mul_f32 v[234:235], v[234:235], v[4:5] op_sel:[0,1] op_sel_hi:[1,1]\n"
    "v_pk_mul_f32 v[236:237], v[236:237], v[4:5] op_sel:[0,1] op_sel_hi:[1,1]\n"
    "v_pk_mul_f32 v[238:239], v[238:239], v[4:5] op_sel:[0,1] op_sel_hi:[1,1]\n"
    "s_mul_i32 s44, s3, 10240\n"
    "s_mul_i32 s45, s32, 40960\n"
    "s_add_u32 s45, s45, s44\n"
    "s_add_u32 s45, s45, 5242880\n"
    "v_lshrrev_b32 v0, 1, v251\n"
    "s_add_u32 m0, s53, 51200\n"
    "s_add_u32 s54, s52, 512000\n"
    "buffer_load_dwordx4 v249, s[8:11], s54 offen lds\n"
    "v_xor_b32 v0, v0, v250\n"
    "v_lshlrev_b32 v1, 7, v251\n"
    "v_add_u32 v1, 131072, v1\n"
    "v_lshl_add_u32 v152, v0, 4, v1\n"
    "v_xor_b32 v153, 64, v152\n"
    "v_add_u32 v154, s43, v152\n"
    "v_add_u32 v155, s43, v153\n"
    "s_lshl_b32 s34, s3, 10\n"
    "s_add_u32 s34, s34, 147456\n"
    "v_add_u32 v156, s34, v254\n"
    "v_add_u32 v157, 147456, v254\n"
    "v_min_u32 v244, 6016, v248\n"
    "v_lshlrev_b32 v149, 4, v250\n"
    "v_lshl_add_u32 v149, v251, 7, v149\n"
    "s_cmp_eq_u32 s2, 0\n"
    "s_cselect_b64 s[48:49], -1, 0\n"
    "s_cmp_eq_u32 s2, 0\n"
    "s_cselect_b32 s34, 0, 0x7f000000\n"
    "s_add_u32 m0, s53, 52224\n"
    "s_add_u32 s54, s52, 522240\n"
    "buffer_load_dwordx4 v249, s[8:11], s54 offen lds\n"
    "v_or_b32 v150, s34, v149\n"
    "s_add_u32 s41, s55, 65536\n"
    "s_and_b32 s34, s29, 0\n"
    "s_mov_b32 s12, s44\n"
    "s_mov_b32 s16, s45\n"
    "s_add_u32 s13, s44, 40960\n"
    "s_add_u32 s17, s45, 655360\n"
    "s_lshl_b32 s14, s3, 10\n"
    "s_mul_i32 s18, s32, 40960\n"
    "s_add_u32 s18, s18, s14\n"
    "s_add_u32 s18, s18, 6553600\n"
    "s_add_u32 s14, s14, 81920\n"
    "s_mul_i32 s34, s3, 0x10000000\n"
    "v_or_b32 v244, s34, v244\n"
    "s_and_b32 s38, s28, 1\n"
    "s_mul_i32 s38, s38, 5\n"
    "s_lshr_b32 s39, s29, 2\n"
    "s_add_u32 s38, s38, s39\n"
    "s_add_u32 m0, s53, 65536\n"
    "s_add_u32 s54, s52, 655360\n"
    "buffer_load_dwordx4 v249, s[8:11], s54 offen lds\n"
    "s_lshl_b32 s38, s38, 5\n"
    "v_lshrrev_b32 v64, 2, v251\n"
    "v_and_b32 v65, 3, v255\n"
    "v_lshl_add_u32 v64, v64, 3, v65\n"
    "v_add_u32 v64, s38, v64\n"
    "v_cvt_f32_u32 v65, v64\n"
    "s_mul_i32 s38, s3, 10\n"
    "s_cmp_ge_u32 s3, 2\n"
    "s_cselect_b32 s39, 19, 0\n"
    "s_sub_u32 s38, s38, s39\n"
    "v_cmp_gt_u32 vcc, 32, v255\n"
    "v_mul_u32_u24 v66, 0xa0, v250\n"
    "v_add_u32 v67, s38, v66\n"
    "v_sub_u32 v68, 640, v67\n"
    "v_subrev_u32 v69, 300, v66\n"
    "v_cndmask_b32 v67, v68, v67, vcc\n"
    "v_cndmask_b32 v69, v69, v66, vcc\n"
    "v_cvt_f32_u32 v67, v67\n"
    "s_add_u32 m0, s53, 66560\n"
    "s_add_u32 s54, s52, 665600\n"
    "buffer_load_dwordx4 v249, s[8:11], s54 offen lds\n"
    "v_cvt_f32_u32 v69, v69\n"
    "v_cndmask_b32_e64 v68, -1.0, 1.0, vcc\n"
    "s_cmp_eq_u32 s3, 0\n"
    "s_cselect_b64 s[36:37], -1, 0\n"
    "v_cndmask_b32_e64 v69, v67, v69, s[36:37]\n"
    "v_cndmask_b32_e64 v70, v68, 1.0, s[36:37]\n"
    "v_cmp_gt_u32 vcc, 16, v255\n"
    "v_and_b32 v71, 1, v255\n"
    "s_and_b64 s[36:37], s[36:37], vcc\n"
    "v_lshlrev_b32 v71, 15, v71\n"
    "v_or_b32 v71, 0x3f353f35, v71\n"
    "v_mul_f32 v73, 0x3acccccd, v65\n"
    "v_mul_f32 v74, v73, v67\n"
    "v_mul_f32 v75, v73, v68\n"
    "v_mul_f32 v76, v73, v69\n"
    "v_mul_f32 v77, v73, v70\n"
    "v_add_f32 v78, v73, v73\n"
    "v_cos_f32 v78, v78\n"
    "s_add_u32 m0, s53, 67584\n"
    "s_add_u32 s54, s52, 675840\n"
    "buffer_load_dwordx4 v249, s[8:11], s54 offen lds\n"
    "v_sin_f32_e64 v81, -v76\n"
    "v_cos_f32 v80, v76\n"
    "v_fmamk_f32 v82, v77, 0x42200000, v76\n"
    "v_sin_f32_e64 v83, -v82\n"
    "v_cos_f32 v82, v82\n"
    "v_fmamk_f32 v84, v77, 0x42a00000, v76\n"
    "v_sin_f32_e64 v85, -v84\n"
    "v_cos_f32 v84, v84\n"
    "v_fmamk_f32 v86, v77, 0x42f00000, v76\n"
    "v_sin_f32_e64 v87, -v86\n"
    "s_add_u32 m0, s53, 68608\n"
    "s_add_u32 s54, s52, 686080\n"
    "buffer_load_dwordx4 v249, s[8:11], s54 offen lds\n"
    "v_cos_f32 v86, v86\n"
    "v_fmamk_f32 v88, v75, 0x40000000, v74\n"
    "v_sin_f32_e64 v89, -v88\n"
    "v_cos_f32 v88, v88\n"
    "v_fmamk_f32 v90, v75, 0x42280000, v74\n"
    "v_sin_f32_e64 v91, -v90\n"
    "v_cos_f32 v90, v90\n"
    "v_fmamk_f32 v92, v75, 0x42a40000, v74\n"
    "v_sin_f32_e64 v93, -v92\n"
    "v_cos_f32 v92, v92\n"
    "v_fmamk_f32 v94, v75, 0x42f40000, v74\n"
    "s_add_u32 m0, s53, 81920\n"
    "s_add_u32 s54, s52, 819200\n"
    "buffer_load_dwordx4 v249, s[8:11], s54 offen lds\n"
    "v_sin_f32_e64 v95, -v94\n"
    "v_cos_f32 v94, v94\n"
    "v_fmamk_f32 v96, v75, 0x40800000, v74\n"
    "v_sin_f32_e64 v97, -v96\n"
    "v_cos_f32 v96, v96\n"
    "v_fmamk_f32 v98, v75, 0x42300000, v74\n"
    "v_sin_f32_e64 v99, -v98\n"
    "v_cos_f32 v98, v98\n"
    "v_fmamk_f32 v100, v75, 0x42a80000, v74\n"
    "v_sin_f32_e64 v101, -v100\n"
    "v_cos_f32 v100, v100\n"
    "s_add_u32 m0, s53, 82944\n"
    "s_add_u32 s54, s52, 829440\n"
    "buffer_load_dwordx4 v249, s[8:11], s54 offen lds\n"
    "v_fmamk_f32 v102, v75, 0x42f80000, v74\n"
    "v_sin_f32_e64 v103, -v102\n"
    "v_cos_f32 v102, v102\n"
    "v_add_f32 v78, v78, v78\n"
    "v_pk_fma_f32 v[104:105], v[96:97], v[78:79], v[88:89] op_sel_hi:[1,0,1] neg_lo:[0,0,1] neg_hi:[0,0,1]\n"
    "v_pk_fma_f32 v[106:107], v[98:99], v[78:79], v[90:91] op_sel_hi:[1,0,1] neg_lo:[0,0,1] neg_hi:[0,0,1]\n"
    "v_pk_fma_f32 v[108:109], v[100:101], v[78:79], v[92:93] op_sel_hi:[1,0,1] neg_lo:[0,0,1] neg_hi:[0,0,1]\n"
    "v_pk_fma_f32 v[110:111], v[102:103], v[78:79], v[94:95] op_sel_hi:[1,0,1] neg_lo:[0,0,1] neg_hi:[0,0,1]\n"
    "v_pk_fma_f32 v[112:113], v[104:105], v[78:79], v[96:97] op_sel_hi:[1,0,1] neg_lo:[0,0,1] neg_hi:[0,0,1]\n"
    "v_pk_fma_f32 v[114:115], v[106:107], v[78:79], v[98:99] op_sel_hi:[1,0,1] neg_lo:[0,0,1] neg_hi:[0,0,1]\n"
    "v_pk_fma_f32 v[116:117], v[108:109], v[78:79], v[100:101] op_sel_hi:[1,0,1] neg_lo:[0,0,1] neg_hi:[0,0,1]\n"
    "v_pk_fma_f32 v[118:119], v[110:111], v[78:79], v[102:103] op_sel_hi:[1,0,1] neg_lo:[0,0,1] neg_hi:[0,0,1]\n"
    "v_cvt_pk_bf16_f32 v80, v80, v81\n"
    "v_cvt_pk_bf16_f32 v81, v82, v83\n"
    "v_cvt_pk_bf16_f32 v82, v84, v85\n"
    "s_add_u32 m0, s53, 83968\n"
    "s_add_u32 s54, s52, 839680\n"
    "buffer_load_dwordx4 v249, s[8:11], s54 offen lds\n"
    "v_cvt_pk_bf16_f32 v83, v86, v87\n"
    "v_cvt_pk_bf16_f32 v84, v88, v89\n"
    "v_cvt_pk_bf16_f32 v85, v90, v91\n"
    "v_cvt_pk_bf16_f32 v86, v92, v93\n"
    "v_cvt_pk_bf16_f32 v87, v94, v95\n"
    "v_cvt_pk_bf16_f32 v88, v96, v97\n"
    "v_cvt_pk_bf16_f32 v89, v98, v99\n"
    "v_cvt_pk_bf16_f32 v90, v100, v101\n"
    "v_cvt_pk_bf16_f32 v91, v102, v103\n"
    "v_cvt_pk_bf16_f32 v92, v104, v105\n"
    "v_cvt_pk_bf16_f32 v93, v106, v107\n"
    "v_cvt_pk_bf16_f32 v94, v108, v109\n"
    "v_cvt_pk_bf16_f32 v95, v110, v111\n"
    "v_cvt_pk_bf16_f32 v96, v112, v113\n"
    "v_cvt_pk_bf16_f32 v97, v114, v115\n"
    "v_cvt_pk_bf16_f32 v98, v116, v117\n"
    "v_cvt_pk_bf16_f32 v99, v118, v119\n"
    "v_cndmask_b32_e64 v80, v80, v71, s[36:37]\n"
    "s_add_u32 m0, s53, 84992\n"
    "s_add_u32 s54, s52, 849920\n"
    "buffer_load_dwordx4 v249, s[8:11], s54 offen lds\n"
    "v_accvgpr_write_b32 a128, v80\n"
    "v_accvgpr_write_b32 a129, v81\n"
    "v_accvgpr_write_b32 a130, v82\n"
    "v_accvgpr_write_b32 a131, v83\n"
    "v_accvgpr_write_b32 a132, v84\n"
    "v_accvgpr_write_b32 a133, v85\n"
    "v_accvgpr_write_b32 a134, v86\n"
    "v_accvgpr_write_b32 a135, v87\n"
    "v_accvgpr_write_b32 a136, v88\n"
    "v_accvgpr_write_b32 a137, v89\n"
    "v_accvgpr_write_b32 a138, v90\n"
    "v_accvgpr_write_b32 a139, v91\n"
    "v_accvgpr_write_b32 a140, v92\n"
    "v_accvgpr_write_b32 a141, v93\n"
    "v_accvgpr_write_b32 a142, v94\n"
    "v_accvgpr_write_b32 a143, v95\n"
    "v_accvgpr_write_b32 a144, v96\n"
    "v_accvgpr_write_b32 a145, v97\n"
    "s_add_u32 m0, s53, 98304\n"
    "s_add_u32 s54, s52, 983040\n"
    "buffer_load_dwordx4 v249, s[8:11], s54 offen lds\n"
    "v_accvgpr_write_b32 a146, v98\n"
    "v_accvgpr_write_b32 a147, v99\n"
    "s_add_u32 m0, s53, 99328\n"
    "s_add_u32 s54, s52, 993280\n"
    "buffer_load_dwordx4 v249, s[8:11], s54 offen lds\n"
    "s_add_u32 m0, s53, 100352\n"
    "s_add_u32 s54, s52, 1003520\n"
    "buffer_load_dwordx4 v249, s[8:11], s54 offen lds\n"
    "s_add_u32 m0, s53, 101376\n"
    "s_add_u32 s54, s52, 1013760\n"
    "buffer_load_dwordx4 v249, s[8:11], s54 offen lds\n"
    "s_add_u32 m0, s53, 114688\n"
    "s_add_u32 s54, s52, 1146880\n"
    "buffer_load_dwordx4 v249, s[8:11], s54 offen lds\n"
    "buffer_load_dwordx4 v[16:19], v244, s[4:7], s40 offen sc1\n"
    "s_add_u32 m0, s53, 115712\n"
    "s_add_u32 s54, s52, 1157120\n"
    "buffer_load_dwordx4 v249, s[8:11], s54 offen lds\n"
    "s_add_u32 m0, s53, 116736\n"
    "s_add_u32 s54, s52, 1167360\n"
    "buffer_load_dwordx4 v249, s[8:11], s54 offen lds\n"
    "s_add_u32 m0, s53, 117760\n"
    "s_add_u32 s54, s52, 1177600\n"
    "buffer_load_dwordx4 v249, s[8:11], s54 offen lds\n"
    "s_cmp_lg_u32 s3, 0\n"
    "s_cbranch_scc1 .Lf_warm\n"
    "s_waitcnt vmcnt(3)\n"
    "v_cmp_ne_u32 vcc, v16, v253\n"
    "s_nop 0\n"
    "v_cmp_ne_u32 s[58:59], v17, v253\n"
    "s_nop 0\n"
    "s_or_b64 vcc, vcc, s[58:59]\n"
    "v_cmp_ne_u32 s[58:59], v18, v253\n"
    "s_nop 0\n"
    "s_or_b64 vcc, vcc, s[58:59]\n"
    "v_cmp_ne_u32 s[58:59], v19, v253\n"
    "s_nop 0\n"
    "s_or_b64 vcc, vcc, s[58:59]\n"
    "s_and_b32 s59, vcc_hi, 0xffff\n"
    "s_or_b32 s58, vcc_lo, s59\n"
    "s_cbranch_scc1 .Lp1\n"
    ".Lpr1:\n"
    "s_branch .Lf_go\n"
    ".Lf_warm:\n"
    "s_call_b64 s[0:1], .Lwarm\n"
    ".Lf_go:\n"
    "s_add_u32 m0, s43, 131072\n"
    "s_add_u32 s34, s43, 20185088\n"
    ".Lpad2:\n"
    "s_cbranch_execz .Lpad6\n"
    "s_add_u32 s35, s42, 2621440\n"
    "s_waitcnt lgkmcnt(0)\n"
    "s_barrier\n"
    "buffer_load_dwordx4 v254, s[4:7], s42 offen offset:0 sc1 lds\n"
    "buffer_load_dwordx4 v254, s[4:7], s42 offen offset:1024 sc1 lds\n"
    "buffer_load_dwordx4 v254, s[4:7], s42 offen offset:2048 sc1 lds\n"
    "buffer_load_dwordx4 v254, s[4:7], s42 offen offset:3072 sc1 lds\n"
    "s_and_b32 s38, s32, 1\n"
    "s_lshl_b32 s38, s38, 1\n"
    "s_lshr_b32 s39, s32, 3\n"
    "s_add_u32 s38, s38, s39\n"
    "s_add_u32 s38, s38, 48\n"
    "s_mul_i32 s38, s38, 128\n"
    "s_bfe_u32 s39, s32, 0x20001\n"
    "s_lshl_b32 s39, s39, 2\n"
    "s_add_u32 s38, s38, s39\n"
    "v_min_u32 v244, 2, v255\n"
    "v_mul_u32_u24 v244, 512, v244\n"
    "v_add_u32 v244, s38, v244\n"
    "s_waitcnt vmcnt(4)\n"
    "ds_read_b64_tr_b16 a[0:1], v240 offset:0\n"
    "ds_read_b64_tr_b16 a[2:3], v240 offset:2048\n"
    "ds_read_b64_tr_b16 a[4:5], v241 offset:0\n"
    "ds_read_b64_tr_b16 a[6:7], v241 offset:2048\n"
    "ds_read_b64_tr_b16 a[8:9], v242 offset:0\n"
    "ds_read_b64_tr_b16 a[10:11], v242 offset:2048\n"
    "ds_read_b64_tr_b16 a[12:13], v243 offset:0\n"
    "ds_read_b64_tr_b16 a[14:15], v243 offset:2048\n"
    "ds_read_b64_tr_b16 a[16:17], v240 offset:16384\n"
    "ds_read_b64_tr_b16 a[18:19], v240 offset:18432\n"
    "ds_read_b64_tr_b16 a[20:21], v241 offset:16384\n"
    "ds_read_b64_tr_b16 a[22:23], v241 offset:18432\n"
    "ds_read_b64_tr_b16 a[24:25], v242 offset:16384\n"
    "ds_read_b64_tr_b16 a[26:27], v242 offset:18432\n"
    "ds_read_b64_tr_b16 a[28:29], v243 offset:16384\n"
    "ds_read_b64_tr_b16 a[30:31], v243 offset:18432\n"
    "ds_read_b64_tr_b16 a[32:33], v240 offset:32768\n"
    "ds_read_b64_tr_b16 a[34:35], v240 offset:34816\n"
    "ds_read_b64_tr_b16 a[36:37], v241 offset:32768\n"
    "ds_read_b64_tr_b16 a[38:39], v241 offset:34816\n"
    "ds_read_b64_tr_b16 a[40:41], v242 offset:32768\n"
    "ds_read_b64_tr_b16 a[42:43], v242 offset:34816\n"
    "ds_read_b64_tr_b16 a[44:45], v243 offset:32768\n"
    "ds_read_b64_tr_b16 a[46:47], v243 offset:34816\n"
    "ds_read_b64_tr_b16 a[48:49], v240 offset:49152\n"
    "ds_read_b64_tr_b16 a[50:51], v240 offset:51200\n"
    "ds_read_b64_tr_b16 a[52:53], v241 offset:49152\n"
    "ds_read_b64_tr_b16 a[54:55], v241 offset:51200\n"
    "ds_read_b64_tr_b16 a[56:57], v242 offset:49152\n"
    "ds_read_b64_tr_b16 a[58:59], v242 offset:51200\n"
    "ds_read_b64_tr_b16 a[60:61], v243 offset:49152\n"
    "ds_read_b64_tr_b16 a[62:63], v243 offset:51200\n"
    "v_add_u32 v240, 0x10000, v240\n"
    "v_add_u32 v241, 0x10000, v241\n"
    "v_add_u32 v242, 0x10000, v242\n"
    "v_add_u32 v243, 0x10000, v243\n"
    "ds_read_b64_tr_b16 a[64:65], v240 offset:0\n"
    "ds_read_b64_tr_b16 a[66:67], v240 offset:2048\n"
    "ds_read_b64_tr_b16 a[68:69], v241 offset:0\n"
    "ds_read_b64_tr_b16 a[70:71], v241 offset:2048\n"
    "ds_read_b64_tr_b16 a[72:73], v242 offset:0\n"
    "ds_read_b64_tr_b16 a[74:75], v242 offset:2048\n"
    "ds_read_b64_tr_b16 a[76:77], v243 offset:0\n"
    "ds_read_b64_tr_b16 a[78:79], v243 offset:2048\n"
    "ds_read_b64_tr_b16 a[80:81], v240 offset:16384\n"
    "ds_read_b64_tr_b16 a[82:83], v240 offset:18432\n"
    "ds_read_b64_tr_b16 a[84:85], v241 offset:16384\n"
    "ds_read_b64_tr_b16 a[86:87], v241 offset:18432\n"
    "ds_read_b64_tr_b16 a[88:89], v242 offset:16384\n"
    "ds_read_b64_tr_b16 a[90:91], v242 offset:18432\n"
    "ds_read_b64_tr_b16 a[92:93], v243 offset:16384\n"
    "ds_read_b64_tr_b16 a[94:95], v243 offset:18432\n"
    "ds_read_b64_tr_b16 a[96:97], v240 offset:32768\n"
    "ds_read_b64_tr_b16 a[98:99], v240 offset:34816\n"
    "ds_read_b64_tr_b16 a[100:101], v241 offset:32768\n"
    "ds_read_b64_tr_b16 a[102:103], v241 offset:34816\n"
    "ds_read_b64_tr_b16 a[104:105], v242 offset:32768\n"
    "ds_read_b64_tr_b16 a[106:107], v242 offset:34816\n"
    "ds_read_b64_tr_b16 a[108:109], v243 offset:32768\n"
    "ds_read_b64_tr_b16 a[110:111], v243 offset:34816\n"
    "ds_read_b64_tr_b16 a[112:113], v240 offset:49152\n"
    "ds_read_b64_tr_b16 a[114:115], v240 offset:51200\n"
    "ds_read_b64_tr_b16 a[116:117], v241 offset:49152\n"
    "ds_read_b64_tr_b16 a[118:119], v241 offset:51200\n"
    "ds_read_b64_tr_b16 a[120:121], v242 offset:49152\n"
    "ds_read_b64_tr_b16 a[122:123], v242 offset:51200\n"
    "ds_read_b64_tr_b16 a[124:125], v243 offset:49152\n"
    "ds_read_b64_tr_b16 a[126:127], v243 offset:51200\n"
    "buffer_load_dwordx4 v[48:51], v254, s[4:7], s35 offen offset:0 sc1\n"
    "buffer_load_dwordx4 v[52:55], v254, s[4:7], s35 offen offset:1024 sc1\n"
    "buffer_load_dwordx4 v[56:59], v254, s[4:7], s35 offen offset:2048 sc1\n"
    "buffer_load_dwordx4 v[60:63], v254, s[4:7], s35 offen offset:3072 sc1\n"
    "s_waitcnt vmcnt(4)\n"
    "buffer_load_dword v158, v244, s[4:7], s40 offen sc1\n"
    "s_memrealtime s[36:37]\n"
    "s_waitcnt lgkmcnt(0)\n"
    "v_mov_b32 v246, s36\n"
    "s_barrier\n"
    "ds_read_b128 a[160:163], v152 offset:0\n"
    "ds_read_b128 a[164:167], v152 offset:4096\n"
    "ds_read_b128 a[168:171], v152 offset:8192\n"
    "ds_read_b128 a[172:175], v152 offset:12288\n"
    "ds_read_b128 a[176:179], v152 offset:2048\n"
    "ds_read_b128 a[180:183], v152 offset:6144\n"
    "s_waitcnt lgkmcnt(5)\n"
    "v_mfma_f32_16x16x32_bf16 v[0:3], a[160:163], v[162:165], 0\n"
    "ds_read_b128 a[184:187], v152 offset:10240\n"
    "v_mfma_f32_16x16x32_bf16 v[4:7], a[160:163], v[160:163], 0\n"
    "s_waitcnt lgkmcnt(5)\n"
    "ds_read_b128 a[188:191], v152 offset:14336\n"
    "buffer_store_dword v246, v252, s[4:7], s41 offen sc1\n"
    "buffer_load_dwordx4 v[96:99], v149, s[4:7], s34 offen sc1\n"
    "v_mfma_f32_16x16x32_bf16 v[0:3], a[164:167], v[168:171], v[0:3]\n"
    "ds_read_b128 a[192:195], v153 offset:0\n"
    "v_mfma_f32_16x16x32_bf16 v[4:7], a[164:167], v[166:169], v[4:7]\n"
    "s_waitcnt lgkmcnt(6)\n"
    "ds_read_b128 a[196:199], v153 offset:4096\n"
    "v_mfma_f32_16x16x32_bf16 v[0:3], a[168:171], v[174:177], v[0:3]\n"
    "ds_read_b128 a[200:203], v153 offset:8192\n"
    "v_mfma_f32_16x16x32_bf16 v[4:7], a[168:171], v[172:175], v[4:7]\n"
    "s_waitcnt lgkmcnt(7)\n"
    "ds_read_b128 a[204:207], v153 offset:12288\n"
    "v_mfma_f32_16x16x32_bf16 v[0:3], a[172:175], v[180:183], v[0:3]\n"
    "ds_read_b128 a[208:211], v153 offset:2048\n"
    "v_mfma_f32_16x16x32_bf16 v[4:7], a[172:175], v[178:181], v[4:7]\n"
    "s_waitcnt lgkmcnt(8)\n"
    "ds_read_b128 a[212:215], v153 offset:6144\n"
    "v_mfma_f32_16x16x32_bf16 v[8:11], a[176:179], v[162:165], 0\n"
    "ds_read_b128 a[216:219], v153 offset:10240\n"
    "buffer_load_dwordx4 v[100:103], v149, s[4:7], s34 offen offset:2048 sc1\n"
    "buffer_load_dwordx4 v[104:107], v150, s[4:7], s34 offen offset:64 sc1\n"
    "buffer_load_dwordx4 v[108:111], v150, s[4:7], s34 offen offset:2112 sc1\n"
    "v_mfma_f32_16x16x32_bf16 v[12:15], a[176:179], v[160:163], 0\n"
    "s_waitcnt lgkmcnt(9)\n"
    "ds_read_b128 a[220:223], v153 offset:14336\n"
    "v_mfma_f32_16x16x32_bf16 v[8:11], a[180:183], v[168:171], v[8:11]\n"
    "ds_read_b128 v[64:67], v154 offset:0\n"
    "v_mfma_f32_16x16x32_bf16 v[12:15], a[180:183], v[166:169], v[12:15]\n"
    "s_waitcnt lgkmcnt(10)\n"
    "ds_read_b128 v[68:71], v154 offset:2048\n"
    "v_mfma_f32_16x16x32_bf16 v[8:11], a[184:187], v[174:177], v[8:11]\n"
    "ds_read_b128 v[72:75], v155 offset:0\n"
    "v_mfma_f32_16x16x32_bf16 v[12:15], a[184:187], v[172:175], v[12:15]\n"
    "s_waitcnt lgkmcnt(11)\n"
    "ds_read_b128 v[76:79], v155 offset:2048\n"
    "v_mfma_f32_16x16x32_bf16 v[8:11], a[188:191], v[180:183], v[8:11]\n"
    "v_mfma_f32_16x16x32_bf16 v[12:15], a[188:191], v[178:181], v[12:15]\n"
    "s_waitcnt lgkmcnt(11)\n"
    "v_mfma_f32_16x16x32_bf16 v[16:19], a[192:195], v[162:165], 0\n"
    "s_nop 0\n"
    "s_nop 0\n"
    "v_mfma_f32_16x16x32_bf16 v[20:23], a[192:195], v[160:163], 0\n"
    "s_nop 0\n"
    "s_nop 0\n"
    "s_waitcnt lgkmcnt(10)\n"
    "v_mfma_f32_16x16x32_bf16 v[16:19], a[196:199], v[168:171], v[16:19]\n"
    "s_waitcnt lgkmcnt(3)\n"
    "v_lshlrev_b32 v112, 16, v64\n"
    "v_and_b32 v113, 0xffff0000, v64\n"
    "v_mfma_f32_16x16x32_bf16 v[20:23], a[196:199], v[166:169], v[20:23]\n"
    "v_lshlrev_b32 v114, 16, v65\n"
    "v_and_b32 v115, 0xffff0000, v65\n"
    "v_mfma_f32_16x16x32_bf16 v[16:19], a[200:203], v[174:177], v[16:19]\n"
    "v_lshlrev_b32 v116, 16, v66\n"
    "v_and_b32 v117, 0xffff0000, v66\n"
    "v_mfma_f32_16x16x32_bf16 v[20:23], a[200:203], v[172:175], v[20:23]\n"
    "v_lshlrev_b32 v118, 16, v67\n"
    "v_and_b32 v119, 0xffff0000, v67\n"
    "v_mfma_f32_16x16x32_bf16 v[16:19], a[204:207], v[180:183], v[16:19]\n"
    "s_waitcnt lgkmcnt(2)\n"
    "v_lshlrev_b32 v120, 16, v68\n"
    "v_and_b32 v121, 0xffff0000, v68\n"
    "v_mfma_f32_16x16x32_bf16 v[20:23], a[204:207], v[178:181], v[20:23]\n"
    "v_lshlrev_b32 v122, 16, v69\n"
    "v_and_b32 v123, 0xffff0000, v69\n"
    "v_mfma_f32_16x16x32_bf16 v[24:27], a[208:211], v[162:165], 0\n"
    "v_lshlrev_b32 v124, 16, v70\n"
    "v_and_b32 v125, 0xffff0000, v70\n"
    "v_mfma_f32_16x16x32_bf16 v[28:31], a[208:211], v[160:163], 0\n"
    "v_lshlrev_b32 v126, 16, v71\n"
    "v_and_b32 v127, 0xffff0000, v71\n"
    "v_mfma_f32_16x16x32_bf16 v[24:27], a[212:215], v[168:171], v[24:27]\n"
    "s_waitcnt lgkmcnt(1)\n"
    "v_lshlrev_b32 v128, 16, v72\n"
    "v_and_b32 v129, 0xffff0000, v72\n"
    "v_mfma_f32_16x16x32_bf16 v[28:31], a[212:215], v[166:169], v[28:31]\n"
    "v_lshlrev_b32 v130, 16, v73\n"
    "v_and_b32 v131, 0xffff0000, v73\n"
    "v_mfma_f32_16x16x32_bf16 v[24:27], a[216:219], v[174:177], v[24:27]\n"
    "v_lshlrev_b32 v132, 16, v74\n"
    "v_and_b32 v133, 0xffff0000, v74\n"
    "v_mfma_f32_16x16x32_bf16 v[28:31], a[216:219], v[172:175], v[28:31]\n"
    "v_lshlrev_b32 v134, 16, v75\n"
    "v_and_b32 v135, 0xffff0000, v75\n"
    "v_mfma_f32_16x16x32_bf16 v[24:27], a[220:223], v[180:183], v[24:27]\n"
    "s_waitcnt lgkmcnt(0)\n"
    "v_lshlrev_b32 v136, 16, v76\n"
    "v_and_b32 v137, 0xffff0000, v76\n"
    "v_mfma_f32_16x16x32_bf16 v[28:31], a[220:223], v[178:181], v[28:31]\n"
    "v_lshlrev_b32 v138, 16, v77\n"
    "v_and_b32 v139, 0xffff0000, v77\n"
    "v_lshlrev_b32 v140, 16, v78\n"
    "v_and_b32 v141, 0xffff0000, v78\n"
    "v_lshlrev_b32 v142, 16, v79\n"
    "v_and_b32 v143, 0xffff0000, v79\n"
    "v_pk_mul_f32 v[144:145], v[4:5], v[216:217]\n"
    "v_pk_mul_f32 v[146:147], v[0:1], v[216:217]\n"
    "v_pk_mul_f32 v[148:149], v[6:7], v[218:219]\n"
    "v_pk_mul_f32 v[150:151], v[2:3], v[218:219]\n"
    "v_pk_fma_f32 v[0:1], v[0:1], v[208:209], v[144:145]\n"
    "v_pk_fma_f32 v[4:5], v[4:5], v[208:209], v[146:147] neg_lo:[0,0,1] neg_hi:[0,0,1]\n"
    "v_pk_fma_f32 v[2:3], v[2:3], v[210:211], v[148:149]\n"
    "v_pk_fma_f32 v[6:7], v[6:7], v[210:211], v[150:151] neg_lo:[0,0,1] neg_hi:[0,0,1]\n"
    "v_pk_mul_f32 v[144:145], v[12:13], v[220:221]\n"
    "v_pk_mul_f32 v[146:147], v[8:9], v[220:221]\n"
    "v_pk_mul_f32 v[148:149], v[14:15], v[222:223]\n"
    "v_pk_mul_f32 v[150:151], v[10:11], v[222:223]\n"
    "v_pk_fma_f32 v[8:9], v[8:9], v[212:213], v[144:145]\n"
    "v_pk_fma_f32 v[12:13], v[12:13], v[212:213], v[146:147] neg_lo:[0,0,1] neg_hi:[0,0,1]\n"
    "v_pk_fma_f32 v[10:11], v[10:11], v[214:215], v[148:149]\n"
    "v_pk_fma_f32 v[14:15], v[14:15], v[214:215], v[150:151] neg_lo:[0,0,1] neg_hi:[0,0,1]\n"
    "v_pk_mul_f32 v[144:145], v[20:21], v[216:217]\n"
    "v_pk_mul_f32 v[146:147], v[16:17], v[216:217]\n"
    "v_pk_mul_f32 v[148:149], v[22:23], v[218:219]\n"
    "v_pk_mul_f32 v[150:151], v[18:19], v[218:219]\n"
    "v_pk_fma_f32 v[16:17], v[16:17], v[208:209], v[144:145]\n"
    "v_pk_fma_f32 v[20:21], v[20:21], v[208:209], v[146:147] neg_lo:[0,0,1] neg_hi:[0,0,1]\n"
    "v_pk_fma_f32 v[18:19], v[18:19], v[210:211], v[148:149]\n"
    "v_pk_fma_f32 v[22:23], v[22:23], v[210:211], v[150:151] neg_lo:[0,0,1] neg_hi:[0,0,1]\n"
    "v_pk_mul_f32 v[144:145], v[28:29], v[220:221]\n"
    "v_pk_mul_f32 v[146:147], v[24:25], v[220:221]\n"
    "v_pk_mul_f32 v[148:149], v[30:31], v[222:223]\n"
    "v_pk_mul_f32 v[150:151], v[26:27], v[222:223]\n"
    "v_pk_fma_f32 v[24:25], v[24:25], v[212:213], v[144:145]\n"
    "v_pk_fma_f32 v[28:29], v[28:29], v[212:213], v[146:147] neg_lo:[0,0,1] neg_hi:[0,0,1]\n"
    "v_pk_fma_f32 v[26:27], v[26:27], v[214:215], v[148:149]\n"
    "v_pk_fma_f32 v[30:31], v[30:31], v[214:215], v[150:151] neg_lo:[0,0,1] neg_hi:[0,0,1]\n"
    "v_cvt_pk_bf16_f32 v32, v0, v1\n"
    "v_cvt_pk_bf16_f32 v33, v2, v3\n"
    "v_cvt_pk_bf16_f32 v34, v8, v9\n"
    "v_cvt_pk_bf16_f32 v35, v10, v11\n"
    "v_cvt_pk_bf16_f32 v36, v4, v5\n"
    "v_cvt_pk_bf16_f32 v37, v6, v7\n"
    "v_cvt_pk_bf16_f32 v38, v12, v13\n"
    "v_cvt_pk_bf16_f32 v39, v14, v15\n"
    "s_waitcnt vmcnt(5)\n"
    "v_cmp_ne_u32 vcc, v158, v253\n"
    "s_nop 0\n"
    "s_and_b32 s58, vcc_lo, 0x7\n"
    "s_cbranch_scc1 .Lp2\n"
    ".Lpr2:\n"
    "v_mfma_f32_16x16x32_bf16 v[0:3], v[184:187], v[32:35], 0\n"
    "v_cvt_pk_bf16_f32 v40, v16, v17\n"
    "v_cvt_pk_bf16_f32 v41, v18, v19\n"
    "v_mfma_f32_16x16x32_bf16 v[4:7], v[200:203], v[32:35], 0\n"
    "s_add_u32 s47, s16, 0\n"
    "v_cvt_pk_bf16_f32 v42, v24, v25\n"
    "v_cvt_pk_bf16_f32 v43, v26, v27\n"
    "v_mfma_f32_16x16x32_bf16 v[8:11], v[188:191], v[32:35], 0\n"
    "s_add_u32 m0, s12, 0\n"
    "s_nop 0\n"
    "buffer_load_dwordx4 v254, s[4:7], s47 offen offset:0 sc1 lds\n"
    "v_cvt_pk_bf16_f32 v44, v20, v21\n"
    "v_cvt_pk_bf16_f32 v45, v22, v23\n"
    "v_mfma_f32_16x16x32_bf16 v[12:15], v[204:207], v[32:35], 0\n"
    "v_cvt_pk_bf16_f32 v46, v28, v29\n"
    "v_cvt_pk_bf16_f32 v47, v30, v31\n"
    "v_mfma_f32_16x16x32_bf16 v[0:3], v[192:195], v[36:39], v[0:3]\n"
    "v_lshlrev_b32 v64, 16, v48\n"
    "v_and_b32 v68, 0xffff0000, v48\n"
    "v_mfma_f32_16x16x32_bf16 v[4:7], v[184:187], v[36:39], v[4:7]\n"
    "buffer_load_dwordx4 v254, s[4:7], s47 offen offset:1024 sc1 lds\n"
    "v_lshlrev_b32 v65, 16, v49\n"
    "v_and_b32 v69, 0xffff0000, v49\n"
    "v_mfma_f32_16x16x32_bf16 v[8:11], v[196:199], v[36:39], v[8:11]\n"
    "v_lshlrev_b32 v66, 16, v52\n"
    "v_and_b32 v70, 0xffff0000, v52\n"
    "v_mfma_f32_16x16x32_bf16 v[12:15], v[188:191], v[36:39], v[12:15]\n"
    "v_lshlrev_b32 v67, 16, v53\n"
    "v_and_b32 v71, 0xffff0000, v53\n"
    "v_mfma_f32_16x16x32_bf16 v[16:19], v[184:187], v[40:43], 0\n"
    "v_lshlrev_b32 v72, 16, v56\n"
    "v_and_b32 v76, 0xffff0000, v56\n"
    "v_mfma_f32_16x16x32_bf16 v[20:23], v[200:203], v[40:43], 0\n"
    "buffer_load_dwordx4 v254, s[4:7], s47 offen offset:2048 sc1 lds\n"
    "v_lshlrev_b32 v73, 16, v57\n"
    "v_and_b32 v77, 0xffff0000, v57\n"
    "v_mfma_f32_16x16x32_bf16 v[24:27], v[188:191], v[40:43], 0\n"
    "v_lshlrev_b32 v74, 16, v60\n"
    "v_and_b32 v78, 0xffff0000, v60\n"
    "v_mfma_f32_16x16x32_bf16 v[28:31], v[204:207], v[40:43], 0\n"
    "v_lshlrev_b32 v75, 16, v61\n"
    "v_and_b32 v79, 0xffff0000, v61\n"
    "v_mfma_f32_16x16x32_bf16 v[16:19], v[192:195], v[44:47], v[16:19]\n"
    "buffer_load_dwordx4 v254, s[4:7], s47 offen offset:3072 sc1 lds\n"
    "v_lshlrev_b32 v80, 16, v50\n"
    "v_and_b32 v84, 0xffff0000, v50\n"
    "v_mfma_f32_16x16x32_bf16 v[20:23], v[184:187], v[44:47], v[20:23]\n"
    "v_lshlrev_b32 v81, 16, v51\n"
    "v_and_b32 v85, 0xffff0000, v51\n"
    "v_mfma_f32_16x16x32_bf16 v[24:27], v[196:199], v[44:47], v[24:27]\n"
    "s_add_u32 s47, s16, 4096\n"
    "v_lshlrev_b32 v82, 16, v54\n"
    "v_and_b32 v86, 0xffff0000, v54\n"
    "v_mfma_f32_16x16x32_bf16 v[28:31], v[188:191], v[44:47], v[28:31]\n"
    "s_add_u32 m0, s12, 4096\n"
    "v_lshlrev_b32 v83, 16, v55\n"
    "v_and_b32 v87, 0xffff0000, v55\n"
    "v_lshlrev_b32 v88, 16, v58\n"
    "v_and_b32 v92, 0xffff0000, v58\n"
    "v_lshlrev_b32 v89, 16, v59\n"
    "v_and_b32 v93, 0xffff0000, v59\n"
    "v_lshlrev_b32 v90, 16, v62\n"
    "v_and_b32 v94, 0xffff0000, v62\n"
    "v_lshlrev_b32 v91, 16, v63\n"
    "v_and_b32 v95, 0xffff0000, v63\n"
    "buffer_load_dwordx4 v254, s[4:7], s47 offen offset:0 sc1 lds\n"
    "v_pk_mul_f32 v[144:145], v[4:5], v[68:69]\n"
    "v_pk_mul_f32 v[146:147], v[0:1], v[68:69]\n"
    "v_pk_mul_f32 v[148:149], v[6:7], v[70:71]\n"
    "v_pk_mul_f32 v[150:151], v[2:3], v[70:71]\n"
    "v_pk_fma_f32 v[0:1], v[0:1], v[64:65], v[144:145] neg_lo:[0,0,1] neg_hi:[0,0,1]\n"
    "v_pk_fma_f32 v[4:5], v[4:5], v[64:65], v[146:147]\n"
    "v_pk_fma_f32 v[2:3], v[2:3], v[66:67], v[148:149] neg_lo:[0,0,1] neg_hi:[0,0,1]\n"
    "v_pk_fma_f32 v[6:7], v[6:7], v[66:67], v[150:151]\n"
    "v_pk_mul_f32 v[144:145], v[12:13], v[76:77]\n"
    "v_pk_mul_f32 v[146:147], v[8:9], v[76:77]\n"
    "v_pk_mul_f32 v[148:149], v[14:15], v[78:79]\n"
    "buffer_load_dwordx4 v254, s[4:7], s47 offen offset:1024 sc1 lds\n"
    "v_pk_mul_f32 v[150:151], v[10:11], v[78:79]\n"
    "v_pk_fma_f32 v[8:9], v[8:9], v[72:73], v[144:145] neg_lo:[0,0,1] neg_hi:[0,0,1]\n"
    "v_pk_fma_f32 v[12:13], v[12:13], v[72:73], v[146:147]\n"
    "v_pk_fma_f32 v[10:11], v[10:11], v[74:75], v[148:149] neg_lo:[0,0,1] neg_hi:[0,0,1]\n"
    "v_pk_fma_f32 v[14:15], v[14:15], v[74:75], v[150:151]\n"
    "v_pk_mul_f32 v[144:145], v[20:21], v[84:85]\n"
    "v_pk_mul_f32 v[146:147], v[16:17], v[84:85]\n"
    "v_pk_mul_f32 v[148:149], v[22:23], v[86:87]\n"
    "v_pk_mul_f32 v[150:151], v[18:19], v[86:87]\n"
    "v_pk_fma_f32 v[16:17], v[16:17], v[80:81], v[144:145] neg_lo:[0,0,1] neg_hi:[0,0,1]\n"
    "buffer_load_dwordx4 v254, s[4:7], s47 offen offset:2048 sc1 lds\n"
    "v_pk_fma_f32 v[20:21], v[20:21], v[80:81], v[146:147]\n"
    "v_pk_fma_f32 v[18:19], v[18:19], v[82:83], v[148:149] neg_lo:[0,0,1] neg_hi:[0,0,1]\n"
    "v_pk_fma_f32 v[22:23], v[22:23], v[82:83], v[150:151]\n"
    "v_pk_mul_f32 v[144:145], v[28:29], v[92:93]\n"
    "v_pk_mul_f32 v[146:147], v[24:25], v[92:93]\n"
    "v_pk_mul_f32 v[148:149], v[30:31], v[94:95]\n"
    "v_pk_mul_f32 v[150:151], v[26:27], v[94:95]\n"
    "v_pk_fma_f32 v[24:25], v[24:25], v[88:89], v[144:145] neg_lo:[0,0,1] neg_hi:[0,0,1]\n"
    "v_pk_fma_f32 v[28:29], v[28:29], v[88:89], v[146:147]\n"
    "v_pk_fma_f32 v[26:27], v[26:27], v[90:91], v[148:149] neg_lo:[0,0,1] neg_hi:[0,0,1]\n"
    "v_pk_fma_f32 v[30:31], v[30:31], v[90:91], v[150:151]\n"
    "buffer_load_dwordx4 v254, s[4:7], s47 offen offset:3072 sc1 lds\n"
    "v_cvt_pk_bf16_f32 v32, v0, v1\n"
    "v_cvt_pk_bf16_f32 v33, v2, v3\n"
    "v_cvt_pk_bf16_f32 v34, v8, v9\n"
    "v_cvt_pk_bf16_f32 v35, v10, v11\n"
    "v_cvt_pk_bf16_f32 v36, v4, v5\n"
    "v_cvt_pk_bf16_f32 v37, v6, v7\n"
    "v_cvt_pk_bf16_f32 v38, v12, v13\n"
    "v_cvt_pk_bf16_f32 v39, v14, v15\n"
    "v_mfma_f32_16x16x32_bf16 v[0:3], v[32:35], v[184:187], 0\n"
    "v_cvt_pk_bf16_f32 v40, v16, v17\n"
    "v_cvt_pk_bf16_f32 v41, v18, v19\n"
    "v_mfma_f32_16x16x32_bf16 v[4:7], v[32:35], v[192:195], 0\n"
    "s_add_u32 s47, s16, 8192\n"
    "v_cvt_pk_bf16_f32 v42, v24, v25\n"
    "v_cvt_pk_bf16_f32 v43, v26, v27\n"
    "v_mfma_f32_16x16x32_bf16 v[8:11], v[32:35], v[188:191], 0\n"
    "s_add_u32 m0, s12, 8192\n"
    "s_nop 0\n"
    "buffer_load_dwordx4 v254, s[4:7], s47 offen offset:0 sc1 lds\n"
    "v_cvt_pk_bf16_f32 v44, v20, v21\n"
    "v_cvt_pk_bf16_f32 v45, v22, v23\n"
    "v_mfma_f32_16x16x32_bf16 v[12:15], v[32:35], v[196:199], 0\n"
    "v_cvt_pk_bf16_f32 v46, v28, v29\n"
    "v_cvt_pk_bf16_f32 v47, v30, v31\n"
    "v_mfma_f32_16x16x32_bf16 v[0:3], v[36:39], v[200:203], v[0:3]\n"
    "s_waitcnt vmcnt(12)\n"
    "v_mul_f32 v112, v112, v96\n"
    "v_mul_f32 v113, v113, v97\n"
    "v_mfma_f32_16x16x32_bf16 v[4:7], v[36:39], v[184:187], v[4:7]\n"
    "buffer_load_dwordx4 v254, s[4:7], s47 offen offset:1024 sc1 lds\n"
    "v_mul_f32 v114, v114, v98\n"
    "v_mul_f32 v115, v115, v99\n"
    "v_mfma_f32_16x16x32_bf16 v[8:11], v[36:39], v[204:207], v[8:11]\n"
    "v_mul_f32 v116, v116, v96\n"
    "v_mul_f32 v117, v117, v97\n"
    "v_mfma_f32_16x16x32_bf16 v[12:15], v[36:39], v[188:191], v[12:15]\n"
    "s_add_u32 s47, s17, 0\n"
    "v_mul_f32 v118, v118, v98\n"
    "v_mul_f32 v119, v119, v99\n"
    "v_mfma_f32_16x16x32_bf16 v[16:19], v[40:43], v[184:187], 0\n"
    "s_waitcnt vmcnt(12)\n"
    "v_mul_f32 v120, v120, v100\n"
    "v_mul_f32 v121, v121, v101\n"
    "v_mfma_f32_16x16x32_bf16 v[20:23], v[40:43], v[192:195], 0\n"
    "s_add_u32 m0, s13, 0\n"
    "s_nop 0\n"
    "buffer_load_dwordx4 v254, s[4:7], s47 offen offset:0 sc1 lds\n"
    "v_mul_f32 v122, v122, v102\n"
    "v_mul_f32 v123, v123, v103\n"
    "v_mfma_f32_16x16x32_bf16 v[24:27], v[40:43], v[188:191], 0\n"
    "v_mul_f32 v124, v124, v100\n"
    "v_mul_f32 v125, v125, v101\n"
    "v_mfma_f32_16x16x32_bf16 v[28:31], v[40:43], v[196:199], 0\n"
    "v_mul_f32 v126, v126, v102\n"
    "v_mul_f32 v127, v127, v103\n"
    "v_mfma_f32_16x16x32_bf16 v[16:19], v[44:47], v[200:203], v[16:19]\n"
    "buffer_load_dwordx4 v254, s[4:7], s47 offen offset:1024 sc1 lds\n"
    "v_mul_f32 v128, v128, v96\n"
    "v_mul_f32 v129, v129, v97\n"
    "v_mfma_f32_16x16x32_bf16 v[20:23], v[44:47], v[184:187], v[20:23]\n"
    "v_mul_f32 v130, v130, v98\n"
    "v_mul_f32 v131, v131, v99\n"
    "v_mfma_f32_16x16x32_bf16 v[24:27], v[44:47], v[204:207], v[24:27]\n"
    "v_mul_f32 v132, v132, v96\n"
    "v_mul_f32 v133, v133, v97\n"
    "v_mfma_f32_16x16x32_bf16 v[28:31], v[44:47], v[188:191], v[28:31]\n"
    "v_mul_f32 v134, v134, v98\n"
    "v_mul_f32 v135, v135, v99\n"
    "v_mul_f32 v136, v136, v100\n"
    "v_mul_f32 v137, v137, v101\n"
    "v_mul_f32 v138, v138, v102\n"
    "v_mul_f32 v139, v139, v103\n"
    "v_mul_f32 v140, v140, v100\n"
    "v_mul_f32 v141, v141, v101\n"
    "v_mul_f32 v142, v142, v102\n"
    "v_mul_f32 v143, v143, v103\n"
    "buffer_load_dwordx4 v254, s[4:7], s47 offen offset:2048 sc1 lds\n"
    "buffer_load_dwordx4 v254, s[4:7], s47 offen offset:3072 sc1 lds\n"
    "v_pk_mul_f32 v[144:145], v[4:5], v[232:233]\n"
    "v_pk_mul_f32 v[146:147], v[0:1], v[232:233]\n"
    "v_pk_mul_f32 v[148:149], v[6:7], v[234:235]\n"
    "v_pk_mul_f32 v[150:151], v[2:3], v[234:235]\n"
    "v_pk_fma_f32 v[0:1], v[0:1], v[224:225], v[144:145] neg_lo:[0,0,1] neg_hi:[0,0,1]\n"
    "v_pk_fma_f32 v[4:5], v[4:5], v[224:225], v[146:147]\n"
    "v_pk_fma_f32 v[2:3], v[2:3], v[226:227], v[148:149] neg_lo:[0,0,1] neg_hi:[0,0,1]\n"
    "v_pk_fma_f32 v[6:7], v[6:7], v[226:227], v[150:151]\n"
    "v_pk_mul_f32 v[144:145], v[20:21], v[232:233]\n"
    "v_pk_mul_f32 v[146:147], v[16:17], v[232:233]\n"
    "v_pk_mul_f32 v[148:149], v[22:23], v[234:235]\n"
    "v_pk_mul_f32 v[150:151], v[18:19], v[234:235]\n"
    "v_pk_fma_f32 v[16:17], v[16:17], v[224:225], v[144:145] neg_lo:[0,0,1] neg_hi:[0,0,1]\n"
    "v_pk_fma_f32 v[20:21], v[20:21], v[224:225], v[146:147]\n"
    "v_pk_fma_f32 v[18:19], v[18:19], v[226:227], v[148:149] neg_lo:[0,0,1] neg_hi:[0,0,1]\n"
    "v_pk_fma_f32 v[22:23], v[22:23], v[226:227], v[150:151]\n"
    "v_cvt_pk_bf16_f32 v32, v4, v5\n"
    "v_cvt_pk_bf16_f32 v33, v6, v7\n"
    "v_cvt_pk_bf16_f32 v34, v0, v1\n"
    "v_cvt_pk_bf16_f32 v35, v2, v3\n"
    "v_cvt_pk_bf16_f32 v40, v20, v21\n"
    "v_cvt_pk_bf16_f32 v41, v22, v23\n"
    "v_cvt_pk_bf16_f32 v42, v16, v17\n"
    "v_cvt_pk_bf16_f32 v43, v18, v19\n"
    "ds_write_b128 v156, v[32:35] offset:0\n"
    "ds_write_b128 v156, v[40:43] offset:8192\n"
    "v_pk_mul_f32 v[144:145], v[12:13], v[236:237]\n"
    "v_pk_mul_f32 v[146:147], v[8:9], v[236:237]\n"
    "v_pk_mul_f32 v[148:149], v[14:15], v[238:239]\n"
    "v_pk_mul_f32 v[150:151], v[10:11], v[238:239]\n"
    "v_pk_fma_f32 v[8:9], v[8:9], v[228:229], v[144:145] neg_lo:[0,0,1] neg_hi:[0,0,1]\n"
    "v_pk_fma_f32 v[12:13], v[12:13], v[228:229], v[146:147]\n"
    "v_pk_fma_f32 v[10:11], v[10:11], v[230:231], v[148:149] neg_lo:[0,0,1] neg_hi:[0,0,1]\n"
    "v_pk_fma_f32 v[14:15], v[14:15], v[230:231], v[150:151]\n"
    "v_pk_mul_f32 v[144:145], v[28:29], v[236:237]\n"
    "v_pk_mul_f32 v[146:147], v[24:25], v[236:237]\n"
    "v_pk_mul_f32 v[148:149], v[30:31], v[238:239]\n"
    "v_pk_mul_f32 v[150:151], v[26:27], v[238:239]\n"
    "v_pk_fma_f32 v[24:25], v[24:25], v[228:229], v[144:145] neg_lo:[0,0,1] neg_hi:[0,0,1]\n"
    "v_pk_fma_f32 v[28:29], v[28:29], v[228:229], v[146:147]\n"
    "v_pk_fma_f32 v[26:27], v[26:27], v[230:231], v[148:149] neg_lo:[0,0,1] neg_hi:[0,0,1]\n"
    "v_pk_fma_f32 v[30:31], v[30:31], v[230:231], v[150:151]\n"
    "v_cvt_pk_bf16_f32 v36, v12, v13\n"
    "v_cvt_pk_bf16_f32 v37, v14, v15\n"
    "v_cvt_pk_bf16_f32 v38, v8, v9\n"
    "v_cvt_pk_bf16_f32 v39, v10, v11\n"
    "v_cvt_pk_bf16_f32 v44, v28, v29\n"
    "v_cvt_pk_bf16_f32 v45, v30, v31\n"
    "v_cvt_pk_bf16_f32 v46, v24, v25\n"
    "v_cvt_pk_bf16_f32 v47, v26, v27\n"
    "s_waitcnt lgkmcnt(0)\n"
    "s_barrier\n"
    "ds_read_b128 a[160:163], v157 offset:0\n"
    "ds_read_b128 a[164:167], v157 offset:1024\n"
    "ds_read_b128 a[168:171], v157 offset:2048\n"
    "ds_read_b128 a[172:175], v157 offset:3072\n"
    "ds_write_b128 v156, v[36:39] offset:4096\n"
    "ds_write_b128 v156, v[44:47] offset:12288\n"
    "ds_read_b128 a[192:195], v157 offset:8192\n"
    "ds_read_b128 a[196:199], v157 offset:9216\n"
    "ds_read_b128 a[200:203], v157 offset:10240\n"
    "ds_read_b128 a[204:207], v157 offset:11264\n"
    "s_add_u32 m0, s13, 4096\n"
    "s_add_u32 s47, s17, 4096\n"
    "buffer_load_dwordx4 v254, s[4:7], s47 offen offset:0 sc1 lds\n"
    "s_waitcnt lgkmcnt(9)\n"
    "v_mfma_f32_16x16x32_bf16 v[116:119], v[162:165], a[160:163], v[116:119]\n"
    "v_mfma_f32_16x16x32_bf16 v[112:115], v[160:163], a[160:163], v[112:115]\n"
    "s_waitcnt lgkmcnt(8)\n"
    "v_mfma_f32_16x16x32_bf16 v[116:119], v[168:171], a[164:167], v[116:119]\n"
    "v_mfma_f32_16x16x32_bf16 v[112:115], v[166:169], a[164:167], v[112:115]\n"
    "s_waitcnt lgkmcnt(7)\n"
    "buffer_load_dwordx4 v254, s[4:7], s47 offen offset:1024 sc1 lds\n"
    "v_mfma_f32_16x16x32_bf16 v[116:119], v[174:177], a[168:171], v[116:119]\n"
    "v_mfma_f32_16x16x32_bf16 v[112:115], v[172:175], a[168:171], v[112:115]\n"
    "s_waitcnt lgkmcnt(4)\n"
    "s_barrier\n"
    "v_mfma_f32_16x16x32_bf16 v[116:119], v[180:183], a[172:175], v[116:119]\n"
    "ds_read_b128 a[176:179], v157 offset:4096\n"
    "v_mfma_f32_16x16x32_bf16 v[112:115], v[178:181], a[172:175], v[112:115]\n"
    "s_waitcnt lgkmcnt(4)\n"
    "ds_read_b128 a[180:183], v157 offset:5120\n"
    "buffer_load_dwordx4 v254, s[4:7], s47 offen offset:2048 sc1 lds\n"
    "v_mfma_f32_16x16x32_bf16 v[132:135], v[162:165], a[192:195], v[132:135]\n"
    "ds_read_b128 a[184:187], v157 offset:6144\n"
    "v_mfma_f32_16x16x32_bf16 v[128:131], v[160:163], a[192:195], v[128:131]\n"
    "s_waitcnt lgkmcnt(5)\n"
    "ds_read_b128 a[188:191], v157 offset:7168\n"
    "v_mfma_f32_16x16x32_bf16 v[132:135], v[168:171], a[196:199], v[132:135]\n"
    "ds_read_b128 a[208:211], v157 offset:12288\n"
    "v_mfma_f32_16x16x32_bf16 v[128:131], v[166:169], a[196:199], v[128:131]\n"
    "s_waitcnt lgkmcnt(6)\n"
    "ds_read_b128 a[212:215], v157 offset:13312\n"
    "v_mfma_f32_16x16x32_bf16 v[132:135], v[174:177], a[200:203], v[132:135]\n"
    "ds_read_b128 a[216:219], v157 offset:14336\n"
    "buffer_load_dwordx4 v254, s[4:7], s47 offen offset:3072 sc1 lds\n"
    "v_mfma_f32_16x16x32_bf16 v[128:131], v[172:175], a[200:203], v[128:131]\n"
    "s_waitcnt lgkmcnt(7)\n"
    "ds_read_b128 a[220:223], v157 offset:15360\n"
    "v_mfma_f32_16x16x32_bf16 v[132:135], v[180:183], a[204:207], v[132:135]\n"
    "s_add_u32 s0, s40, 65536\n"
    "v_min_u32 v220, 4992, v248\n"
    "v_lshl_or_b32 v220, s3, 28, v220\n"
    "v_mfma_f32_16x16x32_bf16 v[128:131], v[178:181], a[204:207], v[128:131]\n"
    "s_waitcnt lgkmcnt(7)\n"
    "buffer_load_dwordx4 v[216:219], v220, s[4:7], s0 offen sc1\n"
    "v_mfma_f32_16x16x32_bf16 v[124:127], v[162:165], a[176:179], v[124:127]\n"
    "s_waitcnt vmcnt(20)\n"
    "v_cndmask_b32_e64 v116, v116, v104, s[48:49]\n"
    "v_cndmask_b32_e64 v117, v117, v105, s[48:49]\n"
    "v_mfma_f32_16x16x32_bf16 v[120:123], v[160:163], a[176:179], v[120:123]\n"
    "s_add_u32 m0, s13, 8192\n"
    "s_add_u32 s47, s17, 8192\n"
    "buffer_load_dwordx4 v254, s[4:7], s47 offen offset:0 sc1 lds\n"
    "v_cndmask_b32_e64 v118, v118, v106, s[48:49]\n"
    "v_cndmask_b32_e64 v119, v119, v107, s[48:49]\n"
    "s_waitcnt lgkmcnt(6)\n"
    "v_mfma_f32_16x16x32_bf16 v[124:127], v[168:171], a[180:183], v[124:127]\n"
    "v_cvt_pk_bf16_f32 v0, v112, v116\n"
    "v_cvt_pk_bf16_f32 v2, v113, v117\n"
    "v_mfma_f32_16x16x32_bf16 v[120:123], v[166:169], a[180:183], v[120:123]\n"
    "s_waitcnt lgkmcnt(5)\n"
    "v_cvt_pk_bf16_f32 v1, v128, v132\n"
    "v_cvt_pk_bf16_f32 v3, v129, v133\n"
    "v_mfma_f32_16x16x32_bf16 v[124:127], v[174:177], a[184:187], v[124:127]\n"
    "buffer_store_dwordx4 v[0:3], v254, s[4:7], s42 offen offset:0 sc1\n"
    "v_cvt_pk_bf16_f32 v4, v114, v118\n"
    "v_cvt_pk_bf16_f32 v6, v115, v119\n"
    "v_mfma_f32_16x16x32_bf16 v[120:123], v[172:175], a[184:187], v[120:123]\n"
    "s_waitcnt lgkmcnt(4)\n"
    "v_cvt_pk_bf16_f32 v5, v130, v134\n"
    "v_cvt_pk_bf16_f32 v7, v131, v135\n"
    "v_mfma_f32_16x16x32_bf16 v[124:127], v[180:183], a[188:191], v[124:127]\n"
    "buffer_store_dwordx4 v[4:7], v254, s[4:7], s42 offen offset:1024 sc1\n"
    "v_mfma_f32_16x16x32_bf16 v[120:123], v[178:181], a[188:191], v[120:123]\n"
    "s_waitcnt lgkmcnt(3)\n"
    "v_mfma_f32_16x16x32_bf16 v[140:143], v[162:165], a[208:211], v[140:143]\n"
    "v_mfma_f32_16x16x32_bf16 v[136:139], v[160:163], a[208:211], v[136:139]\n"
    "s_waitcnt lgkmcnt(2)\n"
    "v_mfma_f32_16x16x32_bf16 v[140:143], v[168:171], a[212:215], v[140:143]\n"
    "v_mfma_f32_16x16x32_bf16 v[136:139], v[166:169], a[212:215], v[136:139]\n"
    "s_waitcnt lgkmcnt(1)\n"
    "v_mfma_f32_16x16x32_bf16 v[140:143], v[174:177], a[216:219], v[140:143]\n"
    "v_mfma_f32_16x16x32_bf16 v[136:139], v[172:175], a[216:219], v[136:139]\n"
    "s_waitcnt lgkmcnt(0)\n"
    "v_mfma_f32_16x16x32_bf16 v[140:143], v[180:183], a[220:223], v[140:143]\n"
    "v_mfma_f32_16x16x32_bf16 v[136:139], v[178:181], a[220:223], v[136:139]\n"
    "s_waitcnt vmcnt(22)\n"
    "v_cndmask_b32_e64 v124, v124, v108, s[48:49]\n"
    ".Lpad3:\n"
    "s_cbranch_execz .Lpad7\n"
    "v_cndmask_b32_e64 v125, v125, v109, s[48:49]\n"
    "v_cndmask_b32_e64 v126, v126, v110, s[48:49]\n"
    "v_cndmask_b32_e64 v127, v127, v111, s[48:49]\n"
    "v_cvt_pk_bf16_f32 v8, v120, v124\n"
    "v_cvt_pk_bf16_f32 v10, v121, v125\n"
    "s_nop 0\n"
    "v_cvt_pk_bf16_f32 v9, v136, v140\n"
    "v_cvt_pk_bf16_f32 v11, v137, v141\n"
    "buffer_store_dwordx4 v[8:11], v254, s[4:7], s42 offen offset:2048 sc1\n"
    "v_cvt_pk_bf16_f32 v12, v122, v126\n"
    "v_cvt_pk_bf16_f32 v14, v123, v127\n"
    "v_cvt_pk_bf16_f32 v13, v138, v142\n"
    "v_cvt_pk_bf16_f32 v15, v139, v143\n"
    "buffer_store_dwordx4 v[12:15], v254, s[4:7], s42 offen offset:3072 sc1\n"
    "buffer_load_dwordx4 v254, s[4:7], s47 offen offset:1024 sc1 lds\n"
    "s_add_u32 m0, s14, 0\n"
    "s_add_u32 s47, s18, 0\n"
    "buffer_load_dwordx4 v254, s[4:7], s47 offen sc1 lds\n"
    "s_add_u32 m0, s14, 4096\n"
    "s_add_u32 s47, s18, 4096\n"
    "buffer_load_dwordx4 v254, s[4:7], s47 offen sc1 lds\n"
    "s_add_u32 m0, s14, 8192\n"
    "s_add_u32 s47, s18, 8192\n"
    "buffer_load_dwordx4 v254, s[4:7], s47 offen sc1 lds\n"
    "s_add_u32 m0, s14, 12288\n"
    "s_add_u32 s47, s18, 12288\n"
    "buffer_load_dwordx4 v254, s[4:7], s47 offen sc1 lds\n"
    ".Lgemm1:\n"
    ".Lpad4:\n"
    "s_cbranch_execz .Lpad8\n"
    "s_mov_b32 s12, s40\n"
    "v_max_u32 v231, 7680, v248\n"
    "v_lshlrev_b32 v244, 15, v250\n"
    "v_lshl_add_u32 v244, v251, 4, v244\n"
    "s_lshl_b32 s40, s31, 10\n"
    "s_lshl_b32 s34, s3, 8\n"
    "s_add_u32 s40, s40, s34\n"
    "s_add_u32 s40, s40, 0\n"
    "s_lshl_b32 s48, s30, 16\n"
    "s_lshl_b32 s34, s3, 10\n"
    "s_add_u32 s48, s48, s34\n"
    "s_add_u32 s48, s48, 20463616\n"
    "v_mov_b32 v235, 0xffff\n"
    "v_cmp_gt_u32 vcc, 16, v255\n"
    "s_nop 1\n"
    "v_cndmask_b32 v235, -1, v235, vcc\n"
    "v_mov_b32 v238, 0\n"
    "v_mov_b32 v239, 0\n"
    "v_mov_b32 v236, 0\n"
    "v_mov_b32 v237, 0\n"
    "v_add_u32 v245, 81920, v254\n"
    "v_add_u32 v247, 40960, v254\n"
    "s_lshl_b32 s34, s31, 2\n"
    "s_add_u32 s34, s34, s3\n"
    "s_mul_i32 s42, s34, 110592\n"
    "s_add_u32 s42, s42, 13107200\n"
    "s_lshl_b32 s35, s32, 10\n"
    "s_add_u32 s43, s42, s35\n"
    "s_mul_i32 s44, s34, 40960\n"
    "s_lshr_b32 s35, s34, 1\n"
    "s_lshl_b32 s45, s35, 14\n"
    "s_and_b32 s35, s32, 3\n"
    "s_lshl_b32 s35, s35, 2\n"
    "s_lshr_b32 s36, s32, 2\n"
    "s_add_u32 s35, s35, s36\n"
    "s_lshl_b32 s35, s35, 1\n"
    "s_and_b32 s36, s3, 1\n"
    "s_add_u32 s35, s35, s36\n"
    "s_lshl_b32 s35, s35, 7\n"
    "s_add_u32 s45, s45, s35\n"
    "s_add_u32 s45, s45, 21020672\n"
    "v_not_b32 v222, v253\n"
    "v_not_b32 v223, v253\n"
    "v_lshlrev_b32 v224, 3, v251\n"
    "v_lshl_add_u32 v224, v250, 12, v224\n"
    "buffer_store_dwordx2 v[222:223], v224, s[4:7], s45 offen sc1\n"
    "s_lshl_b32 s39, s33, 14\n"
    "s_add_u32 s39, s39, 21032960\n"
    "s_lshl_b32 s35, s3, 10\n"
    "s_add_u32 s46, s39, s35\n"
    "s_sub_u32 s46, s46, 12288\n"
    "s_and_b32 s34, s28, 1\n"
    "s_lshr_b32 s35, s29, 2\n"
    "s_mul_i32 s36, s34, 5\n"
    "s_add_u32 s36, s36, s35\n"
    "s_lshl_b32 s36, s36, 10\n"
    "s_add_u32 s44, s44, s36\n"
    "s_add_u32 s44, s44, 10485760\n"
    "s_mul_i32 s36, s34, 3\n"
    "s_add_u32 s36, s36, s35\n"
    "s_add_u32 s36, s36, 43\n"
    "s_lshl_b32 s36, s36, 10\n"
    "s_add_u32 s42, s42, s36\n"
    "s_lshl_b32 s48, s30, 16\n"
    "s_lshl_b32 s34, s3, 10\n"
    "s_add_u32 s48, s48, s34\n"
    "s_add_u32 s48, s48, 20463616\n"
    "s_mul_i32 s37, s33, 221184\n"
    "s_add_u32 s37, s37, s34\n"
    "s_add_u32 s37, s37, 13107200\n"
    "v_and_b32 v248, 7, v255\n"
    "v_lshlrev_b32 v248, 7, v248\n"
    "s_lshl_b32 s49, s29, 2\n"
    "s_add_u32 s49, s49, s3\n"
    "s_mul_i32 s49, s49, 7680\n"
    "s_add_u32 s49, s49, 1310720\n"
    "s_and_b32 s34, s28, 1\n"
    "s_lshl_b32 s34, s34, 9\n"
    "s_add_u32 s49, s49, s34\n"
    "v_lshrrev_b32 v232, 2, v255\n"
    "v_min_u32 v232, 5, v232\n"
    "v_mul_u32_u24 v232, 0x500, v232\n"
    "v_and_b32 v233, 3, v255\n"
    "v_lshl_add_u32 v234, v233, 7, v232\n"
    "s_add_u32 s34, s3, 0\n"
    "s_and_b32 s34, s34, 3\n"
    "s_mul_i32 s34, s34, 55296\n"
    "v_add_u32 v240, s34, v254\n"
    "s_add_u32 s34, s3, 1\n"
    "s_and_b32 s34, s34, 3\n"
    "s_mul_i32 s34, s34, 55296\n"
    "v_add_u32 v241, s34, v254\n"
    "s_add_u32 s34, s3, 2\n"
    "s_and_b32 s34, s34, 3\n"
    "s_mul_i32 s34, s34, 55296\n"
    "v_add_u32 v242, s34, v254\n"
    "s_add_u32 s34, s3, 3\n"
    "s_and_b32 s34, s34, 3\n"
    "s_mul_i32 s34, s34, 55296\n"
    "v_add_u32 v243, s34, v254\n"
    "s_mul_i32 s52, s3, 40960\n"
    "s_lshl_b32 s54, s30, 7\n"
    "s_add_u32 s52, s52, s54\n"
    "s_mul_i32 s53, s3, 20480\n"
    "s_lshl_b32 s1, s2, 7\n"
    "s_add_u32 s1, s1, 20398080\n"
    "s_mul_i32 s34, s3, 10240\n"
    "s_add_u32 s34, s34, 122880\n"
    "s_cmp_ge_u32 s29, 20\n"
    "s_cselect_b32 s34, 0x28000, s34\n"
    "v_add_u32 v222, s34, v254\n"
    "s_and_b32 s34, s28, 1\n"
    "s_mul_i32 s34, s34, 5\n"
    "s_lshr_b32 s35, s29, 2\n"
    "s_add_u32 s34, s34, s35\n"
    "s_lshl_b32 s34, s34, 5\n"
    "v_lshrrev_b32 v160, 2, v251\n"
    "v_and_b32 v161, 3, v255\n"
    "v_lshl_add_u32 v160, v160, 3, v161\n"
    "v_add_u32 v160, s34, v160\n"
    "v_cvt_f32_u32 v161, v160\n"
    "s_mul_i32 s34, s3, 10\n"
    "s_cmp_ge_u32 s3, 2\n"
    "s_cselect_b32 s35, 19, 0\n"
    "s_sub_u32 s34, s34, s35\n"
    "v_cmp_gt_u32 vcc, 32, v255\n"
    "v_mul_u32_u24 v162, 0xa0, v250\n"
    "v_add_u32 v163, s34, v162\n"
    "v_sub_u32 v164, 640, v163\n"
    "v_subrev_u32 v165, 300, v162\n"
    "v_cndmask_b32 v163, v164, v163, vcc\n"
    "v_cndmask_b32 v165, v165, v162, vcc\n"
    "v_cvt_f32_u32 v163, v163\n"
    "v_cvt_f32_u32 v165, v165\n"
    "v_cndmask_b32_e64 v164, -1.0, 1.0, vcc\n"
    "s_cmp_eq_u32 s3, 0\n"
    "s_cselect_b64 s[16:17], -1, 0\n"
    "v_cndmask_b32_e64 v165, v163, v165, s[16:17]\n"
    "v_cndmask_b32_e64 v166, v164, 1.0, s[16:17]\n"
    "v_cmp_gt_u32 vcc, 16, v255\n"
    "v_and_b32 v167, 1, v255\n"
    "s_and_b64 s[16:17], s[16:17], vcc\n"
    "v_lshlrev_b32 v167, 15, v167\n"
    "v_or_b32 v167, 0x3f353f35, v167\n"
    "v_add_f32 v161, 4.0, v161\n"
    "v_mul_f32 v169, 0x3acccccd, v161\n"
    "v_mul_f32 v170, v169, v163\n"
    "v_mul_f32 v171, v169, v164\n"
    "v_mul_f32 v172, v169, v165\n"
    "v_mul_f32 v173, v169, v166\n"
    "v_add_f32 v174, v169, v169\n"
    "v_cos_f32 v174, v174\n"
    "s_waitcnt vmcnt(11)\n"
    "v_mov_b32 v221, v216\n"
    "s_cmp_lg_u32 s3, 0\n"
    "s_cbranch_scc1 .Lhd_f\n"
    "s_mov_b32 s57, 64\n"
    ".Lh_f:\n"
    "s_memrealtime s[24:25]\n"
    "s_waitcnt lgkmcnt(0)\n"
    "v_sub_u32 v216, s24, v216\n"
    "v_sub_u32 v217, s24, v217\n"
    "v_sub_u32 v218, s24, v218\n"
    "v_sub_u32 v219, s24, v219\n"
    "v_max3_u32 v221, v216, v217, v218\n"
    "v_max_u32 v221, v221, v219\n"
    "v_min3_u32 v216, v216, v217, v218\n"
    "v_min_u32 v216, v216, v219\n"
    "v_cmp_lt_u32 vcc, 1000, v221\n"
    "s_nop 0\n"
    "s_cbranch_vccnz .Lhr_f\n"
    "s_mov_b32 s57, 4096\n"
    ".Lhs_f:\n"
    "v_cmp_gt_i32 vcc, 166, v216\n"
    "s_nop 0\n"
    "s_cbranch_vccz .Lhd_f\n"
    "s_memrealtime s[26:27]\n"
    "s_waitcnt lgkmcnt(0)\n"
    "s_sub_u32 s35, s26, s24\n"
    "s_mov_b32 s24, s26\n"
    "v_add_u32 v216, s35, v216\n"
    "s_sub_u32 s57, s57, 1\n"
    "s_cbranch_scc0 .Lhs_f\n"
    ".Lhd_f:\n"
    "s_barrier\n"
    "s_add_u32 s41, s40, 0\n"
    "buffer_load_dwordx4 v[0:3], v244, s[4:7], s41 offen sc1\n"
    "s_add_u32 s41, s40, 16384\n"
    "buffer_load_dwordx4 v[4:7], v244, s[4:7], s41 offen sc1\n"
    "s_add_u32 s41, s40, 131072\n"
    "buffer_load_dwordx4 v[8:11], v244, s[4:7], s41 offen sc1\n"
    "s_add_u32 s41, s40, 147456\n"
    "buffer_load_dwordx4 v[12:15], v244, s[4:7], s41 offen sc1\n"
    "s_add_u32 s41, s40, 262144\n"
    "buffer_load_dwordx4 v[16:19], v244, s[4:7], s41 offen sc1\n"
    "v_sin_f32_e64 v177, -v172\n"
    "v_cos_f32 v176, v172\n"
    "v_fmamk_f32 v178, v173, 0x42200000, v172\n"
    "v_sin_f32_e64 v179, -v178\n"
    "v_cos_f32 v178, v178\n"
    "v_fmamk_f32 v180, v173, 0x42a00000, v172\n"
    "v_sin_f32_e64 v181, -v180\n"
    "v_cos_f32 v180, v180\n"
    "s_add_u32 s41, s40, 278528\n"
    "buffer_load_dwordx4 v[20:23], v244, s[4:7], s41 offen sc1\n"
    "v_fmamk_f32 v182, v173, 0x42f00000, v172\n"
    "v_sin_f32_e64 v183, -v182\n"
    "v_cos_f32 v182, v182\n"
    "v_fmamk_f32 v184, v171, 0x40000000, v170\n"
    "v_sin_f32_e64 v185, -v184\n"
    "v_cos_f32 v184, v184\n"
    "v_fmamk_f32 v186, v171, 0x42280000, v170\n"
    "s_add_u32 s41, s40, 393216\n"
    "buffer_load_dwordx4 v[24:27], v244, s[4:7], s41 offen sc1\n"
    "v_sin_f32_e64 v187, -v186\n"
    "v_cos_f32 v186, v186\n"
    "v_fmamk_f32 v188, v171, 0x42a40000, v170\n"
    "v_sin_f32_e64 v189, -v188\n"
    "v_cos_f32 v188, v188\n"
    "v_fmamk_f32 v190, v171, 0x42f40000, v170\n"
    "v_sin_f32_e64 v191, -v190\n"
    "v_cos_f32 v190, v190\n"
    "s_add_u32 s41, s40, 409600\n"
    "buffer_load_dwordx4 v[28:31], v244, s[4:7], s41 offen sc1\n"
    "v_fmamk_f32 v192, v171, 0x40800000, v170\n"
    "v_sin_f32_e64 v193, -v192\n"
    "v_cos_f32 v192, v192\n"
    "v_fmamk_f32 v194, v171, 0x42300000, v170\n"
    "v_sin_f32_e64 v195, -v194\n"
    "v_cos_f32 v194, v194\n"
    "v_fmamk_f32 v196, v171, 0x42a80000, v170\n"
    "s_add_u32 s41, s40, 524288\n"
    "buffer_load_dwordx4 v[32:35], v244, s[4:7], s41 offen sc1\n"
    "v_sin_f32_e64 v197, -v196\n"
    "v_cos_f32 v196, v196\n"
    "v_fmamk_f32 v198, v171, 0x42f80000, v170\n"
    "v_sin_f32_e64 v199, -v198\n"
    "v_cos_f32 v198, v198\n"
    "v_add_f32 v174, v174, v174\n"
    "v_pk_fma_f32 v[200:201], v[192:193], v[174:175], v[184:185] op_sel_hi:[1,0,1] neg_lo:[0,0,1] neg_hi:[0,0,1]\n"
    "v_pk_fma_f32 v[202:203], v[194:195], v[174:175], v[186:187] op_sel_hi:[1,0,1] neg_lo:[0,0,1] neg_hi:[0,0,1]\n"
    "v_pk_fma_f32 v[204:205], v[196:197], v[174:175], v[188:189] op_sel_hi:[1,0,1] neg_lo:[0,0,1] neg_hi:[0,0,1]\n"
    "s_add_u32 s41, s40, 540672\n"
    "buffer_load_dwordx4 v[36:39], v244, s[4:7], s41 offen sc1\n"
    "v_pk_fma_f32 v[206:207], v[198:199], v[174:175], v[190:191] op_sel_hi:[1,0,1] neg_lo:[0,0,1] neg_hi:[0,0,1]\n"
    "v_pk_fma_f32 v[208:209], v[200:201], v[174:175], v[192:193] op_sel_hi:[1,0,1] neg_lo:[0,0,1] neg_hi:[0,0,1]\n"
    "v_pk_fma_f32 v[210:211], v[202:203], v[174:175], v[194:195] op_sel_hi:[1,0,1] neg_lo:[0,0,1] neg_hi:[0,0,1]\n"
    "v_pk_fma_f32 v[212:213], v[204:205], v[174:175], v[196:197] op_sel_hi:[1,0,1] neg_lo:[0,0,1] neg_hi:[0,0,1]\n"
    "v_pk_fma_f32 v[214:215], v[206:207], v[174:175], v[198:199] op_sel_hi:[1,0,1] neg_lo:[0,0,1] neg_hi:[0,0,1]\n"
    "s_nop 0\n"
    "v_cvt_pk_bf16_f32 v215, v214, v215\n"
    "v_cvt_pk_bf16_f32 v214, v212, v213\n"
    "v_cvt_pk_bf16_f32 v213, v210, v211\n"
    "v_cvt_pk_bf16_f32 v212, v208, v209\n"
    "v_cvt_pk_bf16_f32 v211, v206, v207\n"
    "v_cvt_pk_bf16_f32 v210, v204, v205\n"
    "v_cvt_pk_bf16_f32 v209, v202, v203\n"
    "v_cvt_pk_bf16_f32 v208, v200, v201\n"
    "v_cvt_pk_bf16_f32 v207, v198, v199\n"
    "v_cvt_pk_bf16_f32 v206, v196, v197\n"
    "v_cvt_pk_bf16_f32 v205, v194, v195\n"
    "v_cvt_pk_bf16_f32 v204, v192, v193\n"
    "v_cvt_pk_bf16_f32 v203, v190, v191\n"
    "v_cvt_pk_bf16_f32 v202, v188, v189\n"
    "v_cvt_pk_bf16_f32 v201, v186, v187\n"
    "v_cvt_pk_bf16_f32 v200, v184, v185\n"
    "v_cvt_pk_bf16_f32 v199, v182, v183\n"
    "v_cvt_pk_bf16_f32 v198, v180, v181\n"
    "v_cvt_pk_bf16_f32 v197, v178, v179\n"
    "v_cvt_pk_bf16_f32 v196, v176, v177\n"
    "v_cndmask_b32_e64 v196, v196, v167, s[16:17]\n"
    "s_waitcnt vmcnt(9) lgkmcnt(0)\n"
    "s_barrier\n"
    "ds_read_b128 a[216:219], v254 offset:0\n"
    "ds_read_b128 a[220:223], v254 offset:1024\n"
    "ds_read_b128 a[224:227], v254 offset:40960\n"
    "ds_read_b128 a[228:231], v254 offset:41984\n"
    "ds_read_b128 a[232:235], v254 offset:2048\n"
    "ds_read_b128 a[236:239], v254 offset:3072\n"
    "ds_read_b128 a[240:243], v254 offset:43008\n"
    "ds_read_b128 a[244:247], v254 offset:44032\n"
    "v_and_b32 v232, v235, v0\n"
    "v_and_b32 v233, v235, v2\n"
    "v_dot2c_f32_bf16 v238, v232, v232\n"
    "v_dot2c_f32_bf16 v239, v233, v233\n"
    "v_dot2c_f32_bf16 v238, v1, v1\n"
    "v_dot2c_f32_bf16 v239, v3, v3\n"
    "s_add_u32 s41, s40, 655360\n"
    "buffer_load_dwordx4 v[40:43], v244, s[4:7], s41 offen sc1\n"
    "s_waitcnt vmcnt(9)\n"
    "v_dot2c_f32_bf16 v238, v4, v4\n"
    "v_dot2c_f32_bf16 v239, v6, v6\n"
    "v_dot2c_f32_bf16 v238, v5, v5\n"
    "v_dot2c_f32_bf16 v239, v7, v7\n"
    "s_add_u32 s41, s40, 671744\n"
    "buffer_load_dwordx4 v[44:47], v244, s[4:7], s41 offen sc1\n"
    "s_waitcnt vmcnt(9)\n"
    "v_dot2c_f32_bf16 v238, v8, v8\n"
    "v_dot2c_f32_bf16 v239, v10, v10\n"
    "v_dot2c_f32_bf16 v238, v9, v9\n"
    "v_dot2c_f32_bf16 v239, v11, v11\n"
    "s_add_u32 s41, s40, 786432\n"
    "buffer_load_dwordx4 v[48:51], v244, s[4:7], s41 offen sc1\n"
    "s_waitcnt vmcnt(9)\n"
    "v_dot2c_f32_bf16 v238, v12, v12\n"
    "v_dot2c_f32_bf16 v239, v14, v14\n"
    "v_dot2c_f32_bf16 v238, v13, v13\n"
    "v_dot2c_f32_bf16 v239, v15, v15\n"
    "s_add_u32 s41, s40, 802816\n"
    "buffer_load_dwordx4 v[52:55], v244, s[4:7], s41 offen sc1\n"
    "s_waitcnt vmcnt(9)\n"
    "v_dot2c_f32_bf16 v238, v16, v16\n"
    "v_dot2c_f32_bf16 v239, v18, v18\n"
    "v_dot2c_f32_bf16 v238, v17, v17\n"
    "v_dot2c_f32_bf16 v239, v19, v19\n"
    "s_add_u32 s41, s40, 917504\n"
    "buffer_load_dwordx4 v[56:59], v244, s[4:7], s41 offen sc1\n"
    "s_waitcnt vmcnt(9)\n"
    "v_dot2c_f32_bf16 v238, v20, v20\n"
    "v_dot2c_f32_bf16 v239, v22, v22\n"
    "v_dot2c_f32_bf16 v238, v21, v21\n"
    "v_dot2c_f32_bf16 v239, v23, v23\n"
    "s_add_u32 s41, s40, 933888\n"
    "buffer_load_dwordx4 v[60:63], v244, s[4:7], s41 offen sc1\n"
    "s_waitcnt vmcnt(9)\n"
    "v_dot2c_f32_bf16 v238, v24, v24\n"
    "v_dot2c_f32_bf16 v239, v26, v26\n"
    "v_dot2c_f32_bf16 v238, v25, v25\n"
    "v_dot2c_f32_bf16 v239, v27, v27\n"
    "s_waitcnt vmcnt(8)\n"
    "v_dot2c_f32_bf16 v238, v28, v28\n"
    "v_dot2c_f32_bf16 v239, v30, v30\n"
    "v_dot2c_f32_bf16 v238, v29, v29\n"
    "v_dot2c_f32_bf16 v239, v31, v31\n"
    "v_swap_b32 v2, v4\n"
    "v_swap_b32 v3, v5\n"
    "s_nop 0\n"
    "v_add_f32 v232, v238, v239\n"
    "v_cmp_ngt_f32 vcc, 4.0, v232\n"
    "s_nop 0\n"
    "s_cbranch_vccnz .Lre0\n"
    ".Lrr0:\n"
    "s_waitcnt lgkmcnt(6)\n"
    "v_mfma_f32_16x16x32_bf16 v[160:163], a[216:219], v[0:3], 0\n"
    "s_add_u32 s41, s40, 1048576\n"
    "v_mfma_f32_16x16x32_bf16 v[164:167], a[216:219], v[4:7], 0\n"
    "ds_read_b128 a[216:219], v254 offset:4096\n"
    "buffer_load_dwordx4 v[64:67], v244, s[4:7], s41 offen sc1\n"
    "v_mfma_f32_16x16x32_bf16 v[168:171], a[220:223], v[0:3], 0\n"
    "v_swap_b32 v10, v12\n"
    "v_mfma_f32_16x16x32_bf16 v[172:175], a[220:223], v[4:7], 0\n"
    "s_waitcnt lgkmcnt(5)\n"
    "ds_read_b128 a[220:223], v254 offset:5120\n"
    "v_mfma_f32_16x16x32_bf16 v[176:179], a[224:227], v[0:3], 0\n"
    "s_add_u32 s41, s40, 1064960\n"
    "v_swap_b32 v11, v13\n"
    "v_mfma_f32_16x16x32_bf16 v[180:183], a[224:227], v[4:7], 0\n"
    "ds_read_b128 a[224:227], v254 offset:45056\n"
    "buffer_load_dwordx4 v[68:71], v244, s[4:7], s41 offen sc1\n"
    "v_mfma_f32_16x16x32_bf16 v[184:187], a[228:231], v[0:3], 0\n"
    "ds_write_b128 v222, v[196:199] offset:1024\n"
    "v_mfma_f32_16x16x32_bf16 v[188:191], a[228:231], v[4:7], 0\n"
    "s_waitcnt lgkmcnt(6)\n"
    "ds_read_b128 a[228:231], v254 offset:46080\n"
    "v_mfma_f32_16x16x32_bf16 v[160:163], a[232:235], v[8:11], v[160:163]\n"
    "s_add_u32 s41, s40, 1179648\n"
    "v_mfma_f32_16x16x32_bf16 v[164:167], a[232:235], v[12:15], v[164:167]\n"
    "ds_read_b128 a[232:235], v254 offset:6144\n"
    "buffer_load_dwordx4 v[72:75], v244, s[4:7], s41 offen sc1\n"
    "v_mfma_f32_16x16x32_bf16 v[168:171], a[236:239], v[8:11], v[168:171]\n"
    "v_swap_b32 v18, v20\n"
    "v_mfma_f32_16x16x32_bf16 v[172:175], a[236:239], v[12:15], v[172:175]\n"
    "s_waitcnt lgkmcnt(6)\n"
    "ds_read_b128 a[236:239], v254 offset:7168\n"
    "v_mfma_f32_16x16x32_bf16 v[176:179], a[240:243], v[8:11], v[176:179]\n"
    "s_add_u32 s41, s40, 1196032\n"
    "v_swap_b32 v19, v21\n"
    "v_mfma_f32_16x16x32_bf16 v[180:183], a[240:243], v[12:15], v[180:183]\n"
    "ds_read_b128 a[240:243], v254 offset:47104\n"
    "buffer_load_dwordx4 v[76:79], v244, s[4:7], s41 offen sc1\n"
    "v_mfma_f32_16x16x32_bf16 v[184:187], a[244:247], v[8:11], v[184:187]\n"
    "ds_write_b128 v222, v[200:203] offset:3072\n"
    "v_mfma_f32_16x16x32_bf16 v[188:191], a[244:247], v[12:15], v[188:191]\n"
    "s_waitcnt lgkmcnt(7)\n"
    "ds_read_b128 a[244:247], v254 offset:48128\n"
    "v_mfma_f32_16x16x32_bf16 v[160:163], a[216:219], v[16:19], v[160:163]\n"
    "s_add_u32 s41, s40, 1310720\n"
    "v_mfma_f32_16x16x32_bf16 v[164:167], a[216:219], v[20:23], v[164:167]\n"
    "ds_read_b128 a[216:219], v254 offset:8192\n"
    "buffer_load_dwordx4 v[80:83], v244, s[4:7], s41 offen sc1\n"
    "v_mfma_f32_16x16x32_bf16 v[168:171], a[220:223], v[16:19], v[168:171]\n"
    "v_swap_b32 v26, v28\n"
    "v_mfma_f32_16x16x32_bf16 v[172:175], a[220:223], v[20:23], v[172:175]\n"
    "s_waitcnt lgkmcnt(7)\n"
    "ds_read_b128 a[220:223], v254 offset:9216\n"
    "v_mfma_f32_16x16x32_bf16 v[176:179], a[224:227], v[16:19], v[176:179]\n"
    "s_add_u32 s41, s40, 1327104\n"
    "v_swap_b32 v27, v29\n"
    "v_mfma_f32_16x16x32_bf16 v[180:183], a[224:227], v[20:23], v[180:183]\n"
    "s_waitcnt lgkmcnt(6)\n"
    "ds_read_b128 a[224:227], v254 offset:49152\n"
    "buffer_load_dwordx4 v[84:87], v244, s[4:7], s41 offen sc1\n"
    "v_mfma_f32_16x16x32_bf16 v[184:187], a[228:231], v[16:19], v[184:187]\n"
    "ds_write_b128 v222, a[128:131] offset:0\n"
    "v_mfma_f32_16x16x32_bf16 v[188:191], a[228:231], v[20:23], v[188:191]\n"
    "ds_read_b128 a[228:231], v254 offset:50176\n"
    "v_mfma_f32_16x16x32_bf16 v[160:163], a[232:235], v[24:27], v[160:163]\n"
    "s_add_u32 s41, s40, 1441792\n"
    "v_mfma_f32_16x16x32_bf16 v[164:167], a[232:235], v[28:31], v[164:167]\n"
    "s_waitcnt lgkmcnt(7)\n"
    "ds_read_b128 a[232:235], v254 offset:10240\n"
    "buffer_load_dwordx4 v[88:91], v244, s[4:7], s41 offen sc1\n"
    "v_mfma_f32_16x16x32_bf16 v[168:171], a[236:239], v[24:27], v[168:171]\n"
    "v_mov_b32 v236, v238\n"
    "v_mov_b32 v237, v239\n"
    "v_mfma_f32_16x16x32_bf16 v[172:175], a[236:239], v[28:31], v[172:175]\n"
    "ds_read_b128 a[236:239], v254 offset:11264\n"
    "v_mfma_f32_16x16x32_bf16 v[176:179], a[240:243], v[24:27], v[176:179]\n"
    "s_add_u32 s41, s40, 1458176\n"
    "v_mfma_f32_16x16x32_bf16 v[180:183], a[240:243], v[28:31], v[180:183]\n"
    "s_waitcnt lgkmcnt(6)\n"
    "ds_read_b128 a[240:243], v254 offset:51200\n"
    "buffer_load_dwordx4 v[92:95], v244, s[4:7], s41 offen sc1\n"
    "v_mfma_f32_16x16x32_bf16 v[184:187], a[244:247], v[24:27], v[184:187]\n"
    "v_mfma_f32_16x16x32_bf16 v[188:191], a[244:247], v[28:31], v[188:191]\n"
    "s_waitcnt vmcnt(8)\n"
    "ds_read_b128 a[244:247], v254 offset:52224\n"
    "buffer_load_dwordx4 v[216:219], v231, s[4:7], s12 offen sc1\n"
    "v_dot2c_f32_bf16 v238, v32, v32\n"
    "v_dot2c_f32_bf16 v239, v34, v34\n"
    "v_dot2c_f32_bf16 v238, v33, v33\n"
    "v_dot2c_f32_bf16 v239, v35, v35\n"
    "v_dot2c_f32_bf16 v238, v36, v36\n"
    "v_dot2c_f32_bf16 v239, v38, v38\n"
    "v_dot2c_f32_bf16 v238, v37, v37\n"
    "v_dot2c_f32_bf16 v239, v39, v39\n"
    "v_dot2c_f32_bf16 v238, v40, v40\n"
    "v_dot2c_f32_bf16 v239, v42, v42\n"
    "v_dot2c_f32_bf16 v238, v41, v41\n"
    "v_dot2c_f32_bf16 v239, v43, v43\n"
    "v_dot2c_f32_bf16 v238, v44, v44\n"
    "v_dot2c_f32_bf16 v239, v46, v46\n"
    "v_dot2c_f32_bf16 v238, v45, v45\n"
    "v_dot2c_f32_bf16 v239, v47, v47\n"
    "v_dot2c_f32_bf16 v238, v48, v48\n"
    "v_dot2c_f32_bf16 v239, v50, v50\n"
    "v_dot2c_f32_bf16 v238, v49, v49\n"
    "v_dot2c_f32_bf16 v239, v51, v51\n"
    "v_dot2c_f32_bf16 v238, v52, v52\n"
    "v_dot2c_f32_bf16 v239, v54, v54\n"
    "v_dot2c_f32_bf16 v238, v53, v53\n"
    "v_dot2c_f32_bf16 v239, v55, v55\n"
    "v_dot2c_f32_bf16 v238, v56, v56\n"
    "v_dot2c_f32_bf16 v239, v58, v58\n"
    "v_dot2c_f32_bf16 v238, v57, v57\n"
    "v_dot2c_f32_bf16 v239, v59, v59\n"
    "v_dot2c_f32_bf16 v238, v60, v60\n"
    "v_dot2c_f32_bf16 v239, v62, v62\n"
    "v_dot2c_f32_bf16 v238, v61, v61\n"
    "v_dot2c_f32_bf16 v239, v63, v63\n"
    "v_swap_b32 v34, v36\n"
    "v_swap_b32 v35, v37\n"
    "s_nop 0\n"
    "v_add_f32 v232, v238, v239\n"
    "v_cmp_ngt_f32 vcc, 4.0, v232\n"
    "s_nop 0\n"
    "s_cbranch_vccnz .Lre1\n"
    ".Lrr1:\n"
    "v_mfma_f32_16x16x32_bf16 v[160:163], a[216:219], v[32:35], v[160:163]\n"
    "s_add_u32 s41, s40, 1572864\n"
    "v_mfma_f32_16x16x32_bf16 v[164:167], a[216:219], v[36:39], v[164:167]\n"
    "s_waitcnt lgkmcnt(6)\n"
    "ds_read_b128 a[216:219], v254 offset:12288\n"
    "buffer_load_dwordx4 v[96:99], v244, s[4:7], s41 offen sc1\n"
    "v_mfma_f32_16x16x32_bf16 v[168:171], a[220:223], v[32:35], v[168:171]\n"
    "v_swap_b32 v42, v44\n"
    "v_mfma_f32_16x16x32_bf16 v[172:175], a[220:223], v[36:39], v[172:175]\n"
    "ds_read_b128 a[220:223], v254 offset:13312\n"
    "v_mfma_f32_16x16x32_bf16 v[176:179], a[224:227], v[32:35], v[176:179]\n"
    "s_add_u32 s41, s40, 1589248\n"
    "v_swap_b32 v43, v45\n"
    "v_mfma_f32_16x16x32_bf16 v[180:183], a[224:227], v[36:39], v[180:183]\n"
    "s_waitcnt lgkmcnt(5)\n"
    "ds_read_b128 a[224:227], v254 offset:53248\n"
    "buffer_load_dwordx4 v[100:103], v244, s[4:7], s41 offen sc1\n"
    "v_mfma_f32_16x16x32_bf16 v[184:187], a[228:231], v[32:35], v[184:187]\n"
    "ds_write_b128 v222, a[132:135] offset:2048\n"
    "v_mfma_f32_16x16x32_bf16 v[188:191], a[228:231], v[36:39], v[188:191]\n"
    "ds_read_b128 a[228:231], v254 offset:54272\n"
    "v_mfma_f32_16x16x32_bf16 v[160:163], a[232:235], v[40:43], v[160:163]\n"
    "s_add_u32 s41, s40, 1703936\n"
    "v_mfma_f32_16x16x32_bf16 v[164:167], a[232:235], v[44:47], v[164:167]\n"
    "s_waitcnt lgkmcnt(6)\n"
    "ds_read_b128 a[232:235], v254 offset:14336\n"
    "buffer_load_dwordx4 v[104:107], v244, s[4:7], s41 offen sc1\n"
    "v_mfma_f32_16x16x32_bf16 v[168:171], a[236:239], v[40:43], v[168:171]\n"
    "v_swap_b32 v50, v52\n"
    "v_mfma_f32_16x16x32_bf16 v[172:175], a[236:239], v[44:47], v[172:175]\n"
    "ds_read_b128 a[236:239], v254 offset:15360\n"
    "v_mfma_f32_16x16x32_bf16 v[176:179], a[240:243], v[40:43], v[176:179]\n"
    "s_add_u32 s41, s40, 1720320\n"
    "v_swap_b32 v51, v53\n"
    "v_mfma_f32_16x16x32_bf16 v[180:183], a[240:243], v[44:47], v[180:183]\n"
    "s_waitcnt lgkmcnt(6)\n"
    "ds_read_b128 a[240:243], v254 offset:55296\n"
    "buffer_load_dwordx4 v[108:111], v244, s[4:7], s41 offen sc1\n"
    "v_mfma_f32_16x16x32_bf16 v[184:187], a[244:247], v[40:43], v[184:187]\n"
    "ds_write_b128 v222, a[136:139] offset:4096\n"
    "v_mfma_f32_16x16x32_bf16 v[188:191], a[244:247], v[44:47], v[188:191]\n"
    "ds_read_b128 a[244:247], v254 offset:56320\n"
    "v_mfma_f32_16x16x32_bf16 v[160:163], a[216:219], v[48:51], v[160:163]\n"
    "s_add_u32 s41, s40, 1835008\n"
    "v_mfma_f32_16x16x32_bf16 v[164:167], a[216:219], v[52:55], v[164:167]\n"
    "s_waitcnt lgkmcnt(7)\n"
    "ds_read_b128 a[216:219], v254 offset:16384\n"
    "buffer_load_dwordx4 v[112:115], v244, s[4:7], s41 offen sc1\n"
    "v_mfma_f32_16x16x32_bf16 v[168:171], a[220:223], v[48:51], v[168:171]\n"
    "v_swap_b32 v58, v60\n"
    "v_mfma_f32_16x16x32_bf16 v[172:175], a[220:223], v[52:55], v[172:175]\n"
    "ds_read_b128 a[220:223], v254 offset:17408\n"
    "v_mfma_f32_16x16x32_bf16 v[176:179], a[224:227], v[48:51], v[176:179]\n"
    "s_add_u32 s41, s40, 1851392\n"
    "v_swap_b32 v59, v61\n"
    "v_mfma_f32_16x16x32_bf16 v[180:183], a[224:227], v[52:55], v[180:183]\n"
    "s_waitcnt lgkmcnt(6)\n"
    "ds_read_b128 a[224:227], v254 offset:57344\n"
    "buffer_load_dwordx4 v[116:119], v244, s[4:7], s41 offen sc1\n"
    "v_mfma_f32_16x16x32_bf16 v[184:187], a[228:231], v[48:51], v[184:187]\n"
    "ds_write_b128 v222, v[204:207] offset:5120\n"
    "v_mfma_f32_16x16x32_bf16 v[188:191], a[228:231], v[52:55], v[188:191]\n"
    "ds_read_b128 a[228:231], v254 offset:58368\n"
    "v_mfma_f32_16x16x32_bf16 v[160:163], a[232:235], v[56:59], v[160:163]\n"
    "s_add_u32 s41, s40, 1966080\n"
    "v_mfma_f32_16x16x32_bf16 v[164:167], a[232:235], v[60:63], v[164:167]\n"
    "s_waitcnt lgkmcnt(7)\n"
    "ds_read_b128 a[232:235], v254 offset:18432\n"
    "buffer_load_dwordx4 v[120:123], v244, s[4:7], s41 offen sc1\n"
    "v_mfma_f32_16x16x32_bf16 v[168:171], a[236:239], v[56:59], v[168:171]\n"
    "v_mov_b32 v236, v238\n"
    "v_mov_b32 v237, v239\n"
    "v_mfma_f32_16x16x32_bf16 v[172:175], a[236:239], v[60:63], v[172:175]\n"
    "ds_read_b128 a[236:239], v254 offset:19456\n"
    "v_mfma_f32_16x16x32_bf16 v[176:179], a[240:243], v[56:59], v[176:179]\n"
    "s_add_u32 s41, s40, 1982464\n"
    "v_mfma_f32_16x16x32_bf16 v[180:183], a[240:243], v[60:63], v[180:183]\n"
    "s_waitcnt lgkmcnt(6)\n"
    "ds_read_b128 a[240:243], v254 offset:59392\n"
    "buffer_load_dwordx4 v[124:127], v244, s[4:7], s41 offen sc1\n"
    "v_mfma_f32_16x16x32_bf16 v[184:187], a[244:247], v[56:59], v[184:187]\n"
    "v_mfma_f32_16x16x32_bf16 v[188:191], a[244:247], v[60:63], v[188:191]\n"
    "s_waitcnt vmcnt(9)\n"
    "ds_read_b128 a[244:247], v254 offset:60416\n"
    "v_dot2c_f32_bf16 v238, v64, v64\n"
    "v_dot2c_f32_bf16 v239, v66, v66\n"
    "v_dot2c_f32_bf16 v238, v65, v65\n"
    "v_dot2c_f32_bf16 v239, v67, v67\n"
    "v_dot2c_f32_bf16 v238, v68, v68\n"
    "v_dot2c_f32_bf16 v239, v70, v70\n"
    "v_dot2c_f32_bf16 v238, v69, v69\n"
    "v_dot2c_f32_bf16 v239, v71, v71\n"
    "v_dot2c_f32_bf16 v238, v72, v72\n"
    "v_dot2c_f32_bf16 v239, v74, v74\n"
    "v_dot2c_f32_bf16 v238, v73, v73\n"
    "v_dot2c_f32_bf16 v239, v75, v75\n"
    "v_dot2c_f32_bf16 v238, v76, v76\n"
    "v_dot2c_f32_bf16 v239, v78, v78\n"
    "v_dot2c_f32_bf16 v238, v77, v77\n"
    "v_dot2c_f32_bf16 v239, v79, v79\n"
    "v_dot2c_f32_bf16 v238, v80, v80\n"
    "v_dot2c_f32_bf16 v239, v82, v82\n"
    "v_dot2c_f32_bf16 v238, v81, v81\n"
    "v_dot2c_f32_bf16 v239, v83, v83\n"
    "v_dot2c_f32_bf16 v238, v84, v84\n"
    "v_dot2c_f32_bf16 v239, v86, v86\n"
    "v_dot2c_f32_bf16 v238, v85, v85\n"
    "v_dot2c_f32_bf16 v239, v87, v87\n"
    "v_dot2c_f32_bf16 v238, v88, v88\n"
    "v_dot2c_f32_bf16 v239, v90, v90\n"
    "v_dot2c_f32_bf16 v238, v89, v89\n"
    "v_dot2c_f32_bf16 v239, v91, v91\n"
    "v_dot2c_f32_bf16 v238, v92, v92\n"
    "v_dot2c_f32_bf16 v239, v94, v94\n"
    "v_dot2c_f32_bf16 v238, v93, v93\n"
    "v_dot2c_f32_bf16 v239, v95, v95\n"
    "v_swap_b32 v66, v68\n"
    "v_swap_b32 v67, v69\n"
    "s_nop 0\n"
    "v_add_f32 v232, v238, v239\n"
    "v_cmp_ngt_f32 vcc, 4.0, v232\n"
    "s_nop 0\n"
    "s_cbranch_vccnz .Lre2\n"
    ".Lrr2:\n"
    "v_mfma_f32_16x16x32_bf16 v[160:163], a[216:219], v[64:67], v[160:163]\n"
    "s_add_u32 s41, s40, 2097152\n"
    "v_mfma_f32_16x16x32_bf16 v[164:167], a[216:219], v[68:71], v[164:167]\n"
    "s_waitcnt lgkmcnt(6)\n"
    "ds_read_b128 a[216:219], v254 offset:20480\n"
    "buffer_load_dwordx4 v[128:131], v244, s[4:7], s41 offen sc1\n"
    "v_mfma_f32_16x16x32_bf16 v[168:171], a[220:223], v[64:67], v[168:171]\n"
    "v_swap_b32 v74, v76\n"
    "v_mfma_f32_16x16x32_bf16 v[172:175], a[220:223], v[68:71], v[172:175]\n"
    "ds_read_b128 a[220:223], v254 offset:21504\n"
    "v_mfma_f32_16x16x32_bf16 v[176:179], a[224:227], v[64:67], v[176:179]\n"
    "s_add_u32 s41, s40, 2113536\n"
    "v_swap_b32 v75, v77\n"
    "v_mfma_f32_16x16x32_bf16 v[180:183], a[224:227], v[68:71], v[180:183]\n"
    "s_waitcnt lgkmcnt(5)\n"
    "ds_read_b128 a[224:227], v254 offset:61440\n"
    "buffer_load_dwordx4 v[132:135], v244, s[4:7], s41 offen sc1\n"
    "v_mfma_f32_16x16x32_bf16 v[184:187], a[228:231], v[64:67], v[184:187]\n"
    "ds_write_b128 v222, v[208:211] offset:7168\n"
    "v_mfma_f32_16x16x32_bf16 v[188:191], a[228:231], v[68:71], v[188:191]\n"
    "ds_read_b128 a[228:231], v254 offset:62464\n"
    "v_mfma_f32_16x16x32_bf16 v[160:163], a[232:235], v[72:75], v[160:163]\n"
    "s_add_u32 s41, s40, 2228224\n"
    "v_mfma_f32_16x16x32_bf16 v[164:167], a[232:235], v[76:79], v[164:167]\n"
    "s_waitcnt lgkmcnt(6)\n"
    "ds_read_b128 a[232:235], v254 offset:22528\n"
    "buffer_load_dwordx4 v[136:139], v244, s[4:7], s41 offen sc1\n"
    "v_mfma_f32_16x16x32_bf16 v[168:171], a[236:239], v[72:75], v[168:171]\n"
    "v_swap_b32 v82, v84\n"
    "v_mfma_f32_16x16x32_bf16 v[172:175], a[236:239], v[76:79], v[172:175]\n"
    "ds_read_b128 a[236:239], v254 offset:23552\n"
    "v_mfma_f32_16x16x32_bf16 v[176:179], a[240:243], v[72:75], v[176:179]\n"
    "s_add_u32 s41, s40, 2244608\n"
    "v_swap_b32 v83, v85\n"
    "v_mfma_f32_16x16x32_bf16 v[180:183], a[240:243], v[76:79], v[180:183]\n"
    "s_waitcnt lgkmcnt(6)\n"
    "ds_read_b128 a[240:243], v254 offset:63488\n"
    "buffer_load_dwordx4 v[140:143], v244, s[4:7], s41 offen sc1\n"
    "v_mfma_f32_16x16x32_bf16 v[184:187], a[244:247], v[72:75], v[184:187]\n"
    "ds_write_b128 v222, v[212:215] offset:9216\n"
    "v_mfma_f32_16x16x32_bf16 v[188:191], a[244:247], v[76:79], v[188:191]\n"
    "ds_read_b128 a[244:247], v254 offset:64512\n"
    "v_mfma_f32_16x16x32_bf16 v[160:163], a[216:219], v[80:83], v[160:163]\n"
    "s_add_u32 s41, s40, 2359296\n"
    "v_mfma_f32_16x16x32_bf16 v[164:167], a[216:219], v[84:87], v[164:167]\n"
    "s_waitcnt lgkmcnt(7)\n"
    "ds_read_b128 a[216:219], v254 offset:24576\n"
    "buffer_load_dwordx4 v[144:147], v244, s[4:7], s41 offen sc1\n"
    "v_mfma_f32_16x16x32_bf16 v[168:171], a[220:223], v[80:83], v[168:171]\n"
    "v_swap_b32 v90, v92\n"
    "v_mfma_f32_16x16x32_bf16 v[172:175], a[220:223], v[84:87], v[172:175]\n"
    "ds_read_b128 a[220:223], v254 offset:25600\n"
    "v_mfma_f32_16x16x32_bf16 v[176:179], a[224:227], v[80:83], v[176:179]\n"
    "s_add_u32 s41, s40, 2375680\n"
    "v_swap_b32 v91, v93\n"
    "v_mfma_f32_16x16x32_bf16 v[180:183], a[224:227], v[84:87], v[180:183]\n"
    "s_waitcnt lgkmcnt(6)\n"
    "ds_read_b128 a[224:227], v247 offset:24576\n"
    "buffer_load_dwordx4 v[148:151], v244, s[4:7], s41 offen sc1\n"
    "v_mfma_f32_16x16x32_bf16 v[184:187], a[228:231], v[80:83], v[184:187]\n"
    "ds_write_b128 v222, a[140:143] offset:6144\n"
    ".Lpad5:\n"
    "s_cbranch_execz .Lw_ret\n"
    "v_mfma_f32_16x16x32_bf16 v[188:191], a[228:231], v[84:87], v[188:191]\n"
    "ds_read_b128 a[228:231], v247 offset:25600\n"
    "v_mfma_f32_16x16x32_bf16 v[160:163], a[232:235], v[88:91], v[160:163]\n"
    "s_add_u32 s41, s40, 2490368\n"
    "v_mfma_f32_16x16x32_bf16 v[164:167], a[232:235], v[92:95], v[164:167]\n"
    "s_waitcnt lgkmcnt(7)\n"
    "ds_read_b128 a[232:235], v254 offset:26624\n"
    "buffer_load_dwordx4 v[152:155], v244, s[4:7], s41 offen sc1\n"
    "v_mfma_f32_16x16x32_bf16 v[168:171], a[236:239], v[88:91], v[168:171]\n"
    "v_mov_b32 v236, v238\n"
    "v_mov_b32 v237, v239\n"
    "v_mfma_f32_16x16x32_bf16 v[172:175], a[236:239], v[92:95], v[172:175]\n"
    "ds_read_b128 a[236:239], v254 offset:27648\n"
    "v_mfma_f32_16x16x32_bf16 v[176:179], a[240:243], v[88:91], v[176:179]\n"
    "s_add_u32 s41, s40, 2506752\n"
    "s_waitcnt vmcnt(15)\n"
    "v_cmp_ne_u32 vcc, v216, v253\n"
    "s_nop 0\n"
    "v_cmp_ne_u32 s[58:59], v217, v253\n"
    "s_nop 0\n"
    "s_or_b64 vcc, vcc, s[58:59]\n"
    "v_cmp_ne_u32 s[58:59], v218, v253\n"
    "s_nop 0\n"
    "s_or_b64 vcc, vcc, s[58:59]\n"
    "v_cmp_ne_u32 s[58:59], v219, v253\n"
    "s_nop 0\n"
    "s_or_b64 vcc, vcc, s[58:59]\n"
    "s_or_b32 s58, vcc_lo, vcc_hi\n"
    "s_cbranch_scc1 .Lp4\n"
    ".Lpr4:\n"
    "v_mfma_f32_16x16x32_bf16 v[180:183], a[240:243], v[92:95], v[180:183]\n"
    "s_waitcnt lgkmcnt(6)\n"
    "ds_read_b128 a[240:243], v247 offset:26624\n"
    "buffer_load_dwordx4 v[156:159], v244, s[4:7], s41 offen sc1\n"
    "v_mfma_f32_16x16x32_bf16 v[184:187], a[244:247], v[88:91], v[184:187]\n"
    "v_mfma_f32_16x16x32_bf16 v[188:191], a[244:247], v[92:95], v[188:191]\n"
    "s_waitcnt vmcnt(8)\n"
    "ds_read_b128 a[244:247], v247 offset:27648\n"
    "v_dot2c_f32_bf16 v238, v96, v96\n"
    "v_dot2c_f32_bf16 v239, v98, v98\n"
    "v_dot2c_f32_bf16 v238, v97, v97\n"
    "v_dot2c_f32_bf16 v239, v99, v99\n"
    "v_dot2c_f32_bf16 v238, v100, v100\n"
    "v_dot2c_f32_bf16 v239, v102, v102\n"
    "v_dot2c_f32_bf16 v238, v101, v101\n"
    "v_dot2c_f32_bf16 v239, v103, v103\n"
    "v_dot2c_f32_bf16 v238, v104, v104\n"
    "v_dot2c_f32_bf16 v239, v106, v106\n"
    "v_dot2c_f32_bf16 v238, v105, v105\n"
    "v_dot2c_f32_bf16 v239, v107, v107\n"
    "v_dot2c_f32_bf16 v238, v108, v108\n"
    "v_dot2c_f32_bf16 v239, v110, v110\n"
    "v_dot2c_f32_bf16 v238, v109, v109\n"
    "v_dot2c_f32_bf16 v239, v111, v111\n"
    "v_dot2c_f32_bf16 v238, v112, v112\n"
    "v_dot2c_f32_bf16 v239, v114, v114\n"
    "v_dot2c_f32_bf16 v238, v113, v113\n"
    "v_dot2c_f32_bf16 v239, v115, v115\n"
    "v_dot2c_f32_bf16 v238, v116, v116\n"
    "v_dot2c_f32_bf16 v239, v118, v118\n"
    "v_dot2c_f32_bf16 v238, v117, v117\n"
    "v_dot2c_f32_bf16 v239, v119, v119\n"
    "v_dot2c_f32_bf16 v238, v120, v120\n"
    "v_dot2c_f32_bf16 v239, v122, v122\n"
    "v_dot2c_f32_bf16 v238, v121, v121\n"
    "v_dot2c_f32_bf16 v239, v123, v123\n"
    "v_dot2c_f32_bf16 v238, v124, v124\n"
    "v_dot2c_f32_bf16 v239, v126, v126\n"
    "v_dot2c_f32_bf16 v238, v125, v125\n"
    "v_dot2c_f32_bf16 v239, v127, v127\n"
    "v_swap_b32 v98, v100\n"
    "v_swap_b32 v99, v101\n"
    "s_nop 0\n"
    "v_add_f32 v232, v238, v239\n"
    "v_cmp_ngt_f32 vcc, 4.0, v232\n"
    "s_nop 0\n"
    "s_cbranch_vccnz .Lre3\n"
    ".Lrr3:\n"
    "v_mfma_f32_16x16x32_bf16 v[160:163], a[216:219], v[96:99], v[160:163]\n"
    "s_add_u32 m0, s14, 16384\n"
    "s_add_u32 s47, s18, 16384\n"
    "v_mfma_f32_16x16x32_bf16 v[164:167], a[216:219], v[100:103], v[164:167]\n"
    "s_waitcnt lgkmcnt(6)\n"
    "ds_read_b128 a[216:219], v254 offset:28672\n"
    "buffer_load_dwordx4 v254, s[4:7], s47 offen sc1 lds\n"
    "v_mfma_f32_16x16x32_bf16 v[168:171], a[220:223], v[96:99], v[168:171]\n"
    "v_swap_b32 v106, v108\n"
    "v_mfma_f32_16x16x32_bf16 v[172:175], a[220:223], v[100:103], v[172:175]\n"
    "s_add_u32 s47, s18, 20480\n"
    "ds_read_b128 a[220:223], v254 offset:29696\n"
    "v_mfma_f32_16x16x32_bf16 v[176:179], a[224:227], v[96:99], v[176:179]\n"
    "s_add_u32 m0, s14, 20480\n"
    "v_swap_b32 v107, v109\n"
    "v_mfma_f32_16x16x32_bf16 v[180:183], a[224:227], v[100:103], v[180:183]\n"
    "s_waitcnt lgkmcnt(5)\n"
    "ds_read_b128 a[224:227], v247 offset:28672\n"
    "buffer_load_dwordx4 v254, s[4:7], s47 offen sc1 lds\n"
    "v_mfma_f32_16x16x32_bf16 v[184:187], a[228:231], v[96:99], v[184:187]\n"
    "ds_write_b128 v222, a[144:147] offset:8192\n"
    "v_mfma_f32_16x16x32_bf16 v[188:191], a[228:231], v[100:103], v[188:191]\n"
    "s_add_u32 s47, s18, 24576\n"
    "ds_read_b128 a[228:231], v247 offset:29696\n"
    "v_mfma_f32_16x16x32_bf16 v[160:163], a[232:235], v[104:107], v[160:163]\n"
    "s_add_u32 m0, s14, 24576\n"
    "v_mfma_f32_16x16x32_bf16 v[164:167], a[232:235], v[108:111], v[164:167]\n"
    "s_waitcnt lgkmcnt(6)\n"
    "ds_read_b128 a[232:235], v254 offset:30720\n"
    "buffer_load_dwordx4 v254, s[4:7], s47 offen sc1 lds\n"
    "v_mfma_f32_16x16x32_bf16 v[168:171], a[236:239], v[104:107], v[168:171]\n"
    "v_swap_b32 v114, v116\n"
    "v_mfma_f32_16x16x32_bf16 v[172:175], a[236:239], v[108:111], v[172:175]\n"
    "s_add_u32 s47, s18, 28672\n"
    "ds_read_b128 a[236:239], v254 offset:31744\n"
    "v_mfma_f32_16x16x32_bf16 v[176:179], a[240:243], v[104:107], v[176:179]\n"
    "s_add_u32 m0, s14, 28672\n"
    "v_swap_b32 v115, v117\n"
    "v_mfma_f32_16x16x32_bf16 v[180:183], a[240:243], v[108:111], v[180:183]\n"
    "s_waitcnt lgkmcnt(6)\n"
    "ds_read_b128 a[240:243], v247 offset:30720\n"
    "buffer_load_dwordx4 v254, s[4:7], s47 offen sc1 lds\n"
    "v_mfma_f32_16x16x32_bf16 v[184:187], a[244:247], v[104:107], v[184:187]\n"
    "v_mfma_f32_16x16x32_bf16 v[188:191], a[244:247], v[108:111], v[188:191]\n"
    "s_add_u32 s47, s18, 32768\n"
    "ds_read_b128 a[244:247], v247 offset:31744\n"
    "v_mfma_f32_16x16x32_bf16 v[160:163], a[216:219], v[112:115], v[160:163]\n"
    "s_add_u32 m0, s14, 32768\n"
    "v_mfma_f32_16x16x32_bf16 v[164:167], a[216:219], v[116:119], v[164:167]\n"
    "s_waitcnt lgkmcnt(6)\n"
    "ds_read_b128 a[216:219], v254 offset:32768\n"
    "buffer_load_dwordx4 v254, s[4:7], s47 offen sc1 lds\n"
    "v_mfma_f32_16x16x32_bf16 v[168:171], a[220:223], v[112:115], v[168:171]\n"
    "v_swap_b32 v122, v124\n"
    "v_mfma_f32_16x16x32_bf16 v[172:175], a[220:223], v[116:119], v[172:175]\n"
    "s_add_u32 s47, s18, 36864\n"
    "ds_read_b128 a[220:223], v254 offset:33792\n"
    "v_mfma_f32_16x16x32_bf16 v[176:179], a[224:227], v[112:115], v[176:179]\n"
    "s_add_u32 m0, s14, 36864\n"
    "v_swap_b32 v123, v125\n"
    "v_mfma_f32_16x16x32_bf16 v[180:183], a[224:227], v[116:119], v[180:183]\n"
    "s_waitcnt lgkmcnt(5)\n"
    "ds_read_b128 a[224:227], v247 offset:32768\n"
    "buffer_load_dwordx4 v254, s[4:7], s47 offen sc1 lds\n"
    "v_mfma_f32_16x16x32_bf16 v[184:187], a[228:231], v[112:115], v[184:187]\n"
    "v_mfma_f32_16x16x32_bf16 v[188:191], a[228:231], v[116:119], v[188:191]\n"
    "ds_read_b128 a[228:231], v247 offset:33792\n"
    "v_mfma_f32_16x16x32_bf16 v[160:163], a[232:235], v[120:123], v[160:163]\n"
    "v_mfma_f32_16x16x32_bf16 v[164:167], a[232:235], v[124:127], v[164:167]\n"
    "s_waitcnt lgkmcnt(5)\n"
    "ds_read_b128 a[232:235], v254 offset:34816\n"
    "buffer_load_dword a254, v234, s[8:11], s49 offen\n"
    "v_mfma_f32_16x16x32_bf16 v[168:171], a[236:239], v[120:123], v[168:171]\n"
    "v_mov_b32 v236, v238\n"
    "v_mov_b32 v237, v239\n"
    "v_mfma_f32_16x16x32_bf16 v[172:175], a[236:239], v[124:127], v[172:175]\n"
    "ds_read_b128 a[236:239], v254 offset:35840\n"
    "v_mfma_f32_16x16x32_bf16 v[176:179], a[240:243], v[120:123], v[176:179]\n"
    "s_add_u32 s54, s48, 0\n"
    "v_mfma_f32_16x16x32_bf16 v[180:183], a[240:243], v[124:127], v[180:183]\n"
    "s_waitcnt lgkmcnt(5)\n"
    "ds_read_b128 a[240:243], v247 offset:34816\n"
    "buffer_load_dwordx4 a[160:163], v254, s[4:7], s54 offen sc1\n"
    "v_mfma_f32_16x16x32_bf16 v[184:187], a[244:247], v[120:123], v[184:187]\n"
    "v_mfma_f32_16x16x32_bf16 v[188:191], a[244:247], v[124:127], v[188:191]\n"
    "s_waitcnt vmcnt(8)\n"
    "ds_read_b128 a[244:247], v247 offset:35840\n"
    "v_dot2c_f32_bf16 v238, v128, v128\n"
    "v_dot2c_f32_bf16 v239, v130, v130\n"
    "v_dot2c_f32_bf16 v238, v129, v129\n"
    "v_dot2c_f32_bf16 v239, v131, v131\n"
    "v_dot2c_f32_bf16 v238, v132, v132\n"
    "v_dot2c_f32_bf16 v239, v134, v134\n"
    "v_dot2c_f32_bf16 v238, v133, v133\n"
    "v_dot2c_f32_bf16 v239, v135, v135\n"
    "v_dot2c_f32_bf16 v238, v136, v136\n"
    "v_dot2c_f32_bf16 v239, v138, v138\n"
    "v_dot2c_f32_bf16 v238, v137, v137\n"
    "v_dot2c_f32_bf16 v239, v139, v139\n"
    "v_dot2c_f32_bf16 v238, v140, v140\n"
    "v_dot2c_f32_bf16 v239, v142, v142\n"
    "v_dot2c_f32_bf16 v238, v141, v141\n"
    "v_dot2c_f32_bf16 v239, v143, v143\n"
    "v_dot2c_f32_bf16 v238, v144, v144\n"
    "v_dot2c_f32_bf16 v239, v146, v146\n"
    "v_dot2c_f32_bf16 v238, v145, v145\n"
    "v_dot2c_f32_bf16 v239, v147, v147\n"
    "v_dot2c_f32_bf16 v238, v148, v148\n"
    "v_dot2c_f32_bf16 v239, v150, v150\n"
    "v_dot2c_f32_bf16 v238, v149, v149\n"
    "v_dot2c_f32_bf16 v239, v151, v151\n"
    "v_dot2c_f32_bf16 v238, v152, v152\n"
    "v_dot2c_f32_bf16 v239, v154, v154\n"
    "v_dot2c_f32_bf16 v238, v153, v153\n"
    "v_dot2c_f32_bf16 v239, v155, v155\n"
    "v_dot2c_f32_bf16 v238, v156, v156\n"
    "v_dot2c_f32_bf16 v239, v158, v158\n"
    "v_dot2c_f32_bf16 v238, v157, v157\n"
    "v_dot2c_f32_bf16 v239, v159, v159\n"
    "v_swap_b32 v130, v132\n"
    "v_swap_b32 v131, v133\n"
    "s_nop 0\n"
    "v_add_f32 v232, v238, v239\n"
    "v_cmp_ngt_f32 vcc, 4.0, v232\n"
    "s_nop 0\n"
    "s_cbranch_vccnz .Lre4\n"
    ".Lrr4:\n"
    "v_mfma_f32_16x16x32_bf16 v[160:163], a[216:219], v[128:131], v[160:163]\n"
    "s_add_u32 s54, s48, 4096\n"
    "v_permlane16_swap_b32 v238, v239\n"
    "v_mfma_f32_16x16x32_bf16 v[164:167], a[216:219], v[132:135], v[164:167]\n"
    "s_waitcnt lgkmcnt(5)\n"
    "ds_read_b128 a[216:219], v254 offset:36864\n"
    "buffer_load_dwordx4 a[164:167], v254, s[4:7], s54 offen sc1\n"
    "v_mfma_f32_16x16x32_bf16 v[168:171], a[220:223], v[128:131], v[168:171]\n"
    "v_swap_b32 v138, v140\n"
    "v_mfma_f32_16x16x32_bf16 v[172:175], a[220:223], v[132:135], v[172:175]\n"
    "ds_read_b128 a[220:223], v254 offset:37888\n"
    "v_add_f32 v238, v238, v239\n"
    "v_mfma_f32_16x16x32_bf16 v[176:179], a[224:227], v[128:131], v[176:179]\n"
    "s_add_u32 s54, s48, 8192\n"
    "v_swap_b32 v139, v141\n"
    "v_mfma_f32_16x16x32_bf16 v[180:183], a[224:227], v[132:135], v[180:183]\n"
    "s_waitcnt lgkmcnt(5)\n"
    "ds_read_b128 a[224:227], v247 offset:36864\n"
    "buffer_load_dwordx4 a[168:171], v254, s[4:7], s54 offen sc1\n"
    "v_mfma_f32_16x16x32_bf16 v[184:187], a[228:231], v[128:131], v[184:187]\n"
    "v_mov_b32 v239, v238\n"
    "v_and_b32 v244, 31, v255\n"
    "v_mfma_f32_16x16x32_bf16 v[188:191], a[228:231], v[132:135], v[188:191]\n"
    "ds_read_b128 a[228:231], v247 offset:37888\n"
    "v_mfma_f32_16x16x32_bf16 v[160:163], a[232:235], v[136:139], v[160:163]\n"
    "s_add_u32 s54, s48, 12288\n"
    "v_permlane32_swap_b32 v238, v239\n"
    "v_mfma_f32_16x16x32_bf16 v[164:167], a[232:235], v[140:143], v[164:167]\n"
    "s_waitcnt lgkmcnt(5)\n"
    "ds_read_b128 a[232:235], v254 offset:38912\n"
    "buffer_load_dwordx4 a[172:175], v254, s[4:7], s54 offen sc1\n"
    "v_mfma_f32_16x16x32_bf16 v[168:171], a[236:239], v[136:139], v[168:171]\n"
    "v_swap_b32 v146, v148\n"
    "v_mfma_f32_16x16x32_bf16 v[172:175], a[236:239], v[140:143], v[172:175]\n"
    "ds_read_b128 a[236:239], v254 offset:39936\n"
    "v_add_f32 v238, v238, v239\n"
    "v_mfma_f32_16x16x32_bf16 v[176:179], a[240:243], v[136:139], v[176:179]\n"
    "s_add_u32 s54, s48, 16384\n"
    "v_swap_b32 v147, v149\n"
    "v_mfma_f32_16x16x32_bf16 v[180:183], a[240:243], v[140:143], v[180:183]\n"
    "s_waitcnt lgkmcnt(5)\n"
    "ds_read_b128 a[240:243], v247 offset:38912\n"
    "buffer_load_dwordx4 a[176:179], v254, s[4:7], s54 offen sc1\n"
    "v_mfma_f32_16x16x32_bf16 v[184:187], a[244:247], v[136:139], v[184:187]\n"
    "v_mov_b32 v239, v238\n"
    "v_mov_b32 v236, 0x3727c5ac\n"
    "v_mfma_f32_16x16x32_bf16 v[188:191], a[244:247], v[140:143], v[188:191]\n"
    "ds_read_b128 a[244:247], v247 offset:39936\n"
    "v_mov_b32 v237, 0x3727c5ac\n"
    "v_mfma_f32_16x16x32_bf16 v[160:163], a[216:219], v[144:147], v[160:163]\n"
    "s_add_u32 s54, s48, 20480\n"
    "v_permlane16_swap_b32 v238, v239\n"
    "v_mfma_f32_16x16x32_bf16 v[164:167], a[216:219], v[148:151], v[164:167]\n"
    "s_waitcnt lgkmcnt(5)\n"
    "ds_read_b128 a[216:219], v245 offset:0\n"
    "buffer_load_dwordx4 a[180:183], v254, s[4:7], s54 offen sc1\n"
    "v_mfma_f32_16x16x32_bf16 v[168:171], a[220:223], v[144:147], v[168:171]\n"
    "v_swap_b32 v154, v156\n"
    "v_mfma_f32_16x16x32_bf16 v[172:175], a[220:223], v[148:151], v[172:175]\n"
    "ds_read_b128 a[220:223], v245 offset:1024\n"
    "v_fmac_f32 v236, 0x58cccccd, v238\n"
    "v_mfma_f32_16x16x32_bf16 v[176:179], a[224:227], v[144:147], v[176:179]\n"
    "s_add_u32 s54, s48, 24576\n"
    "v_swap_b32 v155, v157\n"
    "v_mfma_f32_16x16x32_bf16 v[180:183], a[224:227], v[148:151], v[180:183]\n"
    "s_waitcnt lgkmcnt(5)\n"
    "ds_read_b128 a[224:227], v245 offset:2048\n"
    "buffer_load_dwordx4 a[184:187], v254, s[4:7], s54 offen sc1\n"
    "v_mfma_f32_16x16x32_bf16 v[184:187], a[228:231], v[144:147], v[184:187]\n"
    "v_fmac_f32 v237, 0x58cccccd, v239\n"
    "v_mfma_f32_16x16x32_bf16 v[188:191], a[228:231], v[148:151], v[188:191]\n"
    "ds_read_b128 a[228:231], v245 offset:3072\n"
    "v_mfma_f32_16x16x32_bf16 v[160:163], a[232:235], v[152:155], v[160:163]\n"
    "s_add_u32 s54, s48, 28672\n"
    "v_rsq_f32 v238, v236\n"
    "v_mfma_f32_16x16x32_bf16 v[164:167], a[232:235], v[156:159], v[164:167]\n"
    "s_waitcnt lgkmcnt(5)\n"
    "ds_read_b128 a[232:235], v245 offset:4096\n"
    "buffer_load_dwordx4 a[188:191], v254, s[4:7], s54 offen sc1\n"
    "v_mfma_f32_16x16x32_bf16 v[168:171], a[236:239], v[152:155], v[168:171]\n"
    "v_rsq_f32 v239, v237\n"
    "v_mfma_f32_16x16x32_bf16 v[172:175], a[236:239], v[156:159], v[172:175]\n"
    "ds_read_b128 a[236:239], v245 offset:5120\n"
    "v_mul_f32 v236, 0x328f1bbd, v236\n"
    "v_mfma_f32_16x16x32_bf16 v[176:179], a[240:243], v[152:155], v[176:179]\n"
    "s_add_u32 s54, s48, 32768\n"
    "v_mul_f32 v237, 0x328f1bbd, v237\n"
    "v_mul_f32 v230, 0xcca52b5c, v238\n"
    "v_mfma_f32_16x16x32_bf16 v[180:183], a[240:243], v[156:159], v[180:183]\n"
    "s_waitcnt lgkmcnt(5)\n"
    "ds_read_b128 a[240:243], v245 offset:6144\n"
    "buffer_load_dwordx4 a[192:195], v254, s[4:7], s54 offen sc1\n"
    "v_mfma_f32_16x16x32_bf16 v[184:187], a[244:247], v[152:155], v[184:187]\n"
    "v_mul_f32 v231, 0xcca52b5c, v239\n"
    "v_mul_f32 v232, v236, v238\n"
    "v_mfma_f32_16x16x32_bf16 v[188:191], a[244:247], v[156:159], v[188:191]\n"
    "s_waitcnt vmcnt(10)\n"
    "ds_read_b128 a[244:247], v245 offset:7168\n"
    "v_mul_f32 v233, v237, v239\n"
    "v_lshlrev_b32 v244, 2, v244\n"
    "v_add_u32 v247, 122880, v254\n"
    "s_barrier\n"
    "s_add_u32 s54, s48, 36864\n"
    "buffer_load_dwordx4 a[196:199], v254, s[4:7], s54 offen sc1\n"
    "v_pk_mul_f32 v[228:229], v[160:161], v[230:231] op_sel:[0,0] op_sel_hi:[1,0]\n"
    "v_pk_mul_f32 v[234:235], v[162:163], v[230:231] op_sel:[0,0] op_sel_hi:[1,0]\n"
    "v_exp_f32 v228, v228\n"
    "v_exp_f32 v229, v229\n"
    "v_exp_f32 v234, v234\n"
    "v_exp_f32 v235, v235\n"
    "v_pk_fma_f32 v[228:229], v[228:229], v[232:233], v[232:233] op_sel:[0,0,0] op_sel_hi:[1,0,0]\n"
    "v_pk_fma_f32 v[234:235], v[234:235], v[232:233], v[232:233] op_sel:[0,0,0] op_sel_hi:[1,0,0]\n"
    "v_rcp_f32 v228, v228\n"
    "v_rcp_f32 v229, v229\n"
    "v_rcp_f32 v234, v234\n"
    "v_rcp_f32 v235, v235\n"
    "s_add_u32 s54, s48, 40960\n"
    "buffer_load_dwordx4 a[200:203], v254, s[4:7], s54 offen sc1\n"
    "v_pk_mul_f32 v[228:229], v[160:161], v[228:229]\n"
    "v_pk_mul_f32 v[234:235], v[162:163], v[234:235]\n"
    "v_cvt_pk_bf16_f32 v224, v228, v229\n"
    "v_cvt_pk_bf16_f32 v225, v234, v235\n"
    "v_pk_mul_f32 v[228:229], v[168:169], v[230:231] op_sel:[0,0] op_sel_hi:[1,0]\n"
    "v_pk_mul_f32 v[234:235], v[170:171], v[230:231] op_sel:[0,0] op_sel_hi:[1,0]\n"
    "v_exp_f32 v228, v228\n"
    "v_exp_f32 v229, v229\n"
    "v_exp_f32 v234, v234\n"
    "v_exp_f32 v235, v235\n"
    "v_pk_fma_f32 v[228:229], v[228:229], v[232:233], v[232:233] op_sel:[0,0,0] op_sel_hi:[1,0,0]\n"
    "v_pk_fma_f32 v[234:235], v[234:235], v[232:233], v[232:233] op_sel:[0,0,0] op_sel_hi:[1,0,0]\n"
    "v_rcp_f32 v228, v228\n"
    "v_rcp_f32 v229, v229\n"
    "s_add_u32 s54, s48, 45056\n"
    "buffer_load_dwordx4 a[204:207], v254, s[4:7], s54 offen sc1\n"
    "v_rcp_f32 v234, v234\n"
    "v_rcp_f32 v235, v235\n"
    "v_pk_mul_f32 v[228:229], v[168:169], v[228:229]\n"
    "v_pk_mul_f32 v[234:235], v[170:171], v[234:235]\n"
    "v_cvt_pk_bf16_f32 v226, v228, v229\n"
    "v_cvt_pk_bf16_f32 v227, v234, v235\n"
    "s_add_u32 s47, s43, 0\n"
    "buffer_store_dwordx4 v[224:227], v254, s[4:7], s47 offen sc1\n"
    "v_pk_mul_f32 v[228:229], v[164:165], v[230:231] op_sel:[0,1] op_sel_hi:[1,1]\n"
    "v_pk_mul_f32 v[234:235], v[166:167], v[230:231] op_sel:[0,1] op_sel_hi:[1,1]\n"
    "v_exp_f32 v228, v228\n"
    "v_exp_f32 v229, v229\n"
    "v_exp_f32 v234, v234\n"
    "v_exp_f32 v235, v235\n"
    "s_add_u32 m0, s53, 0\n"
    "s_add_u32 s54, s52, 1310720\n"
    "buffer_load_dwordx4 v249, s[8:11], s54 offen lds\n"
    "v_pk_fma_f32 v[228:229], v[228:229], v[232:233], v[232:233] op_sel:[0,1,1] op_sel_hi:[1,1,1]\n"
    "v_pk_fma_f32 v[234:235], v[234:235], v[232:233], v[232:233] op_sel:[0,1,1] op_sel_hi:[1,1,1]\n"
    "v_rcp_f32 v228, v228\n"
    "v_rcp_f32 v229, v229\n"
    "v_rcp_f32 v234, v234\n"
    "v_rcp_f32 v235, v235\n"
    "v_pk_mul_f32 v[228:229], v[164:165], v[228:229]\n"
    "v_pk_mul_f32 v[234:235], v[166:167], v[234:235]\n"
    "v_cvt_pk_bf16_f32 v236, v228, v229\n"
    "v_cvt_pk_bf16_f32 v237, v234, v235\n"
    "v_pk_mul_f32 v[228:229], v[172:173], v[230:231] op_sel:[0,1] op_sel_hi:[1,1]\n"
    "v_pk_mul_f32 v[234:235], v[174:175], v[230:231] op_sel:[0,1] op_sel_hi:[1,1]\n"
    "v_exp_f32 v228, v228\n"
    "v_exp_f32 v229, v229\n"
    "v_exp_f32 v234, v234\n"
    "v_exp_f32 v235, v235\n"
    "v_pk_fma_f32 v[228:229], v[228:229], v[232:233], v[232:233] op_sel:[0,1,1] op_sel_hi:[1,1,1]\n"
    "v_pk_fma_f32 v[234:235], v[234:235], v[232:233], v[232:233] op_sel:[0,1,1] op_sel_hi:[1,1,1]\n"
    "v_rcp_f32 v228, v228\n"
    "v_rcp_f32 v229, v229\n"
    "v_rcp_f32 v234, v234\n"
    "v_rcp_f32 v235, v235\n"
    "v_pk_mul_f32 v[228:229], v[172:173], v[228:229]\n"
    "v_pk_mul_f32 v[234:235], v[174:175], v[234:235]\n"
    "v_cvt_pk_bf16_f32 v238, v228, v229\n"
    "v_cvt_pk_bf16_f32 v239, v234, v235\n"
    "s_add_u32 s47, s43, 55296\n"
    "buffer_store_dwordx4 v[236:239], v254, s[4:7], s47 offen sc1\n"
    "v_mfma_f32_16x16x32_bf16 v[192:195], a[216:219], v[0:3], 0\n"
    "s_add_u32 m0, s53, 1024\n"
    "s_add_u32 s54, s52, 1320960\n"
    "buffer_load_dwordx4 v249, s[8:11], s54 offen lds\n"
    "v_mul_f32 v228, v176, v230\n"
    "v_mul_f32 v229, v177, v230\n"
    "v_mfma_f32_16x16x32_bf16 v[196:199], a[216:219], v[4:7], 0\n"
    "s_waitcnt lgkmcnt(5)\n"
    "ds_read_b128 a[216:219], v245 offset:8192\n"
    "s_nop 0\n"
    "v_mfma_f32_16x16x32_bf16 v[200:203], a[220:223], v[0:3], 0\n"
    "s_add_u32 m0, s53, 2048\n"
    "v_exp_f32 v228, v228\n"
    "v_mfma_f32_16x16x32_bf16 v[204:207], a[220:223], v[4:7], 0\n"
    "s_add_u32 s54, s52, 1331200\n"
    "ds_read_b128 a[220:223], v245 offset:9216\n"
    "v_fma_f32 v228, v228, v232, v232\n"
    "v_mfma_f32_16x16x32_bf16 v[192:195], a[224:227], v[8:11], v[192:195]\n"
    "v_rcp_f32 v228, v228\n"
    "v_mfma_f32_16x16x32_bf16 v[196:199], a[224:227], v[12:15], v[196:199]\n"
    "s_waitcnt lgkmcnt(5)\n"
    "ds_read_b128 a[224:227], v245 offset:10240\n"
    "buffer_load_dwordx4 v249, s[8:11], s54 offen lds\n"
    "v_mul_f32 v176, v176, v228\n"
    "v_mfma_f32_16x16x32_bf16 v[200:203], a[228:231], v[8:11], v[200:203]\n"
    "v_exp_f32 v229, v229\n"
    "v_mfma_f32_16x16x32_bf16 v[204:207], a[228:231], v[12:15], v[204:207]\n"
    "s_add_u32 m0, s53, 3072\n"
    "ds_read_b128 a[228:231], v245 offset:11264\n"
    "v_fma_f32 v229, v229, v232, v232\n"
    "v_mfma_f32_16x16x32_bf16 v[192:195], a[232:235], v[16:19], v[192:195]\n"
    "s_add_u32 s54, s52, 1341440\n"
    "v_rcp_f32 v229, v229\n"
    "v_mfma_f32_16x16x32_bf16 v[196:199], a[232:235], v[20:23], v[196:199]\n"
    "s_waitcnt lgkmcnt(5)\n"
    "ds_read_b128 a[232:235], v245 offset:12288\n"
    "v_mul_f32 v177, v177, v229\n"
    "v_mfma_f32_16x16x32_bf16 v[200:203], a[236:239], v[16:19], v[200:203]\n"
    "buffer_load_dwordx4 v249, s[8:11], s54 offen lds\n"
    "v_mul_f32 v228, v178, v230\n"
    "v_mul_f32 v229, v179, v230\n"
    "v_mfma_f32_16x16x32_bf16 v[204:207], a[236:239], v[20:23], v[204:207]\n"
    "ds_read_b128 a[236:239], v245 offset:13312\n"
    "v_cvt_pk_bf16_f32 v224, v176, v177\n"
    "v_mfma_f32_16x16x32_bf16 v[192:195], a[240:243], v[24:27], v[192:195]\n"
    "s_add_u32 m0, s53, 4096\n"
    "v_exp_f32 v228, v228\n"
    "v_mfma_f32_16x16x32_bf16 v[196:199], a[240:243], v[28:31], v[196:199]\n"
    "s_add_u32 s54, s52, 1474560\n"
    "ds_read_b128 a[240:243], v245 offset:14336\n"
    "v_fma_f32 v228, v228, v232, v232\n"
    "s_waitcnt lgkmcnt(6)\n"
    "v_mfma_f32_16x16x32_bf16 v[200:203], a[244:247], v[24:27], v[200:203]\n"
    "v_rcp_f32 v228, v228\n"
    "v_mfma_f32_16x16x32_bf16 v[204:207], a[244:247], v[28:31], v[204:207]\n"
    "ds_read_b128 a[244:247], v245 offset:15360\n"
    "buffer_load_dwordx4 v249, s[8:11], s54 offen lds\n"
    "v_mul_f32 v178, v178, v228\n"
    "v_mfma_f32_16x16x32_bf16 v[192:195], a[216:219], v[32:35], v[192:195]\n"
    "v_exp_f32 v229, v229\n"
    "v_mfma_f32_16x16x32_bf16 v[196:199], a[216:219], v[36:39], v[196:199]\n"
    "s_add_u32 m0, s53, 5120\n"
    "ds_read_b128 a[216:219], v245 offset:16384\n"
    "v_fma_f32 v229, v229, v232, v232\n"
    "s_waitcnt lgkmcnt(6)\n"
    "v_mfma_f32_16x16x32_bf16 v[200:203], a[220:223], v[32:35], v[200:203]\n"
    "s_add_u32 s54, s52, 1484800\n"
    "v_rcp_f32 v229, v229\n"
    "v_mfma_f32_16x16x32_bf16 v[204:207], a[220:223], v[36:39], v[204:207]\n"
    "ds_read_b128 a[220:223], v245 offset:17408\n"
    "v_mul_f32 v179, v179, v229\n"
    "v_mfma_f32_16x16x32_bf16 v[192:195], a[224:227], v[40:43], v[192:195]\n"
    "buffer_load_dwordx4 v249, s[8:11], s54 offen lds\n"
    "v_mul_f32 v228, v184, v230\n"
    "v_mul_f32 v229, v185, v230\n"
    "v_mfma_f32_16x16x32_bf16 v[196:199], a[224:227], v[44:47], v[196:199]\n"
    "s_waitcnt lgkmcnt(5)\n"
    "ds_read_b128 a[224:227], v245 offset:18432\n"
    "v_cvt_pk_bf16_f32 v225, v178, v179\n"
    "v_mfma_f32_16x16x32_bf16 v[200:203], a[228:231], v[40:43], v[200:203]\n"
    "s_add_u32 m0, s53, 6144\n"
    "v_exp_f32 v228, v228\n"
    "v_mfma_f32_16x16x32_bf16 v[204:207], a[228:231], v[44:47], v[204:207]\n"
    "s_add_u32 s54, s52, 1495040\n"
    "ds_read_b128 a[228:231], v245 offset:19456\n"
    "v_fma_f32 v228, v228, v232, v232\n"
    "v_mfma_f32_16x16x32_bf16 v[192:195], a[232:235], v[48:51], v[192:195]\n"
    "v_rcp_f32 v228, v228\n"
    "v_mfma_f32_16x16x32_bf16 v[196:199], a[232:235], v[52:55], v[196:199]\n"
    "s_waitcnt lgkmcnt(5)\n"
    "ds_read_b128 a[232:235], v245 offset:20480\n"
    "buffer_load_dwordx4 v249, s[8:11], s54 offen lds\n"
    "v_mul_f32 v184, v184, v228\n"
    "v_mfma_f32_16x16x32_bf16 v[200:203], a[236:239], v[48:51], v[200:203]\n"
    "v_exp_f32 v229, v229\n"
    "v_mfma_f32_16x16x32_bf16 v[204:207], a[236:239], v[52:55], v[204:207]\n"
    "s_add_u32 m0, s53, 7168\n"
    "ds_read_b128 a[236:239], v245 offset:21504\n"
    "v_fma_f32 v229, v229, v232, v232\n"
    "v_mfma_f32_16x16x32_bf16 v[192:195], a[240:243], v[56:59], v[192:195]\n"
    "s_add_u32 s54, s52, 1505280\n"
    "v_rcp_f32 v229, v229\n"
    "v_mfma_f32_16x16x32_bf16 v[196:199], a[240:243], v[60:63], v[196:199]\n"
    "s_waitcnt lgkmcnt(5)\n"
    "ds_read_b128 a[240:243], v245 offset:22528\n"
    "v_mul_f32 v185, v185, v229\n"
    "v_mfma_f32_16x16x32_bf16 v[200:203], a[244:247], v[56:59], v[200:203]\n"
    "buffer_load_dwordx4 v249, s[8:11], s54 offen lds\n"
    "v_mul_f32 v228, v186, v230\n"
    "v_mul_f32 v229, v187, v230\n"
    "v_mfma_f32_16x16x32_bf16 v[204:207], a[244:247], v[60:63], v[204:207]\n"
    "ds_read_b128 a[244:247], v245 offset:23552\n"
    "v_cvt_pk_bf16_f32 v226, v184, v185\n"
    "v_mfma_f32_16x16x32_bf16 v[192:195], a[216:219], v[64:67], v[192:195]\n"
    "s_add_u32 m0, s53, 8192\n"
    "v_exp_f32 v228, v228\n"
    "v_mfma_f32_16x16x32_bf16 v[196:199], a[216:219], v[68:71], v[196:199]\n"
    "s_add_u32 s54, s52, 1638400\n"
    "ds_read_b128 a[216:219], v245 offset:24576\n"
    "v_fma_f32 v228, v228, v232, v232\n"
    "s_waitcnt lgkmcnt(6)\n"
    "v_mfma_f32_16x16x32_bf16 v[200:203], a[220:223], v[64:67], v[200:203]\n"
    "v_rcp_f32 v228, v228\n"
    "v_mfma_f32_16x16x32_bf16 v[204:207], a[220:223], v[68:71], v[204:207]\n"
    "ds_read_b128 a[220:223], v245 offset:25600\n"
    "buffer_load_dwordx4 v249, s[8:11], s54 offen lds\n"
    "v_mul_f32 v186, v186, v228\n"
    "v_mfma_f32_16x16x32_bf16 v[192:195], a[224:227], v[72:75], v[192:195]\n"
    "v_exp_f32 v229, v229\n"
    "v_mfma_f32_16x16x32_bf16 v[196:199], a[224:227], v[76:79], v[196:199]\n"
    "s_add_u32 m0, s53, 9216\n"
    "ds_read_b128 a[224:227], v245 offset:26624\n"
    "v_fma_f32 v229, v229, v232, v232\n"
    "s_waitcnt lgkmcnt(6)\n"
    "v_mfma_f32_16x16x32_bf16 v[200:203], a[228:231], v[72:75], v[200:203]\n"
    "s_add_u32 s54, s52, 1648640\n"
    "v_rcp_f32 v229, v229\n"
    "v_mfma_f32_16x16x32_bf16 v[204:207], a[228:231], v[76:79], v[204:207]\n"
    "ds_read_b128 a[228:231], v245 offset:27648\n"
    "v_mul_f32 v187, v187, v229\n"
    "v_mfma_f32_16x16x32_bf16 v[192:195], a[232:235], v[80:83], v[192:195]\n"
    "s_add_u32 s47, s45, 0\n"
    "buffer_load_dwordx4 v249, s[8:11], s54 offen lds\n"
    "v_mul_f32 v228, v180, v231\n"
    "v_mul_f32 v229, v181, v231\n"
    "v_mfma_f32_16x16x32_bf16 v[196:199], a[232:235], v[84:87], v[196:199]\n"
    "s_waitcnt vmcnt(9)\n"
    "ds_read_b128 a[232:235], v245 offset:28672\n"
    "buffer_store_dword v253, v244, s[4:7], s47 offen sc1\n"
    "v_cvt_pk_bf16_f32 v227, v186, v187\n"
    "s_waitcnt lgkmcnt(6)\n"
    "v_mfma_f32_16x16x32_bf16 v[200:203], a[236:239], v[80:83], v[200:203]\n"
    "s_add_u32 m0, s53, 10240\n"
    "s_add_u32 s47, s43, 16384\n"
    "buffer_store_dwordx4 v[224:227], v254, s[4:7], s47 offen sc1\n"
    "v_exp_f32 v228, v228\n"
    "v_mfma_f32_16x16x32_bf16 v[204:207], a[236:239], v[84:87], v[204:207]\n"
    "s_add_u32 s54, s52, 1658880\n"
    "ds_read_b128 a[236:239], v245 offset:29696\n"
    "v_fma_f32 v228, v228, v233, v233\n"
    "v_mfma_f32_16x16x32_bf16 v[192:195], a[240:243], v[88:91], v[192:195]\n"
    "v_rcp_f32 v228, v228\n"
    "v_mfma_f32_16x16x32_bf16 v[196:199], a[240:243], v[92:95], v[196:199]\n"
    "s_waitcnt lgkmcnt(5)\n"
    "ds_read_b128 a[240:243], v245 offset:30720\n"
    "buffer_load_dwordx4 v249, s[8:11], s54 offen lds\n"
    "v_mul_f32 v180, v180, v228\n"
    "v_mfma_f32_16x16x32_bf16 v[200:203], a[244:247], v[88:91], v[200:203]\n"
    "v_exp_f32 v229, v229\n"
    "v_mfma_f32_16x16x32_bf16 v[204:207], a[244:247], v[92:95], v[204:207]\n"
    "s_add_u32 m0, s53, 11264\n"
    "ds_read_b128 a[244:247], v245 offset:31744\n"
    "v_fma_f32 v229, v229, v233, v233\n"
    "v_mfma_f32_16x16x32_bf16 v[192:195], a[216:219], v[96:99], v[192:195]\n"
    "s_add_u32 s54, s52, 1669120\n"
    "v_rcp_f32 v229, v229\n"
    "v_mfma_f32_16x16x32_bf16 v[196:199], a[216:219], v[100:103], v[196:199]\n"
    "s_waitcnt lgkmcnt(5)\n"
    "ds_read_b128 a[216:219], v245 offset:32768\n"
    "v_mul_f32 v181, v181, v229\n"
    "v_mfma_f32_16x16x32_bf16 v[200:203], a[220:223], v[96:99], v[200:203]\n"
    "buffer_load_dwordx4 v249, s[8:11], s54 offen lds\n"
    "v_mul_f32 v228, v182, v231\n"
    "v_mul_f32 v229, v183, v231\n"
    "v_mfma_f32_16x16x32_bf16 v[204:207], a[220:223], v[100:103], v[204:207]\n"
    "ds_read_b128 a[220:223], v245 offset:33792\n"
    "v_cvt_pk_bf16_f32 v236, v180, v181\n"
    "v_mfma_f32_16x16x32_bf16 v[192:195], a[224:227], v[104:107], v[192:195]\n"
    "s_add_u32 m0, s53, 12288\n"
    "v_exp_f32 v228, v228\n"
    "v_mfma_f32_16x16x32_bf16 v[196:199], a[224:227], v[108:111], v[196:199]\n"
    "s_add_u32 s54, s52, 1802240\n"
    "ds_read_b128 a[224:227], v245 offset:34816\n"
    "v_fma_f32 v228, v228, v233, v233\n"
    "s_waitcnt lgkmcnt(6)\n"
    "v_mfma_f32_16x16x32_bf16 v[200:203], a[228:231], v[104:107], v[200:203]\n"
    "v_rcp_f32 v228, v228\n"
    "v_mfma_f32_16x16x32_bf16 v[204:207], a[228:231], v[108:111], v[204:207]\n"
    "ds_read_b128 a[228:231], v245 offset:35840\n"
    "buffer_load_dwordx4 v249, s[8:11], s54 offen lds\n"
    "v_mul_f32 v182, v182, v228\n"
    "v_mfma_f32_16x16x32_bf16 v[192:195], a[232:235], v[112:115], v[192:195]\n"
    "v_exp_f32 v229, v229\n"
    "v_mfma_f32_16x16x32_bf16 v[196:199], a[232:235], v[116:119], v[196:199]\n"
    "s_add_u32 m0, s53, 13312\n"
    "ds_read_b128 a[232:235], v245 offset:36864\n"
    "v_fma_f32 v229, v229, v233, v233\n"
    "s_waitcnt lgkmcnt(6)\n"
    "v_mfma_f32_16x16x32_bf16 v[200:203], a[236:239], v[112:115], v[200:203]\n"
    "s_add_u32 s54, s52, 1812480\n"
    "v_rcp_f32 v229, v229\n"
    "v_mfma_f32_16x16x32_bf16 v[204:207], a[236:239], v[116:119], v[204:207]\n"
    "ds_read_b128 a[236:239], v245 offset:37888\n"
    "v_mul_f32 v183, v183, v229\n"
    "v_mfma_f32_16x16x32_bf16 v[192:195], a[240:243], v[120:123], v[192:195]\n"
    "buffer_load_dwordx4 v249, s[8:11], s54 offen lds\n"
    "v_mul_f32 v228, v188, v231\n"
    "v_mul_f32 v229, v189, v231\n"
    "v_mfma_f32_16x16x32_bf16 v[196:199], a[240:243], v[124:127], v[196:199]\n"
    "s_waitcnt lgkmcnt(5)\n"
    "ds_read_b128 a[240:243], v245 offset:38912\n"
    "v_cvt_pk_bf16_f32 v237, v182, v183\n"
    "v_mfma_f32_16x16x32_bf16 v[200:203], a[244:247], v[120:123], v[200:203]\n"
    "s_add_u32 m0, s53, 14336\n"
    "v_exp_f32 v228, v228\n"
    "v_mfma_f32_16x16x32_bf16 v[204:207], a[244:247], v[124:127], v[204:207]\n"
    "s_add_u32 s54, s52, 1822720\n"
    "ds_read_b128 a[244:247], v245 offset:39936\n"
    "v_fma_f32 v228, v228, v233, v233\n"
    "v_mfma_f32_16x16x32_bf16 v[192:195], a[216:219], v[128:131], v[192:195]\n"
    "v_rcp_f32 v228, v228\n"
    "v_mfma_f32_16x16x32_bf16 v[196:199], a[216:219], v[132:135], v[196:199]\n"
    "s_waitcnt lgkmcnt(5)\n"
    "ds_read_b128 a[216:219], v245 offset:40960\n"
    "buffer_load_dwordx4 v249, s[8:11], s54 offen lds\n"
    "v_mul_f32 v188, v188, v228\n"
    "v_mfma_f32_16x16x32_bf16 v[200:203], a[220:223], v[128:131], v[200:203]\n"
    "v_exp_f32 v229, v229\n"
    "v_mfma_f32_16x16x32_bf16 v[204:207], a[220:223], v[132:135], v[204:207]\n"
    "s_add_u32 m0, s53, 15360\n"
    "ds_read_b128 a[220:223], v245 offset:41984\n"
    "v_fma_f32 v229, v229, v233, v233\n"
    "v_mfma_f32_16x16x32_bf16 v[192:195], a[224:227], v[136:139], v[192:195]\n"
    "s_add_u32 s54, s52, 1832960\n"
    "v_rcp_f32 v229, v229\n"
    "v_mfma_f32_16x16x32_bf16 v[196:199], a[224:227], v[140:143], v[196:199]\n"
    "s_waitcnt lgkmcnt(5)\n"
    "ds_read_b128 a[224:227], v245 offset:43008\n"
    "v_mul_f32 v189, v189, v229\n"
    "v_mfma_f32_16x16x32_bf16 v[200:203], a[228:231], v[136:139], v[200:203]\n"
    "buffer_load_dwordx4 v249, s[8:11], s54 offen lds\n"
    "v_mul_f32 v228, v190, v231\n"
    "v_mul_f32 v229, v191, v231\n"
    "v_mfma_f32_16x16x32_bf16 v[204:207], a[228:231], v[140:143], v[204:207]\n"
    "ds_read_b128 a[228:231], v245 offset:44032\n"
    "v_cvt_pk_bf16_f32 v238, v188, v189\n"
    "v_mfma_f32_16x16x32_bf16 v[192:195], a[232:235], v[144:147], v[192:195]\n"
    "v_exp_f32 v228, v228\n"
    ".Lpad6:\n"
    "s_cbranch_execz .Lw_ret\n"
    "v_mfma_f32_16x16x32_bf16 v[196:199], a[232:235], v[148:151], v[196:199]\n"
    "s_waitcnt lgkmcnt(5)\n"
    "ds_read_b128 a[232:235], v245 offset:45056\n"
    "v_fma_f32 v228, v228, v233, v233\n"
    "v_mfma_f32_16x16x32_bf16 v[200:203], a[236:239], v[144:147], v[200:203]\n"
    "v_rcp_f32 v228, v228\n"
    "v_mfma_f32_16x16x32_bf16 v[204:207], a[236:239], v[148:151], v[204:207]\n"
    "ds_read_b128 a[236:239], v245 offset:46080\n"
    "v_mul_f32 v190, v190, v228\n"
    "v_mfma_f32_16x16x32_bf16 v[192:195], a[240:243], v[152:155], v[192:195]\n"
    "v_exp_f32 v229, v229\n"
    "v_mfma_f32_16x16x32_bf16 v[196:199], a[240:243], v[156:159], v[196:199]\n"
    "s_waitcnt lgkmcnt(5)\n"
    "ds_read_b128 a[240:243], v245 offset:47104\n"
    "v_fma_f32 v229, v229, v233, v233\n"
    "v_mfma_f32_16x16x32_bf16 v[200:203], a[244:247], v[152:155], v[200:203]\n"
    "v_rcp_f32 v229, v229\n"
    "v_mfma_f32_16x16x32_bf16 v[204:207], a[244:247], v[156:159], v[204:207]\n"
    "s_add_u32 s47, s43, 71680\n"
    "ds_read_b128 a[244:247], v245 offset:48128\n"
    "v_mul_f32 v191, v191, v229\n"
    "v_cvt_pk_bf16_f32 v239, v190, v191\n"
    "buffer_store_dwordx4 v[236:239], v254, s[4:7], s47 offen sc1\n"
    "s_barrier\n"
    "s_lshl_b32 s55, s3, 10\n"
    "s_mul_i32 s56, s3, 30720\n"
    "s_sub_u32 s56, s52, s56\n"
    "s_add_u32 m0, s55, 81920\n"
    "s_add_u32 s54, s56, 1966080\n"
    "buffer_load_dwordx4 v249, s[8:11], s54 offen lds\n"
    "v_pk_mul_f32 v[228:229], v[192:193], v[230:231] op_sel:[0,0] op_sel_hi:[1,0]\n"
    "v_pk_mul_f32 v[234:235], v[194:195], v[230:231] op_sel:[0,0] op_sel_hi:[1,0]\n"
    "v_exp_f32 v228, v228\n"
    "v_exp_f32 v229, v229\n"
    "v_exp_f32 v234, v234\n"
    "v_exp_f32 v235, v235\n"
    "v_pk_fma_f32 v[228:229], v[228:229], v[232:233], v[232:233] op_sel:[0,0,0] op_sel_hi:[1,0,0]\n"
    "v_pk_fma_f32 v[234:235], v[234:235], v[232:233], v[232:233] op_sel:[0,0,0] op_sel_hi:[1,0,0]\n"
    "v_rcp_f32 v228, v228\n"
    "v_rcp_f32 v229, v229\n"
    "v_rcp_f32 v234, v234\n"
    "v_rcp_f32 v235, v235\n"
    "s_add_u32 m0, s55, 86016\n"
    "s_add_u32 s54, s56, 2007040\n"
    "buffer_load_dwordx4 v249, s[8:11], s54 offen lds\n"
    "v_pk_mul_f32 v[228:229], v[192:193], v[228:229]\n"
    "v_pk_mul_f32 v[234:235], v[194:195], v[234:235]\n"
    "v_cvt_pk_bf16_f32 v224, v228, v229\n"
    "v_cvt_pk_bf16_f32 v225, v234, v235\n"
    "v_pk_mul_f32 v[228:229], v[200:201], v[230:231] op_sel:[0,0] op_sel_hi:[1,0]\n"
    "v_pk_mul_f32 v[234:235], v[202:203], v[230:231] op_sel:[0,0] op_sel_hi:[1,0]\n"
    "v_exp_f32 v228, v228\n"
    "v_exp_f32 v229, v229\n"
    "v_exp_f32 v234, v234\n"
    "v_exp_f32 v235, v235\n"
    "v_pk_fma_f32 v[228:229], v[228:229], v[232:233], v[232:233] op_sel:[0,0,0] op_sel_hi:[1,0,0]\n"
    "v_pk_fma_f32 v[234:235], v[234:235], v[232:233], v[232:233] op_sel:[0,0,0] op_sel_hi:[1,0,0]\n"
    "v_rcp_f32 v228, v228\n"
    "v_rcp_f32 v229, v229\n"
    "s_add_u32 m0, s55, 90112\n"
    "s_add_u32 s54, s56, 2048000\n"
    "buffer_load_dwordx4 v249, s[8:11], s54 offen lds\n"
    "v_rcp_f32 v234, v234\n"
    "v_rcp_f32 v235, v235\n"
    "v_pk_mul_f32 v[228:229], v[200:201], v[228:229]\n"
    "v_pk_mul_f32 v[234:235], v[202:203], v[234:235]\n"
    "v_cvt_pk_bf16_f32 v226, v228, v229\n"
    "v_cvt_pk_bf16_f32 v227, v234, v235\n"
    "s_add_u32 s47, s43, 32768\n"
    "buffer_store_dwordx4 v[224:227], v254, s[4:7], s47 offen sc1\n"
    "v_pk_mul_f32 v[228:229], v[196:197], v[230:231] op_sel:[0,1] op_sel_hi:[1,1]\n"
    "v_pk_mul_f32 v[234:235], v[198:199], v[230:231] op_sel:[0,1] op_sel_hi:[1,1]\n"
    "v_exp_f32 v228, v228\n"
    "v_exp_f32 v229, v229\n"
    "v_exp_f32 v234, v234\n"
    "v_exp_f32 v235, v235\n"
    "s_add_u32 m0, s55, 94208\n"
    "s_add_u32 s54, s56, 2088960\n"
    "buffer_load_dwordx4 v249, s[8:11], s54 offen lds\n"
    "v_pk_fma_f32 v[228:229], v[228:229], v[232:233], v[232:233] op_sel:[0,1,1] op_sel_hi:[1,1,1]\n"
    "v_pk_fma_f32 v[234:235], v[234:235], v[232:233], v[232:233] op_sel:[0,1,1] op_sel_hi:[1,1,1]\n"
    "v_rcp_f32 v228, v228\n"
    "v_rcp_f32 v229, v229\n"
    "v_rcp_f32 v234, v234\n"
    "v_rcp_f32 v235, v235\n"
    "v_pk_mul_f32 v[228:229], v[196:197], v[228:229]\n"
    "v_pk_mul_f32 v[234:235], v[198:199], v[234:235]\n"
    "v_cvt_pk_bf16_f32 v236, v228, v229\n"
    "v_cvt_pk_bf16_f32 v237, v234, v235\n"
    "v_pk_mul_f32 v[228:229], v[204:205], v[230:231] op_sel:[0,1] op_sel_hi:[1,1]\n"
    "v_pk_mul_f32 v[234:235], v[206:207], v[230:231] op_sel:[0,1] op_sel_hi:[1,1]\n"
    "v_exp_f32 v228, v228\n"
    "v_exp_f32 v229, v229\n"
    "s_add_u32 m0, s55, 98304\n"
    "s_add_u32 s54, s56, 2129920\n"
    "buffer_load_dwordx4 v249, s[8:11], s54 offen lds\n"
    "v_exp_f32 v234, v234\n"
    "v_exp_f32 v235, v235\n"
    "v_pk_fma_f32 v[228:229], v[228:229], v[232:233], v[232:233] op_sel:[0,1,1] op_sel_hi:[1,1,1]\n"
    "v_pk_fma_f32 v[234:235], v[234:235], v[232:233], v[232:233] op_sel:[0,1,1] op_sel_hi:[1,1,1]\n"
    "v_rcp_f32 v228, v228\n"
    "v_rcp_f32 v229, v229\n"
    "v_rcp_f32 v234, v234\n"
    "v_rcp_f32 v235, v235\n"
    "v_pk_mul_f32 v[228:229], v[204:205], v[228:229]\n"
    "v_pk_mul_f32 v[234:235], v[206:207], v[234:235]\n"
    "v_cvt_pk_bf16_f32 v238, v228, v229\n"
    "v_cvt_pk_bf16_f32 v239, v234, v235\n"
    "s_add_u32 s47, s43, 88064\n"
    "buffer_store_dwordx4 v[236:239], v254, s[4:7], s47 offen sc1\n"
    "s_add_u32 m0, s55, 102400\n"
    "s_add_u32 s54, s56, 2170880\n"
    "buffer_load_dwordx4 v249, s[8:11], s54 offen lds\n"
    "v_mfma_f32_16x16x32_bf16 v[160:163], a[216:219], v[0:3], 0\n"
    "v_mfma_f32_16x16x32_bf16 v[164:167], a[216:219], v[4:7], 0\n"
    "s_waitcnt lgkmcnt(5)\n"
    "ds_read_b128 a[216:219], v245 offset:49152\n"
    "v_mfma_f32_16x16x32_bf16 v[168:171], a[220:223], v[0:3], 0\n"
    "s_add_u32 s38, s46, 0\n"
    "buffer_load_dword v246, v248, s[4:7], s38 offen sc1\n"
    "v_mfma_f32_16x16x32_bf16 v[172:175], a[220:223], v[4:7], 0\n"
    "ds_read_b128 a[220:223], v245 offset:50176\n"
    "v_mfma_f32_16x16x32_bf16 v[160:163], a[224:227], v[8:11], v[160:163]\n"
    "v_mfma_f32_16x16x32_bf16 v[164:167], a[224:227], v[12:15], v[164:167]\n"
    "s_waitcnt lgkmcnt(5)\n"
    "ds_read_b128 a[224:227], v245 offset:51200\n"
    "v_mfma_f32_16x16x32_bf16 v[168:171], a[228:231], v[8:11], v[168:171]\n"
    "v_mfma_f32_16x16x32_bf16 v[172:175], a[228:231], v[12:15], v[172:175]\n"
    "ds_read_b128 a[228:231], v245 offset:52224\n"
    "v_mfma_f32_16x16x32_bf16 v[160:163], a[232:235], v[16:19], v[160:163]\n"
    "v_mfma_f32_16x16x32_bf16 v[164:167], a[232:235], v[20:23], v[164:167]\n"
    "s_waitcnt lgkmcnt(5)\n"
    "ds_read_b128 a[232:235], v245 offset:53248\n"
    "v_mfma_f32_16x16x32_bf16 v[168:171], a[236:239], v[16:19], v[168:171]\n"
    "v_mfma_f32_16x16x32_bf16 v[172:175], a[236:239], v[20:23], v[172:175]\n"
    "ds_read_b128 a[236:239], v245 offset:54272\n"
    "v_mfma_f32_16x16x32_bf16 v[160:163], a[240:243], v[24:27], v[160:163]\n"
    "v_mfma_f32_16x16x32_bf16 v[164:167], a[240:243], v[28:31], v[164:167]\n"
    "s_waitcnt lgkmcnt(5)\n"
    "ds_read_b128 a[240:243], v245 offset:55296\n"
    "v_mfma_f32_16x16x32_bf16 v[168:171], a[244:247], v[24:27], v[168:171]\n"
    "v_mfma_f32_16x16x32_bf16 v[172:175], a[244:247], v[28:31], v[172:175]\n"
    "ds_read_b128 a[244:247], v245 offset:56320\n"
    "v_mfma_f32_16x16x32_bf16 v[160:163], a[216:219], v[32:35], v[160:163]\n"
    "v_mfma_f32_16x16x32_bf16 v[164:167], a[216:219], v[36:39], v[164:167]\n"
    "s_waitcnt lgkmcnt(5)\n"
    "ds_read_b128 a[216:219], v245 offset:57344\n"
    "v_mfma_f32_16x16x32_bf16 v[168:171], a[220:223], v[32:35], v[168:171]\n"
    "v_mfma_f32_16x16x32_bf16 v[172:175], a[220:223], v[36:39], v[172:175]\n"
    "ds_read_b128 a[220:223], v245 offset:58368\n"
    "v_mfma_f32_16x16x32_bf16 v[160:163], a[224:227], v[40:43], v[160:163]\n"
    "v_mfma_f32_16x16x32_bf16 v[164:167], a[224:227], v[44:47], v[164:167]\n"
    "s_waitcnt lgkmcnt(5)\n"
    "ds_read_b128 a[224:227], v245 offset:59392\n"
    "v_mfma_f32_16x16x32_bf16 v[168:171], a[228:231], v[40:43], v[168:171]\n"
    "v_mfma_f32_16x16x32_bf16 v[172:175], a[228:231], v[44:47], v[172:175]\n"
    "ds_read_b128 a[228:231], v245 offset:60416\n"
    "v_mfma_f32_16x16x32_bf16 v[160:163], a[232:235], v[48:51], v[160:163]\n"
    "v_mfma_f32_16x16x32_bf16 v[164:167], a[232:235], v[52:55], v[164:167]\n"
    "s_waitcnt lgkmcnt(5)\n"
    "ds_read_b128 a[232:235], v245 offset:61440\n"
    "v_mfma_f32_16x16x32_bf16 v[168:171], a[236:239], v[48:51], v[168:171]\n"
    "v_mfma_f32_16x16x32_bf16 v[172:175], a[236:239], v[52:55], v[172:175]\n"
    "ds_read_b128 a[236:239], v245 offset:62464\n"
    "v_mfma_f32_16x16x32_bf16 v[160:163], a[240:243], v[56:59], v[160:163]\n"
    "v_mfma_f32_16x16x32_bf16 v[164:167], a[240:243], v[60:63], v[164:167]\n"
    "s_waitcnt lgkmcnt(5)\n"
    "ds_read_b128 a[240:243], v245 offset:63488\n"
    "v_mfma_f32_16x16x32_bf16 v[168:171], a[244:247], v[56:59], v[168:171]\n"
    "v_mfma_f32_16x16x32_bf16 v[172:175], a[244:247], v[60:63], v[172:175]\n"
    "ds_read_b128 a[244:247], v245 offset:64512\n"
    "v_mfma_f32_16x16x32_bf16 v[160:163], a[216:219], v[64:67], v[160:163]\n"
    "s_add_u32 s47, s45, 4096\n"
    "v_mfma_f32_16x16x32_bf16 v[164:167], a[216:219], v[68:71], v[164:167]\n"
    "s_waitcnt lgkmcnt(5)\n"
    "ds_read_b128 a[216:219], v247 offset:24576\n"
    "v_mfma_f32_16x16x32_bf16 v[168:171], a[220:223], v[64:67], v[168:171]\n"
    "s_waitcnt vmcnt(9)\n"
    "buffer_store_dword v253, v244, s[4:7], s47 offen sc1\n"
    "v_mfma_f32_16x16x32_bf16 v[172:175], a[220:223], v[68:71], v[172:175]\n"
    "ds_read_b128 a[220:223], v247 offset:25600\n"
    "v_mfma_f32_16x16x32_bf16 v[160:163], a[224:227], v[72:75], v[160:163]\n"
    "v_mfma_f32_16x16x32_bf16 v[164:167], a[224:227], v[76:79], v[164:167]\n"
    "s_waitcnt lgkmcnt(5)\n"
    "ds_read_b128 a[224:227], v247 offset:26624\n"
    "v_mfma_f32_16x16x32_bf16 v[168:171], a[228:231], v[72:75], v[168:171]\n"
    "v_mfma_f32_16x16x32_bf16 v[172:175], a[228:231], v[76:79], v[172:175]\n"
    "ds_read_b128 a[228:231], v247 offset:27648\n"
    "v_mfma_f32_16x16x32_bf16 v[208:211], a[232:235], v[80:83], 0\n"
    "v_mfma_f32_16x16x32_bf16 v[212:215], a[232:235], v[84:87], 0\n"
    "s_waitcnt vmcnt(1)\n"
    "ds_read_b128 a[232:235], v247 offset:28672\n"
    "v_cmp_ne_u32 vcc, v246, v253\n"
    "s_nop 0\n"
    "s_and_b32 s58, vcc_lo, 0xffffffff\n"
    "s_cbranch_scc1 .Lp5\n"
    ".Lpr5:\n"
    "s_waitcnt lgkmcnt(6)\n"
    "v_mfma_f32_16x16x32_bf16 v[216:219], a[236:239], v[80:83], 0\n"
    "s_add_u32 s36, s37, 0\n"
    "v_mfma_f32_16x16x32_bf16 v[220:223], a[236:239], v[84:87], 0\n"
    "ds_read_b128 a[236:239], v247 offset:29696\n"
    "v_mfma_f32_16x16x32_bf16 v[208:211], a[240:243], v[88:91], v[208:211]\n"
    "buffer_load_dwordx4 v[0:3], v240, s[4:7], s36 offen sc1\n"
    "v_mfma_f32_16x16x32_bf16 v[212:215], a[240:243], v[92:95], v[212:215]\n"
    "s_waitcnt lgkmcnt(5)\n"
    "ds_read_b128 a[240:243], v247 offset:30720\n"
    "v_mfma_f32_16x16x32_bf16 v[216:219], a[244:247], v[88:91], v[216:219]\n"
    "s_add_u32 s47, s45, 8192\n"
    "buffer_store_dword v253, v244, s[4:7], s47 offen sc1\n"
    "v_mfma_f32_16x16x32_bf16 v[220:223], a[244:247], v[92:95], v[220:223]\n"
    "ds_read_b128 a[244:247], v247 offset:31744\n"
    "v_mfma_f32_16x16x32_bf16 v[208:211], a[216:219], v[96:99], v[208:211]\n"
    "buffer_load_dwordx4 v[4:7], v241, s[4:7], s36 offen sc1\n"
    "v_mfma_f32_16x16x32_bf16 v[212:215], a[216:219], v[100:103], v[212:215]\n"
    "s_waitcnt lgkmcnt(5)\n"
    "ds_read_b128 a[216:219], v247 offset:32768\n"
    "v_mfma_f32_16x16x32_bf16 v[216:219], a[220:223], v[96:99], v[216:219]\n"
    "v_mfma_f32_16x16x32_bf16 v[220:223], a[220:223], v[100:103], v[220:223]\n"
    "ds_read_b128 a[220:223], v247 offset:33792\n"
    "v_mfma_f32_16x16x32_bf16 v[208:211], a[224:227], v[104:107], v[208:211]\n"
    "buffer_load_dwordx4 v[8:11], v242, s[4:7], s36 offen sc1\n"
    "v_mfma_f32_16x16x32_bf16 v[212:215], a[224:227], v[108:111], v[212:215]\n"
    "s_waitcnt lgkmcnt(5)\n"
    "ds_read_b128 a[224:227], v247 offset:34816\n"
    "v_mfma_f32_16x16x32_bf16 v[216:219], a[228:231], v[104:107], v[216:219]\n"
    "v_mfma_f32_16x16x32_bf16 v[220:223], a[228:231], v[108:111], v[220:223]\n"
    "ds_read_b128 a[228:231], v247 offset:35840\n"
    "v_mfma_f32_16x16x32_bf16 v[208:211], a[232:235], v[112:115], v[208:211]\n"
    "buffer_load_dwordx4 v[12:15], v243, s[4:7], s36 offen sc1\n"
    "v_mfma_f32_16x16x32_bf16 v[212:215], a[232:235], v[116:119], v[212:215]\n"
    "s_waitcnt lgkmcnt(5)\n"
    "ds_read_b128 a[232:235], v247 offset:36864\n"
    "v_mfma_f32_16x16x32_bf16 v[216:219], a[236:239], v[112:115], v[216:219]\n"
    "s_add_u32 s36, s37, 4096\n"
    "v_mfma_f32_16x16x32_bf16 v[220:223], a[236:239], v[116:119], v[220:223]\n"
    "ds_read_b128 a[236:239], v247 offset:37888\n"
    "v_mfma_f32_16x16x32_bf16 v[208:211], a[240:243], v[120:123], v[208:211]\n"
    "buffer_load_dwordx4 v[16:19], v240, s[4:7], s36 offen sc1\n"
    "v_mfma_f32_16x16x32_bf16 v[212:215], a[240:243], v[124:127], v[212:215]\n"
    "s_waitcnt lgkmcnt(5)\n"
    "ds_read_b128 a[240:243], v247 offset:38912\n"
    "v_mfma_f32_16x16x32_bf16 v[216:219], a[244:247], v[120:123], v[216:219]\n"
    "v_mfma_f32_16x16x32_bf16 v[220:223], a[244:247], v[124:127], v[220:223]\n"
    "ds_read_b128 a[244:247], v247 offset:39936\n"
    "v_mfma_f32_16x16x32_bf16 v[208:211], a[216:219], v[128:131], v[208:211]\n"
    "buffer_load_dwordx4 v[20:23], v241, s[4:7], s36 offen sc1\n"
    "v_mfma_f32_16x16x32_bf16 v[212:215], a[216:219], v[132:135], v[212:215]\n"
    "s_waitcnt lgkmcnt(5)\n"
    "v_mfma_f32_16x16x32_bf16 v[216:219], a[220:223], v[128:131], v[216:219]\n"
    "v_mfma_f32_16x16x32_bf16 v[220:223], a[220:223], v[132:135], v[220:223]\n"
    "v_mfma_f32_16x16x32_bf16 v[208:211], a[224:227], v[136:139], v[208:211]\n"
    "buffer_load_dwordx4 v[24:27], v242, s[4:7], s36 offen sc1\n"
    "v_mfma_f32_16x16x32_bf16 v[212:215], a[224:227], v[140:143], v[212:215]\n"
    "s_waitcnt lgkmcnt(3)\n"
    "v_mfma_f32_16x16x32_bf16 v[216:219], a[228:231], v[136:139], v[216:219]\n"
    "v_mfma_f32_16x16x32_bf16 v[220:223], a[228:231], v[140:143], v[220:223]\n"
    "v_mfma_f32_16x16x32_bf16 v[208:211], a[232:235], v[144:147], v[208:211]\n"
    "buffer_load_dwordx4 v[28:31], v243, s[4:7], s36 offen sc1\n"
    "v_mfma_f32_16x16x32_bf16 v[212:215], a[232:235], v[148:151], v[212:215]\n"
    "s_waitcnt lgkmcnt(1)\n"
    "v_mfma_f32_16x16x32_bf16 v[216:219], a[236:239], v[144:147], v[216:219]\n"
    "s_add_u32 s36, s37, 8192\n"
    "v_mfma_f32_16x16x32_bf16 v[220:223], a[236:239], v[148:151], v[220:223]\n"
    "v_mfma_f32_16x16x32_bf16 v[208:211], a[240:243], v[152:155], v[208:211]\n"
    "buffer_load_dwordx4 v[32:35], v240, s[4:7], s36 offen sc1\n"
    "v_mfma_f32_16x16x32_bf16 v[212:215], a[240:243], v[156:159], v[212:215]\n"
    "s_waitcnt lgkmcnt(0)\n"
    "v_mfma_f32_16x16x32_bf16 v[216:219], a[244:247], v[152:155], v[216:219]\n"
    "v_mfma_f32_16x16x32_bf16 v[220:223], a[244:247], v[156:159], v[220:223]\n"
    "buffer_load_dwordx4 v[36:39], v241, s[4:7], s36 offen sc1\n"
    "buffer_load_dwordx4 v[40:43], v242, s[4:7], s36 offen sc1\n"
    "buffer_load_dwordx4 v[44:47], v243, s[4:7], s36 offen sc1\n"
    "s_add_u32 s36, s37, 12288\n"
    "buffer_load_dwordx4 v[48:51], v240, s[4:7], s36 offen sc1\n"
    "s_cmp_ge_u32 s29, 20\n"
    "s_cbranch_scc1 .Lg_ffn\n"
    "v_mov_b32 v228, 0x4c64f92e\n"
    "v_mov_b32 v229, 0x4c64f92e\n"
    "s_add_u32 s48, s44, 0\n"
    "s_add_u32 s49, s44, 20480\n"
    "s_add_u32 s50, s44, 10240\n"
    "s_add_u32 s51, s44, 30720\n"
    "v_pk_mul_f32 v[160:161], v[160:161], v[228:229]\n"
    "v_pk_mul_f32 v[162:163], v[162:163], v[228:229]\n"
    "v_pk_mul_f32 v[164:165], v[164:165], v[228:229]\n"
    "v_pk_mul_f32 v[166:167], v[166:167], v[228:229]\n"
    "v_pk_mul_f32 v[168:169], v[168:169], v[228:229]\n"
    "v_pk_mul_f32 v[170:171], v[170:171], v[228:229]\n"
    "v_pk_mul_f32 v[172:173], v[172:173], v[228:229]\n"
    "v_pk_mul_f32 v[174:175], v[174:175], v[228:229]\n"
    "v_pk_fma_f32 v[176:177], v[208:209], v[228:229], v[160:161]\n"
    "v_pk_fma_f32 v[208:209], v[208:209], v[228:229], v[160:161] neg_lo:[1,0,0] neg_hi:[1,0,0]\n"
    "v_pk_fma_f32 v[178:179], v[210:211], v[228:229], v[162:163]\n"
    "v_pk_fma_f32 v[210:211], v[210:211], v[228:229], v[162:163] neg_lo:[1,0,0] neg_hi:[1,0,0]\n"
    "v_pk_fma_f32 v[180:181], v[212:213], v[228:229], v[164:165]\n"
    "v_pk_fma_f32 v[212:213], v[212:213], v[228:229], v[164:165] neg_lo:[1,0,0] neg_hi:[1,0,0]\n"
    "v_pk_fma_f32 v[182:183], v[214:215], v[228:229], v[166:167]\n"
    "v_pk_fma_f32 v[214:215], v[214:215], v[228:229], v[166:167] neg_lo:[1,0,0] neg_hi:[1,0,0]\n"
    "v_pk_fma_f32 v[184:185], v[216:217], v[228:229], v[168:169]\n"
    "v_pk_fma_f32 v[216:217], v[216:217], v[228:229], v[168:169] neg_lo:[1,0,0] neg_hi:[1,0,0]\n"
    "v_pk_fma_f32 v[186:187], v[218:219], v[228:229], v[170:171]\n"
    "v_pk_fma_f32 v[218:219], v[218:219], v[228:229], v[170:171] neg_lo:[1,0,0] neg_hi:[1,0,0]\n"
    "v_pk_fma_f32 v[188:189], v[220:221], v[228:229], v[172:173]\n"
    "v_pk_fma_f32 v[220:221], v[220:221], v[228:229], v[172:173] neg_lo:[1,0,0] neg_hi:[1,0,0]\n"
    "v_pk_fma_f32 v[190:191], v[222:223], v[228:229], v[174:175]\n"
    "v_pk_fma_f32 v[222:223], v[222:223], v[228:229], v[174:175] neg_lo:[1,0,0] neg_hi:[1,0,0]\n"
    "v_cvt_pk_bf16_f32 v128, v176, v177\n"
    "v_cvt_pk_bf16_f32 v129, v178, v179\n"
    "v_cvt_pk_bf16_f32 v130, v184, v185\n"
    "v_cvt_pk_bf16_f32 v131, v186, v187\n"
    "v_cvt_pk_bf16_f32 v132, v180, v181\n"
    "v_cvt_pk_bf16_f32 v133, v182, v183\n"
    "v_cvt_pk_bf16_f32 v134, v188, v189\n"
    "v_cvt_pk_bf16_f32 v135, v190, v191\n"
    "v_cvt_pk_bf16_f32 v136, v208, v209\n"
    "v_cvt_pk_bf16_f32 v137, v210, v211\n"
    "v_cvt_pk_bf16_f32 v138, v216, v217\n"
    "v_cvt_pk_bf16_f32 v139, v218, v219\n"
    "v_cvt_pk_bf16_f32 v140, v212, v213\n"
    "v_cvt_pk_bf16_f32 v141, v214, v215\n"
    "v_cvt_pk_bf16_f32 v142, v220, v221\n"
    "v_cvt_pk_bf16_f32 v143, v222, v223\n"
    ".Lg_join:\n"
    "s_waitcnt vmcnt(13)\n"
    "v_mfma_f32_16x16x32_bf16 v[160:163], a[0:3], v[0:3], 0\n"
    "buffer_store_dwordx4 v[128:131], v254, s[4:7], s48 offen sc1\n"
    "v_lshrrev_b32 v148, 2, v251\n"
    "v_mfma_f32_16x16x32_bf16 v[164:167], a[4:7], v[0:3], 0\n"
    "v_lshl_add_u32 v148, v250, 2, v148\n"
    "v_mfma_f32_16x16x32_bf16 v[168:171], a[8:11], v[0:3], 0\n"
    "v_bfe_u32 v149, v255, 1, 1\n"
    "v_mfma_f32_16x16x32_bf16 v[172:175], a[12:15], v[0:3], 0\n"
    "s_sub_u32 s56, 81920, s53\n"
    "v_and_b32 v150, 6, v148\n"
    "v_mfma_f32_16x16x32_bf16 v[176:179], a[160:163], v[0:3], 0\n"
    "s_add_u32 s19, s46, 4096\n"
    "buffer_load_dword v252, v248, s[4:7], s19 offen sc1\n"
    "v_or_b32 v149, v149, v150\n"
    "s_waitcnt vmcnt(13)\n"
    "v_mfma_f32_16x16x32_bf16 v[180:183], a[0:3], v[4:7], 0\n"
    "s_lshl_b32 s34, s33, 2\n"
    "buffer_store_dwordx4 v[132:135], v254, s[4:7], s49 offen sc1\n"
    "v_and_b32 v150, 1, v255\n"
    "v_mfma_f32_16x16x32_bf16 v[184:187], a[4:7], v[4:7], 0\n"
    "s_add_u32 s34, s34, s3\n"
    "v_lshlrev_b32 v150, 3, v150\n"
    "v_mfma_f32_16x16x32_bf16 v[188:191], a[8:11], v[4:7], 0\n"
    "s_mul_i32 s40, s34, 20480\n"
    "v_lshl_add_u32 v150, v148, 7, v150\n"
    "v_mfma_f32_16x16x32_bf16 v[192:195], a[12:15], v[4:7], 0\n"
    "s_add_u32 s40, s40, 10485760\n"
    "v_add_u32 v150, s53, v150\n"
    "v_mfma_f32_16x16x32_bf16 v[196:199], a[160:163], v[4:7], 0\n"
    "s_lshl_b32 s35, s30, 9\n"
    "v_xor_b32 v151, 0, v149\n"
    "s_waitcnt vmcnt(13)\n"
    "v_mfma_f32_16x16x32_bf16 v[200:203], a[0:3], v[8:11], 0\n"
    "s_add_u32 s41, s40, s35\n"
    "buffer_store_dwordx4 v[136:139], v254, s[4:7], s50 offen sc1\n"
    "v_lshl_add_u32 v144, v151, 4, v150\n"
    "v_mfma_f32_16x16x32_bf16 v[204:207], a[4:7], v[8:11], 0\n"
    "s_add_u32 s41, s41, 16384\n"
    "v_xor_b32 v151, 2, v149\n"
    "v_mfma_f32_16x16x32_bf16 v[208:211], a[8:11], v[8:11], 0\n"
    "s_lshl_b32 s35, s30, 11\n"
    "v_lshl_add_u32 v145, v151, 4, v150\n"
    "v_mfma_f32_16x16x32_bf16 v[212:215], a[12:15], v[8:11], 0\n"
    "s_add_u32 s40, s40, s35\n"
    "v_xor_b32 v151, 4, v149\n"
    "v_mfma_f32_16x16x32_bf16 v[216:219], a[160:163], v[8:11], 0\n"
    "s_lshr_b32 s35, s33, 3\n"
    "v_lshl_add_u32 v146, v151, 4, v150\n"
    "s_waitcnt vmcnt(13)\n"
    "v_mfma_f32_16x16x32_bf16 v[220:223], a[0:3], v[12:15], 0\n"
    "s_lshl_b32 s42, s35, 4\n"
    "buffer_store_dwordx4 v[140:143], v254, s[4:7], s51 offen sc1\n"
    "v_xor_b32 v151, 6, v149\n"
    "v_mfma_f32_16x16x32_bf16 v[224:227], a[4:7], v[12:15], 0\n"
    "s_and_b32 s35, s33, 1\n"
    "v_lshl_add_u32 v147, v151, 4, v150\n"
    "v_mfma_f32_16x16x32_bf16 v[228:231], a[8:11], v[12:15], 0\n"
    "s_lshl_b32 s35, s35, 3\n"
    "v_and_b32 v158, 1, v250\n"
    "v_mfma_f32_16x16x32_bf16 v[232:235], a[12:15], v[12:15], 0\n"
    "s_add_u32 s42, s42, s35\n"
    "buffer_load_dwordx4 v[52:55], v241, s[4:7], s36 offen sc1\n"
    "v_lshrrev_b32 v159, 1, v250\n"
    "v_mfma_f32_16x16x32_bf16 v[236:239], a[160:163], v[12:15], 0\n"
    "s_lshr_b32 s35, s3, 1\n"
    "v_lshl_add_u32 v148, v158, 1, v159\n"
    "s_waitcnt vmcnt(14)\n"
    "v_mfma_f32_16x16x32_bf16 v[160:163], a[16:19], v[16:19], v[160:163]\n"
    "s_lshl_b32 s35, s35, 2\n"
    "v_lshlrev_b32 v149, 4, v251\n"
    "v_mfma_f32_16x16x32_bf16 v[164:167], a[20:23], v[16:19], v[164:167]\n"
    "s_add_u32 s42, s42, s35\n"
    "ds_read_b64_tr_b16 a[128:129], v144 offset:0\n"
    "v_lshl_add_u32 v150, v148, 8, v149\n"
    "v_mfma_f32_16x16x32_bf16 v[168:171], a[24:27], v[16:19], v[168:171]\n"
    "s_and_b32 s35, s33, 2\n"
    "buffer_load_dwordx4 v[56:59], v242, s[4:7], s36 offen sc1\n"
    "v_lshl_add_u32 v151, v159, 8, v149\n"
    "v_mfma_f32_16x16x32_bf16 v[172:175], a[28:31], v[16:19], v[172:175]\n"
    "s_add_u32 s42, s42, s35\n"
    "ds_read_b64_tr_b16 a[130:131], v144 offset:2048\n"
    "v_lshl_add_u32 v151, v158, 3, v151\n"
    "v_mfma_f32_16x16x32_bf16 v[176:179], a[164:167], v[16:19], v[176:179]\n"
    "s_and_b32 s35, s3, 1\n"
    "v_mul_u32_u24 v152, 0x500, v251\n"
    "s_waitcnt vmcnt(14)\n"
    "v_mfma_f32_16x16x32_bf16 v[180:183], a[16:19], v[20:23], v[180:183]\n"
    "s_add_u32 s42, s42, s35\n"
    "ds_read_b64_tr_b16 a[132:133], v145 offset:0\n"
    "v_lshl_add_u32 v153, v250, 3, v152\n"
    "v_mfma_f32_16x16x32_bf16 v[184:187], a[20:23], v[20:23], v[184:187]\n"
    "s_lshl_b32 s42, s42, 1\n"
    "buffer_load_dwordx4 v[60:63], v243, s[4:7], s36 offen sc1\n"
    "v_lshl_add_u32 v152, v148, 4, v152\n"
    "v_mfma_f32_16x16x32_bf16 v[188:191], a[24:27], v[20:23], v[188:191]\n"
    "s_bfe_u32 s35, s33, 0x10002\n"
    "ds_read_b64_tr_b16 a[134:135], v145 offset:2048\n"
    "v_mfma_f32_16x16x32_bf16 v[192:195], a[28:31], v[20:23], v[192:195]\n"
    "s_add_u32 s42, s42, s35\n"
    "v_mfma_f32_16x16x32_bf16 v[196:199], a[164:167], v[20:23], v[196:199]\n"
    "s_mul_i32 s42, s42, 20480\n"
    "ds_read_b64_tr_b16 a[136:137], v146 offset:0\n"
    "s_waitcnt vmcnt(14)\n"
    "v_mfma_f32_16x16x32_bf16 v[200:203], a[16:19], v[24:27], v[200:203]\n"
    "s_lshl_b32 s35, s30, 5\n"
    "v_mfma_f32_16x16x32_bf16 v[204:207], a[20:23], v[24:27], v[204:207]\n"
    "s_add_u32 s43, s42, s35\n"
    "ds_read_b64_tr_b16 a[138:139], v146 offset:2048\n"
    "v_mfma_f32_16x16x32_bf16 v[208:211], a[24:27], v[24:27], v[208:211]\n"
    "s_lshl_b32 s35, s30, 7\n"
    "v_mfma_f32_16x16x32_bf16 v[212:215], a[28:31], v[24:27], v[212:215]\n"
    "s_add_u32 s42, s42, s35\n"
    "ds_read_b64_tr_b16 a[140:141], v147 offset:0\n"
    "v_mfma_f32_16x16x32_bf16 v[216:219], a[164:167], v[24:27], v[216:219]\n"
    "s_add_u32 s34, s3, 0\n"
    "s_waitcnt vmcnt(13)\n"
    "v_mfma_f32_16x16x32_bf16 v[220:223], a[16:19], v[28:31], v[220:223]\n"
    "s_and_b32 s34, s34, 3\n"
    "ds_read_b64_tr_b16 a[142:143], v147 offset:2048\n"
    "v_mfma_f32_16x16x32_bf16 v[224:227], a[20:23], v[28:31], v[224:227]\n"
    "s_mul_i32 s12, s34, 15360\n"
    "v_mfma_f32_16x16x32_bf16 v[228:231], a[24:27], v[28:31], v[228:231]\n"
    "s_add_u32 s34, s3, 1\n"
    "s_waitcnt vmcnt(6)\n"
    "v_cmp_ne_u32 vcc, v252, v253\n"
    "s_nop 0\n"
    "s_and_b32 s58, vcc_lo, 0xffffffff\n"
    "s_cbranch_scc1 .Lp6\n"
    ".Lpr6:\n"
    "ds_read_b64_tr_b16 a[144:145], v144 offset:4096\n"
    "v_mfma_f32_16x16x32_bf16 v[232:235], a[28:31], v[28:31], v[232:235]\n"
    "s_add_u32 s36, s37, 16384\n"
    "v_mfma_f32_16x16x32_bf16 v[236:239], a[164:167], v[28:31], v[236:239]\n"
    "s_and_b32 s34, s34, 3\n"
    "ds_read_b64_tr_b16 a[146:147], v144 offset:6144\n"
    "v_mfma_f32_16x16x32_bf16 v[160:163], a[32:35], v[32:35], v[160:163]\n"
    "s_mul_i32 s13, s34, 15360\n"
    "buffer_load_dwordx4 v[64:67], v240, s[4:7], s36 offen sc1\n"
    "v_mfma_f32_16x16x32_bf16 v[164:167], a[36:39], v[32:35], v[164:167]\n"
    "s_add_u32 s34, s3, 2\n"
    "ds_read_b64_tr_b16 a[148:149], v145 offset:4096\n"
    "v_mfma_f32_16x16x32_bf16 v[168:171], a[40:43], v[32:35], v[168:171]\n"
    "s_and_b32 s34, s34, 3\n"
    "v_mfma_f32_16x16x32_bf16 v[172:175], a[44:47], v[32:35], v[172:175]\n"
    "s_mul_i32 s14, s34, 15360\n"
    "ds_read_b64_tr_b16 a[150:151], v145 offset:6144\n"
    "v_mfma_f32_16x16x32_bf16 v[176:179], a[168:171], v[32:35], v[176:179]\n"
    "s_add_u32 s34, s3, 3\n"
    "buffer_load_dwordx4 v[68:71], v241, s[4:7], s36 offen sc1\n"
    "v_mfma_f32_16x16x32_bf16 v[180:183], a[32:35], v[36:39], v[180:183]\n"
    "s_and_b32 s34, s34, 3\n"
    "ds_read_b64_tr_b16 a[152:153], v146 offset:4096\n"
    "v_mfma_f32_16x16x32_bf16 v[184:187], a[36:39], v[36:39], v[184:187]\n"
    "s_mul_i32 s15, s34, 15360\n"
    "v_mfma_f32_16x16x32_bf16 v[188:191], a[40:43], v[36:39], v[188:191]\n"
    "s_lshl_b32 s18, s30, 1\n"
    "ds_read_b64_tr_b16 a[154:155], v146 offset:6144\n"
    "v_mfma_f32_16x16x32_bf16 v[192:195], a[44:47], v[36:39], v[192:195]\n"
    "s_cmp_ge_u32 s18, 10\n"
    "s_cselect_b32 s34, 10, 0\n"
    "buffer_load_dwordx4 v[72:75], v242, s[4:7], s36 offen sc1\n"
    "v_mfma_f32_16x16x32_bf16 v[196:199], a[168:171], v[36:39], v[196:199]\n"
    "s_sub_u32 s18, s18, s34\n"
    "ds_read_b64_tr_b16 a[156:157], v147 offset:4096\n"
    "v_mfma_f32_16x16x32_bf16 v[200:203], a[32:35], v[40:43], v[200:203]\n"
    "s_add_u32 s24, s18, 1\n"
    "v_mfma_f32_16x16x32_bf16 v[204:207], a[36:39], v[40:43], v[204:207]\n"
    "s_lshr_b32 s25, s30, 1\n"
    "ds_read_b64_tr_b16 a[158:159], v147 offset:6144\n"
    "v_mfma_f32_16x16x32_bf16 v[208:211], a[40:43], v[40:43], v[208:211]\n"
    "s_add_u32 s25, s25, 6\n"
    "buffer_load_dwordx4 v[76:79], v243, s[4:7], s36 offen sc1\n"
    "v_mfma_f32_16x16x32_bf16 v[212:215], a[44:47], v[40:43], v[212:215]\n"
    "s_lshr_b32 s26, s3, 1\n"
    "ds_read_b64_tr_b16 a[0:1], v144 offset:8192\n"
    "v_mfma_f32_16x16x32_bf16 v[216:219], a[168:171], v[40:43], v[216:219]\n"
    "s_add_u32 s36, s37, 20480\n"
    "v_mfma_f32_16x16x32_bf16 v[220:223], a[32:35], v[44:47], v[220:223]\n"
    "s_lshl_b32 s26, s26, 7\n"
    "ds_read_b64_tr_b16 a[2:3], v144 offset:10240\n"
    "v_mfma_f32_16x16x32_bf16 v[224:227], a[36:39], v[44:47], v[224:227]\n"
    "s_add_u32 s39, s39, s26\n"
    "buffer_load_dwordx4 v[80:83], v240, s[4:7], s36 offen sc1\n"
    "v_mfma_f32_16x16x32_bf16 v[228:231], a[40:43], v[44:47], v[228:231]\n"
    "s_cmp_ge_u32 s18, 5\n"
    "s_cselect_b32 s34, 3, 0\n"
    "ds_read_b64_tr_b16 a[4:5], v145 offset:8192\n"
    "v_mfma_f32_16x16x32_bf16 v[232:235], a[44:47], v[44:47], v[232:235]\n"
    "s_add_u32 s34, s34, s18\n"
    "v_mfma_f32_16x16x32_bf16 v[236:239], a[168:171], v[44:47], v[236:239]\n"
    "s_and_b32 s35, s34, 3\n"
    "ds_read_b64_tr_b16 a[6:7], v145 offset:10240\n"
    "v_mfma_f32_16x16x32_bf16 v[160:163], a[48:51], v[48:51], v[160:163]\n"
    "s_lshl_b32 s35, s35, 2\n"
    "buffer_load_dwordx4 v[84:87], v241, s[4:7], s36 offen sc1\n"
    "s_add_u32 s38, s46, 8192\n"
    "buffer_load_dword v246, v248, s[4:7], s38 offen sc1\n"
    "v_mfma_f32_16x16x32_bf16 v[164:167], a[52:55], v[48:51], v[164:167]\n"
    "s_lshr_b32 s18, s34, 2\n"
    "ds_read_b64_tr_b16 a[8:9], v146 offset:8192\n"
    "v_mfma_f32_16x16x32_bf16 v[168:171], a[56:59], v[48:51], v[168:171]\n"
    "s_waitcnt vmcnt(10)\n"
    "s_add_u32 s47, s45, 12288\n"
    "buffer_store_dword v253, v244, s[4:7], s47 offen sc1\n"
    "v_mfma_f32_16x16x32_bf16 v[172:175], a[60:63], v[48:51], v[172:175]\n"
    "s_add_u32 s18, s18, s35\n"
    "ds_read_b64_tr_b16 a[10:11], v146 offset:10240\n"
    "v_mfma_f32_16x16x32_bf16 v[176:179], a[172:175], v[48:51], v[176:179]\n"
    "s_lshl_b32 s18, s18, 8\n"
    "buffer_load_dwordx4 v[88:91], v242, s[4:7], s36 offen sc1\n"
    "s_waitcnt vmcnt(11)\n"
    "v_mfma_f32_16x16x32_bf16 v[180:183], a[48:51], v[52:55], v[180:183]\n"
    "s_cmp_ge_u32 s24, 5\n"
    "s_cselect_b32 s34, 3, 0\n"
    "ds_read_b64_tr_b16 a[12:13], v147 offset:8192\n"
    "v_mfma_f32_16x16x32_bf16 v[184:187], a[52:55], v[52:55], v[184:187]\n"
    "s_add_u32 s34, s34, s24\n"
    "v_mfma_f32_16x16x32_bf16 v[188:191], a[56:59], v[52:55], v[188:191]\n"
    "s_and_b32 s35, s34, 3\n"
    "ds_read_b64_tr_b16 a[14:15], v147 offset:10240\n"
    "v_mfma_f32_16x16x32_bf16 v[192:195], a[60:63], v[52:55], v[192:195]\n"
    "s_lshl_b32 s35, s35, 2\n"
    "buffer_load_dwordx4 v[92:95], v243, s[4:7], s36 offen sc1\n"
    "v_mfma_f32_16x16x32_bf16 v[196:199], a[172:175], v[52:55], v[196:199]\n"
    "s_lshr_b32 s24, s34, 2\n"
    "ds_read_b64_tr_b16 a[16:17], v144 offset:12288\n"
    "s_waitcnt vmcnt(11)\n"
    "v_mfma_f32_16x16x32_bf16 v[200:203], a[48:51], v[56:59], v[200:203]\n"
    "s_add_u32 s36, s37, 24576\n"
    "v_mfma_f32_16x16x32_bf16 v[204:207], a[52:55], v[56:59], v[204:207]\n"
    "s_add_u32 s24, s24, s35\n"
    "ds_read_b64_tr_b16 a[18:19], v144 offset:14336\n"
    "v_mfma_f32_16x16x32_bf16 v[208:211], a[56:59], v[56:59], v[208:211]\n"
    "s_lshl_b32 s24, s24, 8\n"
    "buffer_load_dwordx4 v[96:99], v240, s[4:7], s36 offen sc1\n"
    "v_mfma_f32_16x16x32_bf16 v[212:215], a[60:63], v[56:59], v[212:215]\n"
    "s_cmp_ge_u32 s25, 5\n"
    "s_cselect_b32 s34, 3, 0\n"
    "ds_read_b64_tr_b16 a[20:21], v145 offset:12288\n"
    "v_mfma_f32_16x16x32_bf16 v[216:219], a[172:175], v[56:59], v[216:219]\n"
    "s_add_u32 s34, s34, s25\n"
    "s_waitcnt vmcnt(11)\n"
    "v_mfma_f32_16x16x32_bf16 v[220:223], a[48:51], v[60:63], v[220:223]\n"
    "s_and_b32 s35, s34, 3\n"
    "ds_read_b64_tr_b16 a[22:23], v145 offset:14336\n"
    "v_mfma_f32_16x16x32_bf16 v[224:227], a[52:55], v[60:63], v[224:227]\n"
    "s_lshl_b32 s35, s35, 2\n"
    "buffer_load_dwordx4 v[100:103], v241, s[4:7], s36 offen sc1\n"
    "v_mfma_f32_16x16x32_bf16 v[228:231], a[56:59], v[60:63], v[228:231]\n"
    "s_lshr_b32 s25, s34, 2\n"
    "ds_read_b64_tr_b16 a[24:25], v146 offset:12288\n"
    "v_mfma_f32_16x16x32_bf16 v[232:235], a[60:63], v[60:63], v[232:235]\n"
    "s_add_u32 s25, s25, s35\n"
    "v_mfma_f32_16x16x32_bf16 v[236:239], a[172:175], v[60:63], v[236:239]\n"
    "s_lshl_b32 s25, s25, 8\n"
    "ds_read_b64_tr_b16 a[26:27], v146 offset:14336\n"
    "s_waitcnt vmcnt(11)\n"
    "v_mfma_f32_16x16x32_bf16 v[160:163], a[64:67], v[64:67], v[160:163]\n"
    "s_lshl_b32 s16, s3, 10\n"
    "buffer_load_dwordx4 v[104:107], v242, s[4:7], s36 offen sc1\n"
    "v_mfma_f32_16x16x32_bf16 v[164:167], a[68:71], v[64:67], v[164:167]\n"
    "s_sub_u32 s16, s37, s16\n"
    "ds_read_b64_tr_b16 a[28:29], v147 offset:12288\n"
    "v_mfma_f32_16x16x32_bf16 v[168:171], a[72:75], v[64:67], v[168:171]\n"
    "s_add_u32 s16, s16, 49152\n"
    "v_mfma_f32_16x16x32_bf16 v[172:175], a[76:79], v[64:67], v[172:175]\n"
    "s_add_u32 s17, s16, 4096\n"
    "ds_read_b64_tr_b16 a[30:31], v147 offset:14336\n"
    "v_mfma_f32_16x16x32_bf16 v[176:179], a[176:179], v[64:67], v[176:179]\n"
    "s_lshl_b32 s26, s30, 16\n"
    "buffer_load_dwordx4 v[108:111], v243, s[4:7], s36 offen sc1\n"
    "s_waitcnt vmcnt(12)\n"
    "v_mfma_f32_16x16x32_bf16 v[180:183], a[64:67], v[68:71], v[180:183]\n"
    "s_add_u32 s26, s26, 20512768\n"
    "v_add_u32 v144, s56, v144\n"
    "v_mfma_f32_16x16x32_bf16 v[184:187], a[68:71], v[68:71], v[184:187]\n"
    "s_add_u32 s36, s37, 28672\n"
    ".Lpad7:\n"
    "s_cbranch_execz .Lw_ret\n"
    "v_add_u32 v145, s56, v145\n"
    "v_mfma_f32_16x16x32_bf16 v[188:191], a[72:75], v[68:71], v[188:191]\n"
    "s_add_u32 s27, s26, 4096\n"
    "v_add_u32 v146, s56, v146\n"
    "v_mfma_f32_16x16x32_bf16 v[192:195], a[76:79], v[68:71], v[192:195]\n"
    "buffer_load_dwordx4 v[112:115], v240, s[4:7], s36 offen sc1\n"
    "v_mov_b32 v158, 1280\n"
    "v_add_u32 v147, s56, v147\n"
    "v_mfma_f32_16x16x32_bf16 v[196:199], a[176:179], v[68:71], v[196:199]\n"
    "s_mov_b32 s34, 2304\n"
    "v_writelane_b32 v158, s34, 1\n"
    "s_waitcnt vmcnt(12)\n"
    "v_mfma_f32_16x16x32_bf16 v[200:203], a[64:67], v[72:75], v[200:203]\n"
    "s_mov_b32 s34, 3328\n"
    "v_writelane_b32 v158, s34, 2\n"
    "v_mfma_f32_16x16x32_bf16 v[204:207], a[68:71], v[72:75], v[204:207]\n"
    "s_mov_b32 s34, 1792\n"
    "v_writelane_b32 v158, s34, 3\n"
    "v_mfma_f32_16x16x32_bf16 v[208:211], a[72:75], v[72:75], v[208:211]\n"
    "s_mov_b32 s34, 2816\n"
    "buffer_load_dwordx4 v[116:119], v241, s[4:7], s36 offen sc1\n"
    "v_writelane_b32 v158, s34, 4\n"
    "v_mfma_f32_16x16x32_bf16 v[212:215], a[76:79], v[72:75], v[212:215]\n"
    "s_mov_b32 s34, 3840\n"
    "v_writelane_b32 v158, s34, 5\n"
    "v_mfma_f32_16x16x32_bf16 v[216:219], a[176:179], v[72:75], v[216:219]\n"
    "s_waitcnt vmcnt(12)\n"
    "v_writelane_b32 v158, s18, 6\n"
    "v_mfma_f32_16x16x32_bf16 v[220:223], a[64:67], v[76:79], v[220:223]\n"
    "v_writelane_b32 v158, s24, 7\n"
    "v_mfma_f32_16x16x32_bf16 v[224:227], a[68:71], v[76:79], v[224:227]\n"
    "buffer_load_dwordx4 v[120:123], v242, s[4:7], s36 offen sc1\n"
    "v_writelane_b32 v158, s25, 8\n"
    "v_mfma_f32_16x16x32_bf16 v[228:231], a[72:75], v[76:79], v[228:231]\n"
    "v_mfma_f32_16x16x32_bf16 v[232:235], a[76:79], v[76:79], v[232:235]\n"
    "v_mfma_f32_16x16x32_bf16 v[236:239], a[176:179], v[76:79], v[236:239]\n"
    "s_waitcnt vmcnt(12)\n"
    "v_mfma_f32_16x16x32_bf16 v[160:163], a[80:83], v[80:83], v[160:163]\n"
    "buffer_load_dwordx4 v[124:127], v243, s[4:7], s36 offen sc1\n"
    "v_mfma_f32_16x16x32_bf16 v[164:167], a[84:87], v[80:83], v[164:167]\n"
    "s_waitcnt vmcnt(11)\n"
    "v_cmp_ne_u32 vcc, v246, v253\n"
    "s_nop 0\n"
    "s_and_b32 s58, vcc_lo, 0xffffffff\n"
    "s_cbranch_scc1 .Lp7\n"
    ".Lpr7:\n"
    "v_mfma_f32_16x16x32_bf16 v[168:171], a[88:91], v[80:83], v[168:171]\n"
    "s_add_u32 s36, s37, 32768\n"
    "v_mfma_f32_16x16x32_bf16 v[172:175], a[92:95], v[80:83], v[172:175]\n"
    "v_mfma_f32_16x16x32_bf16 v[176:179], a[180:183], v[80:83], v[176:179]\n"
    "buffer_load_dwordx4 v[0:3], v240, s[4:7], s36 offen sc1\n"
    "v_mfma_f32_16x16x32_bf16 v[180:183], a[80:83], v[84:87], v[180:183]\n"
    "v_mfma_f32_16x16x32_bf16 v[184:187], a[84:87], v[84:87], v[184:187]\n"
    "v_mfma_f32_16x16x32_bf16 v[188:191], a[88:91], v[84:87], v[188:191]\n"
    "v_mfma_f32_16x16x32_bf16 v[192:195], a[92:95], v[84:87], v[192:195]\n"
    "buffer_load_dwordx4 v[4:7], v241, s[4:7], s36 offen sc1\n"
    "v_mfma_f32_16x16x32_bf16 v[196:199], a[180:183], v[84:87], v[196:199]\n"
    "s_waitcnt vmcnt(11)\n"
    "v_mfma_f32_16x16x32_bf16 v[200:203], a[80:83], v[88:91], v[200:203]\n"
    "s_waitcnt lgkmcnt(0)\n"
    "s_barrier\n"
    "v_mfma_f32_16x16x32_bf16 v[204:207], a[84:87], v[88:91], v[204:207]\n"
    "ds_read_b64_tr_b16 a[32:33], v144 offset:0\n"
    "v_mfma_f32_16x16x32_bf16 v[208:211], a[88:91], v[88:91], v[208:211]\n"
    "buffer_load_dwordx4 v[8:11], v242, s[4:7], s36 offen sc1\n"
    "ds_read_b64_tr_b16 a[34:35], v144 offset:2048\n"
    "v_mfma_f32_16x16x32_bf16 v[212:215], a[92:95], v[88:91], v[212:215]\n"
    "ds_read_b64_tr_b16 a[36:37], v145 offset:0\n"
    "v_mfma_f32_16x16x32_bf16 v[216:219], a[180:183], v[88:91], v[216:219]\n"
    "s_waitcnt vmcnt(11)\n"
    "ds_read_b64_tr_b16 a[38:39], v145 offset:2048\n"
    "v_mfma_f32_16x16x32_bf16 v[220:223], a[80:83], v[92:95], v[220:223]\n"
    "ds_read_b64_tr_b16 a[40:41], v146 offset:0\n"
    "v_mfma_f32_16x16x32_bf16 v[224:227], a[84:87], v[92:95], v[224:227]\n"
    "buffer_load_dwordx4 v[12:15], v243, s[4:7], s36 offen sc1\n"
    "ds_read_b64_tr_b16 a[42:43], v146 offset:2048\n"
    "v_mfma_f32_16x16x32_bf16 v[228:231], a[88:91], v[92:95], v[228:231]\n"
    "ds_read_b64_tr_b16 a[44:45], v147 offset:0\n"
    "v_mfma_f32_16x16x32_bf16 v[232:235], a[92:95], v[92:95], v[232:235]\n"
    "s_add_u32 s36, s37, 36864\n"
    "ds_read_b64_tr_b16 a[46:47], v147 offset:2048\n"
    "v_mfma_f32_16x16x32_bf16 v[236:239], a[180:183], v[92:95], v[236:239]\n"
    "s_waitcnt vmcnt(11)\n"
    "ds_read_b64_tr_b16 a[48:49], v144 offset:4096\n"
    "v_mfma_f32_16x16x32_bf16 v[160:163], a[96:99], v[96:99], v[160:163]\n"
    "buffer_load_dwordx4 v[20:23], v241, s[4:7], s36 offen sc1\n"
    "ds_read_b64_tr_b16 a[50:51], v144 offset:6144\n"
    "v_mfma_f32_16x16x32_bf16 v[164:167], a[100:103], v[96:99], v[164:167]\n"
    "ds_read_b64_tr_b16 a[52:53], v145 offset:4096\n"
    "v_mfma_f32_16x16x32_bf16 v[168:171], a[104:107], v[96:99], v[168:171]\n"
    "s_add_u32 s36, s37, 40960\n"
    "ds_read_b64_tr_b16 a[54:55], v145 offset:6144\n"
    "v_mfma_f32_16x16x32_bf16 v[172:175], a[108:111], v[96:99], v[172:175]\n"
    "ds_read_b64_tr_b16 a[56:57], v146 offset:4096\n"
    "v_mfma_f32_16x16x32_bf16 v[176:179], a[184:187], v[96:99], v[176:179]\n"
    "s_waitcnt vmcnt(11)\n"
    "buffer_load_dwordx4 v[36:39], v241, s[4:7], s36 offen sc1\n"
    "ds_read_b64_tr_b16 a[58:59], v146 offset:6144\n"
    "v_mfma_f32_16x16x32_bf16 v[180:183], a[96:99], v[100:103], v[180:183]\n"
    "ds_read_b64_tr_b16 a[60:61], v147 offset:4096\n"
    "v_mfma_f32_16x16x32_bf16 v[184:187], a[100:103], v[100:103], v[184:187]\n"
    "s_add_u32 s36, s37, 45056\n"
    "ds_read_b64_tr_b16 a[62:63], v147 offset:6144\n"
    "v_mfma_f32_16x16x32_bf16 v[188:191], a[104:107], v[100:103], v[188:191]\n"
    "ds_read_b64_tr_b16 a[64:65], v144 offset:8192\n"
    "v_mfma_f32_16x16x32_bf16 v[192:195], a[108:111], v[100:103], v[192:195]\n"
    "buffer_load_dwordx4 v[52:55], v241, s[4:7], s36 offen sc1\n"
    "ds_read_b64_tr_b16 a[66:67], v144 offset:10240\n"
    "v_mfma_f32_16x16x32_bf16 v[196:199], a[184:187], v[100:103], v[196:199]\n"
    "s_waitcnt vmcnt(12)\n"
    "ds_read_b64_tr_b16 a[68:69], v145 offset:8192\n"
    "v_mfma_f32_16x16x32_bf16 v[200:203], a[96:99], v[104:107], v[200:203]\n"
    "s_add_u32 s36, s37, 36864\n"
    "ds_read_b64_tr_b16 a[70:71], v145 offset:10240\n"
    "v_mfma_f32_16x16x32_bf16 v[204:207], a[100:103], v[104:107], v[204:207]\n"
    "ds_read_b64_tr_b16 a[72:73], v146 offset:8192\n"
    "v_mfma_f32_16x16x32_bf16 v[208:211], a[104:107], v[104:107], v[208:211]\n"
    "buffer_load_dwordx4 v[24:27], v242, s[4:7], s36 offen sc1\n"
    "ds_read_b64_tr_b16 a[74:75], v146 offset:10240\n"
    "v_mfma_f32_16x16x32_bf16 v[212:215], a[108:111], v[104:107], v[212:215]\n"
    "ds_read_b64_tr_b16 a[76:77], v147 offset:8192\n"
    "v_mfma_f32_16x16x32_bf16 v[216:219], a[184:187], v[104:107], v[216:219]\n"
    "s_add_u32 s36, s37, 40960\n"
    "ds_read_b64_tr_b16 a[78:79], v147 offset:10240\n"
    "s_waitcnt vmcnt(12)\n"
    "v_mfma_f32_16x16x32_bf16 v[220:223], a[96:99], v[108:111], v[220:223]\n"
    "ds_read_b64_tr_b16 a[80:81], v144 offset:12288\n"
    "v_mfma_f32_16x16x32_bf16 v[224:227], a[100:103], v[108:111], v[224:227]\n"
    "buffer_load_dwordx4 v[40:43], v242, s[4:7], s36 offen sc1\n"
    "ds_read_b64_tr_b16 a[82:83], v144 offset:14336\n"
    "v_mfma_f32_16x16x32_bf16 v[228:231], a[104:107], v[108:111], v[228:231]\n"
    "ds_read_b64_tr_b16 a[84:85], v145 offset:12288\n"
    "v_mfma_f32_16x16x32_bf16 v[232:235], a[108:111], v[108:111], v[232:235]\n"
    "s_add_u32 s36, s37, 45056\n"
    "ds_read_b64_tr_b16 a[86:87], v145 offset:14336\n"
    "v_mfma_f32_16x16x32_bf16 v[236:239], a[184:187], v[108:111], v[236:239]\n"
    "s_waitcnt vmcnt(12)\n"
    "ds_read_b64_tr_b16 a[88:89], v146 offset:12288\n"
    "v_mfma_f32_16x16x32_bf16 v[160:163], a[112:115], v[112:115], v[160:163]\n"
    "buffer_load_dwordx4 v[56:59], v242, s[4:7], s36 offen sc1\n"
    "buffer_load_dwordx4 a[208:211], v254, s[4:7], s26 offen offset:0 sc1\n"
    "ds_read_b64_tr_b16 a[90:91], v146 offset:14336\n"
    "v_mfma_f32_16x16x32_bf16 v[164:167], a[116:119], v[112:115], v[164:167]\n"
    "ds_read_b64_tr_b16 a[92:93], v147 offset:12288\n"
    "v_mfma_f32_16x16x32_bf16 v[168:171], a[120:123], v[112:115], v[168:171]\n"
    "s_add_u32 s36, s37, 36864\n"
    "ds_read_b64_tr_b16 a[94:95], v147 offset:14336\n"
    "v_mfma_f32_16x16x32_bf16 v[172:175], a[124:127], v[112:115], v[172:175]\n"
    "ds_read_b64_tr_b16 a[96:97], v144 offset:16384\n"
    "v_mfma_f32_16x16x32_bf16 v[176:179], a[188:191], v[112:115], v[176:179]\n"
    "s_waitcnt vmcnt(13)\n"
    "buffer_load_dwordx4 v[28:31], v243, s[4:7], s36 offen sc1\n"
    "ds_read_b64_tr_b16 a[98:99], v144 offset:18432\n"
    "v_mfma_f32_16x16x32_bf16 v[180:183], a[112:115], v[116:119], v[180:183]\n"
    "ds_read_b64_tr_b16 a[100:101], v145 offset:16384\n"
    "v_mfma_f32_16x16x32_bf16 v[184:187], a[116:119], v[116:119], v[184:187]\n"
    "s_add_u32 s36, s37, 40960\n"
    "ds_read_b64_tr_b16 a[102:103], v145 offset:18432\n"
    "v_mfma_f32_16x16x32_bf16 v[188:191], a[120:123], v[116:119], v[188:191]\n"
    "ds_read_b64_tr_b16 a[104:105], v146 offset:16384\n"
    "v_mfma_f32_16x16x32_bf16 v[192:195], a[124:127], v[116:119], v[192:195]\n"
    "buffer_load_dwordx4 v[44:47], v243, s[4:7], s36 offen sc1\n"
    "ds_read_b64_tr_b16 a[106:107], v146 offset:18432\n"
    "v_mfma_f32_16x16x32_bf16 v[196:199], a[188:191], v[116:119], v[196:199]\n"
    "s_waitcnt vmcnt(14)\n"
    "buffer_load_dwordx4 a[212:215], v254, s[4:7], s26 offen offset:1024 sc1\n"
    "ds_read_b64_tr_b16 a[108:109], v147 offset:16384\n"
    "v_mfma_f32_16x16x32_bf16 v[200:203], a[112:115], v[120:123], v[200:203]\n"
    "s_add_u32 s36, s37, 45056\n"
    "buffer_load_dword v159, v158, s[4:7], s39 offen sc1\n"
    "ds_read_b64_tr_b16 a[110:111], v147 offset:18432\n"
    "v_mfma_f32_16x16x32_bf16 v[204:207], a[116:119], v[120:123], v[204:207]\n"
    "v_mfma_f32_16x16x32_bf16 v[208:211], a[120:123], v[120:123], v[208:211]\n"
    "buffer_load_dwordx4 v[60:63], v243, s[4:7], s36 offen sc1\n"
    "v_mfma_f32_16x16x32_bf16 v[212:215], a[124:127], v[120:123], v[212:215]\n"
    "v_mfma_f32_16x16x32_bf16 v[216:219], a[188:191], v[120:123], v[216:219]\n"
    "s_add_u32 s36, s37, 36864\n"
    "s_waitcnt vmcnt(16)\n"
    "v_mfma_f32_16x16x32_bf16 v[220:223], a[112:115], v[124:127], v[220:223]\n"
    "v_mfma_f32_16x16x32_bf16 v[224:227], a[116:119], v[124:127], v[224:227]\n"
    "buffer_load_dwordx4 v[16:19], v240, s[4:7], s36 offen sc1\n"
    "v_mfma_f32_16x16x32_bf16 v[228:231], a[120:123], v[124:127], v[228:231]\n"
    "v_mfma_f32_16x16x32_bf16 v[232:235], a[124:127], v[124:127], v[232:235]\n"
    "s_add_u32 s36, s37, 40960\n"
    "buffer_load_dwordx4 a[216:219], v254, s[4:7], s26 offen offset:2048 sc1\n"
    "v_mfma_f32_16x16x32_bf16 v[236:239], a[188:191], v[124:127], v[236:239]\n"
    "s_waitcnt vmcnt(17)\n"
    "v_mfma_f32_16x16x32_bf16 v[160:163], a[128:131], v[0:3], v[160:163]\n"
    "buffer_load_dwordx4 v[32:35], v240, s[4:7], s36 offen sc1\n"
    "ds_read_b64_tr_b16 a[112:113], v144 offset:20480\n"
    "v_mfma_f32_16x16x32_bf16 v[164:167], a[132:135], v[0:3], v[164:167]\n"
    "ds_read_b64_tr_b16 a[114:115], v144 offset:22528\n"
    "v_mfma_f32_16x16x32_bf16 v[168:171], a[136:139], v[0:3], v[168:171]\n"
    "s_add_u32 s36, s37, 45056\n"
    "ds_read_b64_tr_b16 a[116:117], v145 offset:20480\n"
    "v_mfma_f32_16x16x32_bf16 v[172:175], a[140:143], v[0:3], v[172:175]\n"
    "ds_read_b64_tr_b16 a[118:119], v145 offset:22528\n"
    "v_mfma_f32_16x16x32_bf16 v[176:179], a[192:195], v[0:3], v[176:179]\n"
    "s_waitcnt vmcnt(17)\n"
    "buffer_load_dwordx4 v[48:51], v240, s[4:7], s36 offen sc1\n"
    "ds_read_b64_tr_b16 a[120:121], v146 offset:20480\n"
    "v_mfma_f32_16x16x32_bf16 v[180:183], a[128:131], v[4:7], v[180:183]\n"
    "ds_read_b64_tr_b16 a[122:123], v146 offset:22528\n"
    "v_mfma_f32_16x16x32_bf16 v[184:187], a[132:135], v[4:7], v[184:187]\n"
    "ds_read_b64_tr_b16 a[124:125], v147 offset:20480\n"
    "v_mfma_f32_16x16x32_bf16 v[188:191], a[136:139], v[4:7], v[188:191]\n"
    "buffer_load_dwordx4 a[220:223], v254, s[4:7], s26 offen offset:3072 sc1\n"
    "ds_read_b64_tr_b16 a[126:127], v147 offset:22528\n"
    "v_mfma_f32_16x16x32_bf16 v[192:195], a[140:143], v[4:7], v[192:195]\n"
    "v_mfma_f32_16x16x32_bf16 v[196:199], a[192:195], v[4:7], v[196:199]\n"
    "s_waitcnt vmcnt(18)\n"
    "v_mfma_f32_16x16x32_bf16 v[200:203], a[128:131], v[8:11], v[200:203]\n"
    "v_add_u32 v154, s12, v254\n"
    "v_mfma_f32_16x16x32_bf16 v[204:207], a[132:135], v[8:11], v[204:207]\n"
    "v_add_u32 v155, s13, v254\n"
    "v_mfma_f32_16x16x32_bf16 v[208:211], a[136:139], v[8:11], v[208:211]\n"
    "v_add_u32 v156, s14, v254\n"
    "v_mfma_f32_16x16x32_bf16 v[212:215], a[140:143], v[8:11], v[212:215]\n"
    "v_add_u32 v157, s15, v254\n"
    "v_mfma_f32_16x16x32_bf16 v[216:219], a[192:195], v[8:11], v[216:219]\n"
    "s_waitcnt vmcnt(17)\n"
    "v_mfma_f32_16x16x32_bf16 v[220:223], a[128:131], v[12:15], v[220:223]\n"
    "v_mfma_f32_16x16x32_bf16 v[224:227], a[132:135], v[12:15], v[224:227]\n"
    "buffer_load_dwordx4 a[224:227], v254, s[4:7], s27 offen offset:0 sc1\n"
    "v_mfma_f32_16x16x32_bf16 v[228:231], a[136:139], v[12:15], v[228:231]\n"
    "v_mfma_f32_16x16x32_bf16 v[232:235], a[140:143], v[12:15], v[232:235]\n"
    "v_mfma_f32_16x16x32_bf16 v[236:239], a[192:195], v[12:15], v[236:239]\n"
    "s_waitcnt vmcnt(17)\n"
    "v_mfma_f32_16x16x32_bf16 v[180:183], a[144:147], v[20:23], v[180:183]\n"
    "v_mfma_f32_16x16x32_bf16 v[184:187], a[148:151], v[20:23], v[184:187]\n"
    "v_mfma_f32_16x16x32_bf16 v[188:191], a[152:155], v[20:23], v[188:191]\n"
    "v_mfma_f32_16x16x32_bf16 v[192:195], a[156:159], v[20:23], v[192:195]\n"
    "v_mfma_f32_16x16x32_bf16 v[196:199], a[196:199], v[20:23], v[196:199]\n"
    "s_waitcnt vmcnt(16)\n"
    "v_mfma_f32_16x16x32_bf16 v[180:183], a[0:3], v[36:39], v[180:183]\n"
    "buffer_load_dwordx4 a[228:231], v254, s[4:7], s27 offen offset:1024 sc1\n"
    "v_mfma_f32_16x16x32_bf16 v[184:187], a[4:7], v[36:39], v[184:187]\n"
    "v_mfma_f32_16x16x32_bf16 v[188:191], a[8:11], v[36:39], v[188:191]\n"
    "v_mfma_f32_16x16x32_bf16 v[192:195], a[12:15], v[36:39], v[192:195]\n"
    "v_mfma_f32_16x16x32_bf16 v[196:199], a[200:203], v[36:39], v[196:199]\n"
    "s_waitcnt vmcnt(16)\n"
    "v_mfma_f32_16x16x32_bf16 v[180:183], a[16:19], v[52:55], v[180:183]\n"
    "s_waitcnt vmcnt(8)\n"
    "v_cmp_ne_u32 vcc, v159, v253\n"
    "s_nop 0\n"
    "s_and_b32 s58, vcc_lo, 0x1ff\n"
    "s_cbranch_scc1 .Lp8\n"
    ".Lpr8:\n"
    "v_mfma_f32_16x16x32_bf16 v[184:187], a[20:23], v[52:55], v[184:187]\n"
    "v_mfma_f32_16x16x32_bf16 v[188:191], a[24:27], v[52:55], v[188:191]\n"
    "buffer_load_dwordx4 v[64:67], v240, s[4:7], s16 offen offset:0 sc1\n"
    "v_mfma_f32_16x16x32_bf16 v[192:195], a[28:31], v[52:55], v[192:195]\n"
    "v_mfma_f32_16x16x32_bf16 v[196:199], a[204:207], v[52:55], v[196:199]\n"
    "buffer_load_dwordx4 v[68:71], v240, s[4:7], s16 offen offset:1024 sc1\n"
    "v_mfma_f32_16x16x32_bf16 v[200:203], a[144:147], v[24:27], v[200:203]\n"
    "v_mfma_f32_16x16x32_bf16 v[204:207], a[148:151], v[24:27], v[204:207]\n"
    "buffer_load_dwordx4 v[72:75], v240, s[4:7], s16 offen offset:2048 sc1\n"
    "ds_write_b128 v155, v[180:183] offset:0\n"
    "v_mfma_f32_16x16x32_bf16 v[208:211], a[152:155], v[24:27], v[208:211]\n"
    "v_mfma_f32_16x16x32_bf16 v[212:215], a[156:159], v[24:27], v[212:215]\n"
    "buffer_load_dwordx4 v[76:79], v240, s[4:7], s16 offen offset:3072 sc1\n"
    "v_mfma_f32_16x16x32_bf16 v[216:219], a[196:199], v[24:27], v[216:219]\n"
    "ds_write_b128 v155, v[184:187] offset:1024\n"
    "v_mfma_f32_16x16x32_bf16 v[200:203], a[0:3], v[40:43], v[200:203]\n"
    "buffer_load_dwordx4 v[80:83], v240, s[4:7], s17 offen offset:0 sc1\n"
    "v_mfma_f32_16x16x32_bf16 v[204:207], a[4:7], v[40:43], v[204:207]\n"
    "v_mfma_f32_16x16x32_bf16 v[208:211], a[8:11], v[40:43], v[208:211]\n"
    "buffer_load_dwordx4 v[84:87], v240, s[4:7], s17 offen offset:1024 sc1\n"
    "ds_write_b128 v155, v[188:191] offset:2048\n"
    "v_mfma_f32_16x16x32_bf16 v[212:215], a[12:15], v[40:43], v[212:215]\n"
    "v_mfma_f32_16x16x32_bf16 v[216:219], a[200:203], v[40:43], v[216:219]\n"
    "buffer_load_dwordx4 v[128:131], v150, s[4:7], s40 offen sc1\n"
    "v_mfma_f32_16x16x32_bf16 v[200:203], a[16:19], v[56:59], v[200:203]\n"
    "ds_write_b128 v155, v[192:195] offset:3072\n"
    "v_mfma_f32_16x16x32_bf16 v[204:207], a[20:23], v[56:59], v[204:207]\n"
    "buffer_load_dwordx4 v[132:135], v150, s[4:7], s40 offen offset:1024 sc1\n"
    "v_mfma_f32_16x16x32_bf16 v[208:211], a[24:27], v[56:59], v[208:211]\n"
    "v_mfma_f32_16x16x32_bf16 v[212:215], a[28:31], v[56:59], v[212:215]\n"
    "buffer_load_dwordx2 v[136:137], v151, s[4:7], s41 offen sc1\n"
    "ds_write_b128 v155, v[196:199] offset:4096\n"
    "v_mfma_f32_16x16x32_bf16 v[216:219], a[204:207], v[56:59], v[216:219]\n"
    "v_mfma_f32_16x16x32_bf16 v[220:223], a[144:147], v[28:31], v[220:223]\n"
    "v_and_b32 v159, 31, v255\n"
    "v_mfma_f32_16x16x32_bf16 v[224:227], a[148:151], v[28:31], v[224:227]\n"
    "v_lshlrev_b32 v159, 2, v159\n"
    "ds_write_b128 v156, v[200:203] offset:5120\n"
    "v_mfma_f32_16x16x32_bf16 v[228:231], a[152:155], v[28:31], v[228:231]\n"
    "v_lshl_or_b32 v159, s3, 28, v159\n"
    "v_mfma_f32_16x16x32_bf16 v[232:235], a[156:159], v[28:31], v[232:235]\n"
    "buffer_store_dword v253, v159, s[4:7], s1 offen sc1\n"
    "v_mfma_f32_16x16x32_bf16 v[236:239], a[196:199], v[28:31], v[236:239]\n"
    "s_cmpk_eq_u32 s2, 160\n"
    "s_cselect_b32 s35, 0, 0x7000000\n"
    "s_add_u32 s35, s35, 20201472\n"
    "buffer_store_dword v253, v159, s[4:7], s35 offen sc1\n"
    "ds_write_b128 v156, v[204:207] offset:6144\n"
    "v_mfma_f32_16x16x32_bf16 v[220:223], a[0:3], v[44:47], v[220:223]\n"
    "v_mfma_f32_16x16x32_bf16 v[224:227], a[4:7], v[44:47], v[224:227]\n"
    "v_mfma_f32_16x16x32_bf16 v[228:231], a[8:11], v[44:47], v[228:231]\n"
    "ds_write_b128 v156, v[208:211] offset:7168\n"
    "v_mfma_f32_16x16x32_bf16 v[232:235], a[12:15], v[44:47], v[232:235]\n"
    "v_mfma_f32_16x16x32_bf16 v[236:239], a[200:203], v[44:47], v[236:239]\n"
    "s_waitcnt vmcnt(18)\n"
    "v_mfma_f32_16x16x32_bf16 v[220:223], a[16:19], v[60:63], v[220:223]\n"
    "ds_write_b128 v156, v[212:215] offset:8192\n"
    "v_mfma_f32_16x16x32_bf16 v[224:227], a[20:23], v[60:63], v[224:227]\n"
    "v_mfma_f32_16x16x32_bf16 v[228:231], a[24:27], v[60:63], v[228:231]\n"
    "v_mfma_f32_16x16x32_bf16 v[232:235], a[28:31], v[60:63], v[232:235]\n"
    "ds_write_b128 v156, v[216:219] offset:9216\n"
    "v_mfma_f32_16x16x32_bf16 v[236:239], a[204:207], v[60:63], v[236:239]\n"
    "s_waitcnt vmcnt(17)\n"
    "v_mfma_f32_16x16x32_bf16 v[160:163], a[144:147], v[16:19], v[160:163]\n"
    "v_mfma_f32_16x16x32_bf16 v[164:167], a[148:151], v[16:19], v[164:167]\n"
    "ds_write_b128 v157, v[220:223] offset:10240\n"
    "v_mfma_f32_16x16x32_bf16 v[168:171], a[152:155], v[16:19], v[168:171]\n"
    "v_mfma_f32_16x16x32_bf16 v[172:175], a[156:159], v[16:19], v[172:175]\n"
    "v_mfma_f32_16x16x32_bf16 v[176:179], a[196:199], v[16:19], v[176:179]\n"
    "s_waitcnt vmcnt(15)\n"
    "ds_write_b128 v157, v[224:227] offset:11264\n"
    "v_mfma_f32_16x16x32_bf16 v[160:163], a[0:3], v[32:35], v[160:163]\n"
    "v_mfma_f32_16x16x32_bf16 v[164:167], a[4:7], v[32:35], v[164:167]\n"
    "v_mfma_f32_16x16x32_bf16 v[168:171], a[8:11], v[32:35], v[168:171]\n"
    "ds_write_b128 v157, v[228:231] offset:12288\n"
    "v_mfma_f32_16x16x32_bf16 v[172:175], a[12:15], v[32:35], v[172:175]\n"
    "v_mfma_f32_16x16x32_bf16 v[176:179], a[200:203], v[32:35], v[176:179]\n"
    "s_waitcnt vmcnt(14)\n"
    "v_mfma_f32_16x16x32_bf16 v[160:163], a[16:19], v[48:51], v[160:163]\n"
    "ds_write_b128 v157, v[232:235] offset:13312\n"
    "v_mfma_f32_16x16x32_bf16 v[164:167], a[20:23], v[48:51], v[164:167]\n"
    "v_mfma_f32_16x16x32_bf16 v[168:171], a[24:27], v[48:51], v[168:171]\n"
    "v_mfma_f32_16x16x32_bf16 v[172:175], a[28:31], v[48:51], v[172:175]\n"
    "ds_write_b128 v157, v[236:239] offset:14336\n"
    "v_mfma_f32_16x16x32_bf16 v[176:179], a[204:207], v[48:51], v[176:179]\n"
    "s_waitcnt vmcnt(10) lgkmcnt(14)\n"
    "v_mfma_f32_16x16x32_bf16 v[160:163], a[32:35], v[64:67], v[160:163]\n"
    "s_waitcnt vmcnt(4)\n"
    "v_permlane16_swap_b32 v128, v130\n"
    "v_mfma_f32_16x16x32_bf16 v[164:167], a[36:39], v[64:67], v[164:167]\n"
    "v_permlane16_swap_b32 v129, v131\n"
    "v_mfma_f32_16x16x32_bf16 v[168:171], a[40:43], v[64:67], v[168:171]\n"
    "s_waitcnt vmcnt(3)\n"
    "v_permlane16_swap_b32 v132, v134\n"
    "v_mfma_f32_16x16x32_bf16 v[172:175], a[44:47], v[64:67], v[172:175]\n"
    "v_permlane16_swap_b32 v133, v135\n"
    "v_mfma_f32_16x16x32_bf16 v[176:179], a[208:211], v[64:67], v[176:179]\n"
    "v_lshlrev_b32 v96, 16, v128\n"
    "v_and_b32 v97, 0xffff0000, v128\n"
    "v_mfma_f32_16x16x32_bf16 v[160:163], a[48:51], v[68:71], v[160:163]\n"
    "v_lshlrev_b32 v98, 16, v129\n"
    "v_and_b32 v99, 0xffff0000, v129\n"
    "v_mfma_f32_16x16x32_bf16 v[164:167], a[52:55], v[68:71], v[164:167]\n"
    "v_lshlrev_b32 v100, 16, v130\n"
    "v_and_b32 v101, 0xffff0000, v130\n"
    "v_mfma_f32_16x16x32_bf16 v[168:171], a[56:59], v[68:71], v[168:171]\n"
    "s_waitcnt lgkmcnt(0)\n"
    "s_barrier\n"
    "v_lshlrev_b32 v102, 16, v131\n"
    "v_and_b32 v103, 0xffff0000, v131\n"
    "v_mfma_f32_16x16x32_bf16 v[172:175], a[60:63], v[68:71], v[172:175]\n"
    "ds_read_b128 v[0:3], v154 offset:0\n"
    "ds_read_b128 v[4:7], v154 offset:5120\n"
    "v_lshlrev_b32 v104, 16, v132\n"
    "v_and_b32 v105, 0xffff0000, v132\n"
    "v_mfma_f32_16x16x32_bf16 v[176:179], a[212:215], v[68:71], v[176:179]\n"
    "ds_read_b128 v[8:11], v154 offset:10240\n"
    "ds_read_b128 v[12:15], v154 offset:1024\n"
    "v_lshlrev_b32 v106, 16, v133\n"
    "v_and_b32 v107, 0xffff0000, v133\n"
    "v_mfma_f32_16x16x32_bf16 v[160:163], a[64:67], v[72:75], v[160:163]\n"
    "ds_read_b128 v[16:19], v154 offset:6144\n"
    "ds_read_b128 v[20:23], v154 offset:11264\n"
    "v_lshlrev_b32 v108, 16, v134\n"
    "v_and_b32 v109, 0xffff0000, v134\n"
    "v_mfma_f32_16x16x32_bf16 v[164:167], a[68:71], v[72:75], v[164:167]\n"
    "ds_read_b128 v[24:27], v154 offset:2048\n"
    "ds_read_b128 v[28:31], v154 offset:7168\n"
    "v_lshlrev_b32 v110, 16, v135\n"
    "v_and_b32 v111, 0xffff0000, v135\n"
    "v_mfma_f32_16x16x32_bf16 v[168:171], a[72:75], v[72:75], v[168:171]\n"
    "s_waitcnt vmcnt(2)\n"
    "ds_read_b128 v[32:35], v154 offset:12288\n"
    "ds_read_b128 v[36:39], v154 offset:3072\n"
    "v_lshlrev_b32 v112, 16, v136\n"
    "v_and_b32 v113, 0xffff0000, v136\n"
    "v_mfma_f32_16x16x32_bf16 v[172:175], a[76:79], v[72:75], v[172:175]\n"
    "ds_read_b128 v[40:43], v154 offset:8192\n"
    "ds_read_b128 v[44:47], v154 offset:13312\n"
    "v_lshlrev_b32 v114, 16, v137\n"
    "v_and_b32 v115, 0xffff0000, v137\n"
    "v_mfma_f32_16x16x32_bf16 v[176:179], a[216:219], v[72:75], v[176:179]\n"
    "ds_read_b128 v[48:51], v154 offset:4096\n"
    "ds_read_b128 v[52:55], v154 offset:9216\n"
    "v_mfma_f32_16x16x32_bf16 v[160:163], a[80:83], v[76:79], v[160:163]\n"
    "ds_read_b128 v[56:59], v154 offset:14336\n"
    "v_mfma_f32_16x16x32_bf16 v[164:167], a[84:87], v[76:79], v[164:167]\n"
    "s_waitcnt lgkmcnt(13)\n"
    "v_add_f32 v0, v0, v4\n"
    "v_add_f32 v1, v1, v5\n"
    "v_mfma_f32_16x16x32_bf16 v[168:171], a[88:91], v[76:79], v[168:171]\n"
    "v_add_f32 v2, v2, v6\n"
    "v_add_f32 v3, v3, v7\n"
    "v_mfma_f32_16x16x32_bf16 v[172:175], a[92:95], v[76:79], v[172:175]\n"
    "s_waitcnt lgkmcnt(12)\n"
    "v_add_f32 v8, v8, v96\n"
    "v_add_f32 v9, v9, v97\n"
    "v_mfma_f32_16x16x32_bf16 v[176:179], a[220:223], v[76:79], v[176:179]\n"
    "v_add_f32 v10, v10, v98\n"
    "v_add_f32 v11, v11, v99\n"
    "v_mfma_f32_16x16x32_bf16 v[160:163], a[96:99], v[80:83], v[160:163]\n"
    "v_add_f32 v0, v0, v8\n"
    "v_add_f32 v1, v1, v9\n"
    "v_mfma_f32_16x16x32_bf16 v[164:167], a[100:103], v[80:83], v[164:167]\n"
    "v_add_f32 v2, v2, v10\n"
    "v_add_f32 v3, v3, v11\n"
    "v_mfma_f32_16x16x32_bf16 v[168:171], a[104:107], v[80:83], v[168:171]\n"
    "s_waitcnt lgkmcnt(10)\n"
    "v_add_f32 v12, v12, v16\n"
    "v_add_f32 v13, v13, v17\n"
    "v_mfma_f32_16x16x32_bf16 v[172:175], a[108:111], v[80:83], v[172:175]\n"
    "v_add_f32 v14, v14, v18\n"
    "v_add_f32 v15, v15, v19\n"
    "v_mfma_f32_16x16x32_bf16 v[176:179], a[224:227], v[80:83], v[176:179]\n"
    "s_waitcnt lgkmcnt(9)\n"
    "v_add_f32 v20, v20, v100\n"
    "v_add_f32 v21, v21, v101\n"
    "v_mfma_f32_16x16x32_bf16 v[160:163], a[112:115], v[84:87], v[160:163]\n"
    "v_add_f32 v22, v22, v102\n"
    "v_add_f32 v23, v23, v103\n"
    "v_mfma_f32_16x16x32_bf16 v[164:167], a[116:119], v[84:87], v[164:167]\n"
    "v_add_f32 v12, v12, v20\n"
    "v_add_f32 v13, v13, v21\n"
    "v_mfma_f32_16x16x32_bf16 v[168:171], a[120:123], v[84:87], v[168:171]\n"
    "v_add_f32 v14, v14, v22\n"
    "v_add_f32 v15, v15, v23\n"
    "v_mfma_f32_16x16x32_bf16 v[172:175], a[124:127], v[84:87], v[172:175]\n"
    "v_mfma_f32_16x16x32_bf16 v[176:179], a[228:231], v[84:87], v[176:179]\n"
    "s_waitcnt lgkmcnt(8)\n"
    "v_pk_add_f32 v[160:161], v[160:161], v[0:1]\n"
    "v_pk_add_f32 v[162:163], v[162:163], v[2:3]\n"
    "v_cvt_pk_bf16_f32 v128, v160, v161\n"
    "v_cvt_pk_bf16_f32 v129, v162, v163\n"
    "v_pk_add_f32 v[164:165], v[164:165], v[12:13]\n"
    "v_pk_add_f32 v[166:167], v[166:167], v[14:15]\n"
    "v_cvt_pk_bf16_f32 v130, v164, v165\n"
    "v_cvt_pk_bf16_f32 v131, v166, v167\n"
    "s_nop 0\n"
    "v_permlane16_swap_b32 v128, v130\n"
    "v_permlane16_swap_b32 v129, v131\n"
    "buffer_store_dwordx4 v[128:131], v152, s[20:23], s42 offen offset:0\n"
    "v_pk_add_f32 v[24:25], v[24:25], v[104:105]\n"
    "v_pk_add_f32 v[26:27], v[26:27], v[106:107]\n"
    "s_waitcnt lgkmcnt(6)\n"
    "v_pk_add_f32 v[28:29], v[28:29], v[32:33]\n"
    "v_pk_add_f32 v[30:31], v[30:31], v[34:35]\n"
    "v_pk_add_f32 v[168:169], v[168:169], v[24:25]\n"
    "v_pk_add_f32 v[170:171], v[170:171], v[26:27]\n"
    "v_pk_add_f32 v[168:169], v[168:169], v[28:29]\n"
    "v_pk_add_f32 v[170:171], v[170:171], v[30:31]\n"
    "v_cvt_pk_bf16_f32 v132, v168, v169\n"
    "v_cvt_pk_bf16_f32 v133, v170, v171\n"
    "s_waitcnt lgkmcnt(5)\n"
    "v_pk_add_f32 v[36:37], v[36:37], v[108:109]\n"
    "v_pk_add_f32 v[38:39], v[38:39], v[110:111]\n"
    "s_waitcnt lgkmcnt(3)\n"
    "v_pk_add_f32 v[40:41], v[40:41], v[44:45]\n"
    "v_pk_add_f32 v[42:43], v[42:43], v[46:47]\n"
    "v_pk_add_f32 v[172:173], v[172:173], v[36:37]\n"
    "v_pk_add_f32 v[174:175], v[174:175], v[38:39]\n"
    "v_pk_add_f32 v[172:173], v[172:173], v[40:41]\n"
    "v_pk_add_f32 v[174:175], v[174:175], v[42:43]\n"
    "v_cvt_pk_bf16_f32 v134, v172, v173\n"
    "v_cvt_pk_bf16_f32 v135, v174, v175\n"
    "s_nop 0\n"
    "v_permlane16_swap_b32 v132, v134\n"
    "v_permlane16_swap_b32 v133, v135\n"
    "buffer_store_dwordx4 v[132:135], v152, s[20:23], s42 offen offset:64\n"
    "s_waitcnt lgkmcnt(2)\n"
    "v_pk_add_f32 v[48:49], v[48:49], v[112:113]\n"
    "v_pk_add_f32 v[50:51], v[50:51], v[114:115]\n"
    "s_waitcnt lgkmcnt(0)\n"
    "v_pk_add_f32 v[52:53], v[52:53], v[56:57]\n"
    "v_pk_add_f32 v[54:55], v[54:55], v[58:59]\n"
    "v_pk_add_f32 v[176:177], v[176:177], v[48:49]\n"
    "v_pk_add_f32 v[178:179], v[178:179], v[50:51]\n"
    "v_pk_add_f32 v[176:177], v[176:177], v[52:53]\n"
    "v_pk_add_f32 v[178:179], v[178:179], v[54:55]\n"
    "v_cvt_pk_bf16_f32 v136, v176, v177\n"
    "v_cvt_pk_bf16_f32 v137, v178, v179\n"
    "buffer_store_dwordx2 v[136:137], v153, s[20:23], s43 offen offset:1024\n"
    ".Lfin:\n"
    ".Lexit:\n"
    "s_endpgm\n"
    ".Lidle:\n"
    ".Lpad8:\n"
    "s_cbranch_execz .Lpad9\n"
    "s_add_u32 m0, s53, 16384\n"
    "s_add_u32 s54, s52, 163840\n"
    "buffer_load_dwordx4 v249, s[8:11], s54 offen lds\n"
    "s_add_u32 m0, s53, 17408\n"
    "s_add_u32 s54, s52, 174080\n"
    "buffer_load_dwordx4 v249, s[8:11], s54 offen lds\n"
    "s_add_u32 m0, s53, 18432\n"
    "s_add_u32 s54, s52, 184320\n"
    "buffer_load_dwordx4 v249, s[8:11], s54 offen lds\n"
    "s_add_u32 m0, s53, 19456\n"
    "s_add_u32 s54, s52, 194560\n"
    "buffer_load_dwordx4 v249, s[8:11], s54 offen lds\n"
    "s_add_u32 m0, s53, 32768\n"
    "s_add_u32 s54, s52, 327680\n"
    "buffer_load_dwordx4 v249, s[8:11], s54 offen lds\n"
    "s_add_u32 m0, s53, 33792\n"
    "s_add_u32 s54, s52, 337920\n"
    "buffer_load_dwordx4 v249, s[8:11], s54 offen lds\n"
    "s_add_u32 m0, s53, 34816\n"
    "s_add_u32 s54, s52, 348160\n"
    "buffer_load_dwordx4 v249, s[8:11], s54 offen lds\n"
    "s_add_u32 m0, s53, 35840\n"
    "s_add_u32 s54, s52, 358400\n"
    "buffer_load_dwordx4 v249, s[8:11], s54 offen lds\n"
    "s_add_u32 m0, s53, 49152\n"
    "s_add_u32 s54, s52, 491520\n"
    "buffer_load_dwordx4 v249, s[8:11], s54 offen lds\n"
    "s_add_u32 m0, s53, 50176\n"
    "s_add_u32 s54, s52, 501760\n"
    "buffer_load_dwordx4 v249, s[8:11], s54 offen lds\n"
    "s_add_u32 m0, s53, 51200\n"
    "s_add_u32 s54, s52, 512000\n"
    "buffer_load_dwordx4 v249, s[8:11], s54 offen lds\n"
    "s_add_u32 m0, s53, 52224\n"
    "s_add_u32 s54, s52, 522240\n"
    "buffer_load_dwordx4 v249, s[8:11], s54 offen lds\n"
    ".Lidle2:\n"
    ".Lpad9:\n"
    "s_cbranch_execz .Lpad10\n"
    "s_and_b32 s40, s28, 7\n"
    "s_mul_i32 s40, s40, 8192\n"
    "s_add_u32 s40, s40, 20267008\n"
    "s_sub_u32 s34, s2, 160\n"
    "s_lshl_b32 s34, s34, 2\n"
    "s_add_u32 s34, s34, s3\n"
    "s_mul_i32 s34, s34, 3840\n"
    "s_add_u32 s34, s34, 1310720\n"
    "v_lshrrev_b32 v4, 3, v255\n"
    "v_min_u32 v4, 2, v4\n"
    "v_mul_u32_u24 v4, 0x500, v4\n"
    "v_and_b32 v5, 7, v255\n"
    "v_lshl_add_u32 v5, v5, 7, v4\n"
    "buffer_load_dword a253, v5, s[8:11], s34 offen\n"
    "s_add_u32 m0, s53, 65536\n"
    "s_add_u32 s54, s52, 655360\n"
    "buffer_load_dwordx4 v249, s[8:11], s54 offen lds\n"
    "s_add_u32 m0, s53, 66560\n"
    "s_add_u32 s54, s52, 665600\n"
    "buffer_load_dwordx4 v249, s[8:11], s54 offen lds\n"
    "s_add_u32 m0, s53, 67584\n"
    "s_add_u32 s54, s52, 675840\n"
    "buffer_load_dwordx4 v249, s[8:11], s54 offen lds\n"
    "s_add_u32 m0, s53, 68608\n"
    "s_add_u32 s54, s52, 686080\n"
    "buffer_load_dwordx4 v249, s[8:11], s54 offen lds\n"
    "s_add_u32 m0, s53, 81920\n"
    "s_add_u32 s54, s52, 819200\n"
    "buffer_load_dwordx4 v249, s[8:11], s54 offen lds\n"
    "s_add_u32 m0, s53, 82944\n"
    "s_add_u32 s54, s52, 829440\n"
    "buffer_load_dwordx4 v249, s[8:11], s54 offen lds\n"
    "s_add_u32 m0, s53, 83968\n"
    "s_add_u32 s54, s52, 839680\n"
    "buffer_load_dwordx4 v249, s[8:11], s54 offen lds\n"
    "s_add_u32 m0, s53, 84992\n"
    "s_add_u32 s54, s52, 849920\n"
    "buffer_load_dwordx4 v249, s[8:11], s54 offen lds\n"
    "s_add_u32 m0, s53, 98304\n"
    "s_add_u32 s54, s52, 983040\n"
    "buffer_load_dwordx4 v249, s[8:11], s54 offen lds\n"
    "s_add_u32 m0, s53, 99328\n"
    "s_add_u32 s54, s52, 993280\n"
    ".Lpad10:\n"
    "s_cbranch_execz .Lpad11\n"
    "buffer_load_dwordx4 v249, s[8:11], s54 offen lds\n"
    "s_add_u32 m0, s53, 100352\n"
    "s_add_u32 s54, s52, 1003520\n"
    "buffer_load_dwordx4 v249, s[8:11], s54 offen lds\n"
    "s_add_u32 m0, s53, 101376\n"
    "s_add_u32 s54, s52, 1013760\n"
    "buffer_load_dwordx4 v249, s[8:11], s54 offen lds\n"
    "s_add_u32 m0, s53, 114688\n"
    "s_add_u32 s54, s52, 1146880\n"
    "buffer_load_dwordx4 v249, s[8:11], s54 offen lds\n"
    "s_add_u32 m0, s53, 115712\n"
    "s_add_u32 s54, s52, 1157120\n"
    "buffer_load_dwordx4 v249, s[8:11], s54 offen lds\n"
    "s_add_u32 m0, s53, 116736\n"
    "s_add_u32 s54, s52, 1167360\n"
    "buffer_load_dwordx4 v249, s[8:11], s54 offen lds\n"
    "s_add_u32 m0, s53, 117760\n"
    "s_add_u32 s54, s52, 1177600\n"
    "buffer_load_dwordx4 v249, s[8:11], s54 offen lds\n"
    "s_mul_i32 s12, s3, 10240\n"
    "s_mul_i32 s16, s32, 40960\n"
    "s_add_u32 s16, s16, s12\n"
    "s_add_u32 s16, s16, 5242880\n"
    "s_add_u32 s13, s12, 40960\n"
    "s_add_u32 s17, s16, 655360\n"
    "s_lshl_b32 s36, s3, 10\n"
    "s_add_u32 s15, s36, 122880\n"
    "s_and_b32 s34, s28, 1\n"
    "s_mul_i32 s34, s34, 3\n"
    "s_lshr_b32 s35, s29, 2\n"
    "s_add_u32 s34, s34, s35\n"
    "s_add_u32 s34, s34, 43\n"
    "s_mul_i32 s19, s34, 40960\n"
    "s_add_u32 s19, s19, s36\n"
    "s_add_u32 s19, s19, 5242880\n"
    "s_lshl_b32 s14, s3, 10\n"
    "s_mul_i32 s18, s32, 40960\n"
    "s_add_u32 s18, s18, s14\n"
    "s_add_u32 s18, s18, 6553600\n"
    "s_add_u32 s14, s14, 81920\n"
    "v_mov_b32 v244, v248\n"
    "s_cmp_lg_u32 s3, 0\n"
    "s_cbranch_scc1 .Li_nopoll\n"
    "buffer_load_dwordx4 v[0:3], v244, s[4:7], s40 offen sc1\n"
    "s_waitcnt vmcnt(0)\n"
    "v_cmp_ne_u32 vcc, v0, v253\n"
    "s_nop 0\n"
    "v_cmp_ne_u32 s[58:59], v1, v253\n"
    "s_nop 0\n"
    "s_or_b64 vcc, vcc, s[58:59]\n"
    "v_cmp_ne_u32 s[58:59], v2, v253\n"
    "s_nop 0\n"
    "s_or_b64 vcc, vcc, s[58:59]\n"
    "v_cmp_ne_u32 s[58:59], v3, v253\n"
    "s_nop 0\n"
    "s_or_b64 vcc, vcc, s[58:59]\n"
    "s_or_b32 s58, vcc_lo, vcc_hi\n"
    "s_cbranch_scc1 .Lp3\n"
    ".Lpr3:\n"
    ".Li_nopoll:\n"
    "s_waitcnt lgkmcnt(0)\n"
    "s_barrier\n"
    "s_waitcnt vmcnt(0)\n"
    "s_add_u32 m0, s15, 8192\n"
    "s_add_u32 s47, s19, 8192\n"
    "buffer_load_dwordx4 v254, s[4:7], s47 offen sc1 lds\n"
    "ds_read_b64_tr_b16 a[0:1], v240 offset:0\n"
    "ds_read_b64_tr_b16 a[2:3], v240 offset:2048\n"
    "ds_read_b64_tr_b16 a[4:5], v241 offset:0\n"
    "ds_read_b64_tr_b16 a[6:7], v241 offset:2048\n"
    "ds_read_b64_tr_b16 a[8:9], v242 offset:0\n"
    "ds_read_b64_tr_b16 a[10:11], v242 offset:2048\n"
    "ds_read_b64_tr_b16 a[12:13], v243 offset:0\n"
    "ds_read_b64_tr_b16 a[14:15], v243 offset:2048\n"
    "s_add_u32 m0, s15, 12288\n"
    "s_add_u32 s47, s19, 12288\n"
    "buffer_load_dwordx4 v254, s[4:7], s47 offen sc1 lds\n"
    "ds_read_b64_tr_b16 a[16:17], v240 offset:16384\n"
    "ds_read_b64_tr_b16 a[18:19], v240 offset:18432\n"
    "ds_read_b64_tr_b16 a[20:21], v241 offset:16384\n"
    "ds_read_b64_tr_b16 a[22:23], v241 offset:18432\n"
    "ds_read_b64_tr_b16 a[24:25], v242 offset:16384\n"
    "ds_read_b64_tr_b16 a[26:27], v242 offset:18432\n"
    "ds_read_b64_tr_b16 a[28:29], v243 offset:16384\n"
    "s_add_u32 m0, s15, 16384\n"
    "s_add_u32 s47, s19, 16384\n"
    "buffer_load_dwordx4 v254, s[4:7], s47 offen sc1 lds\n"
    "ds_read_b64_tr_b16 a[30:31], v243 offset:18432\n"
    "ds_read_b64_tr_b16 a[32:33], v240 offset:32768\n"
    "ds_read_b64_tr_b16 a[34:35], v240 offset:34816\n"
    "ds_read_b64_tr_b16 a[36:37], v241 offset:32768\n"
    "ds_read_b64_tr_b16 a[38:39], v241 offset:34816\n"
    "ds_read_b64_tr_b16 a[40:41], v242 offset:32768\n"
    "ds_read_b64_tr_b16 a[42:43], v242 offset:34816\n"
    "ds_read_b64_tr_b16 a[44:45], v243 offset:32768\n"
    "s_add_u32 m0, s15, 20480\n"
    "s_add_u32 s47, s19, 20480\n"
    "buffer_load_dwordx4 v254, s[4:7], s47 offen sc1 lds\n"
    "ds_read_b64_tr_b16 a[46:47], v243 offset:34816\n"
    "ds_read_b64_tr_b16 a[48:49], v240 offset:49152\n"
    "ds_read_b64_tr_b16 a[50:51], v240 offset:51200\n"
    "ds_read_b64_tr_b16 a[52:53], v241 offset:49152\n"
    "ds_read_b64_tr_b16 a[54:55], v241 offset:51200\n"
    "ds_read_b64_tr_b16 a[56:57], v242 offset:49152\n"
    "ds_read_b64_tr_b16 a[58:59], v242 offset:51200\n"
    "s_add_u32 m0, s15, 24576\n"
    "s_add_u32 s47, s19, 24576\n"
    "buffer_load_dwordx4 v254, s[4:7], s47 offen sc1 lds\n"
    "ds_read_b64_tr_b16 a[60:61], v243 offset:49152\n"
    "ds_read_b64_tr_b16 a[62:63], v243 offset:51200\n"
    "v_add_u32 v240, 0x10000, v240\n"
    "v_add_u32 v241, 0x10000, v241\n"
    "v_add_u32 v242, 0x10000, v242\n"
    "v_add_u32 v243, 0x10000, v243\n"
    "ds_read_b64_tr_b16 a[64:65], v240 offset:0\n"
    "ds_read_b64_tr_b16 a[66:67], v240 offset:2048\n"
    "s_add_u32 m0, s15, 28672\n"
    "s_add_u32 s47, s19, 28672\n"
    "buffer_load_dwordx4 v254, s[4:7], s47 offen sc1 lds\n"
    "ds_read_b64_tr_b16 a[68:69], v241 offset:0\n"
    "ds_read_b64_tr_b16 a[70:71], v241 offset:2048\n"
    "ds_read_b64_tr_b16 a[72:73], v242 offset:0\n"
    "ds_read_b64_tr_b16 a[74:75], v242 offset:2048\n"
    "ds_read_b64_tr_b16 a[76:77], v243 offset:0\n"
    "ds_read_b64_tr_b16 a[78:79], v243 offset:2048\n"
    "ds_read_b64_tr_b16 a[80:81], v240 offset:16384\n"
    "s_add_u32 m0, s15, 32768\n"
    "s_add_u32 s47, s19, 32768\n"
    "buffer_load_dwordx4 v254, s[4:7], s47 offen sc1 lds\n"
    "ds_read_b64_tr_b16 a[82:83], v240 offset:18432\n"
    "ds_read_b64_tr_b16 a[84:85], v241 offset:16384\n"
    "ds_read_b64_tr_b16 a[86:87], v241 offset:18432\n"
    "ds_read_b64_tr_b16 a[88:89], v242 offset:16384\n"
    "ds_read_b64_tr_b16 a[90:91], v242 offset:18432\n"
    "ds_read_b64_tr_b16 a[92:93], v243 offset:16384\n"
    "ds_read_b64_tr_b16 a[94:95], v243 offset:18432\n"
    "ds_read_b64_tr_b16 a[96:97], v240 offset:32768\n"
    "s_add_u32 m0, s15, 36864\n"
    "s_add_u32 s47, s19, 36864\n"
    "buffer_load_dwordx4 v254, s[4:7], s47 offen sc1 lds\n"
    "ds_read_b64_tr_b16 a[98:99], v240 offset:34816\n"
    "ds_read_b64_tr_b16 a[100:101], v241 offset:32768\n"
    "ds_read_b64_tr_b16 a[102:103], v241 offset:34816\n"
    "ds_read_b64_tr_b16 a[104:105], v242 offset:32768\n"
    "ds_read_b64_tr_b16 a[106:107], v242 offset:34816\n"
    "ds_read_b64_tr_b16 a[108:109], v243 offset:32768\n"
    "ds_read_b64_tr_b16 a[110:111], v243 offset:34816\n"
    "ds_read_b64_tr_b16 a[112:113], v240 offset:49152\n"
    "ds_read_b64_tr_b16 a[114:115], v240 offset:51200\n"
    "ds_read_b64_tr_b16 a[116:117], v241 offset:49152\n"
    "ds_read_b64_tr_b16 a[118:119], v241 offset:51200\n"
    "ds_read_b64_tr_b16 a[120:121], v242 offset:49152\n"
    "ds_read_b64_tr_b16 a[122:123], v242 offset:51200\n"
    "ds_read_b64_tr_b16 a[124:125], v243 offset:49152\n"
    "ds_read_b64_tr_b16 a[126:127], v243 offset:51200\n"
    "s_waitcnt lgkmcnt(0)\n"
    "s_barrier\n"
    "s_add_u32 m0, s12, 0\n"
    "s_add_u32 s47, s16, 0\n"
    "buffer_load_dwordx4 v254, s[4:7], s47 offen offset:0 sc1 lds\n"
    "buffer_load_dwordx4 v254, s[4:7], s47 offen offset:1024 sc1 lds\n"
    "buffer_load_dwordx4 v254, s[4:7], s47 offen offset:2048 sc1 lds\n"
    "buffer_load_dwordx4 v254, s[4:7], s47 offen offset:3072 sc1 lds\n"
    "s_add_u32 m0, s12, 4096\n"
    "s_add_u32 s47, s16, 4096\n"
    "buffer_load_dwordx4 v254, s[4:7], s47 offen offset:0 sc1 lds\n"
    "buffer_load_dwordx4 v254, s[4:7], s47 offen offset:1024 sc1 lds\n"
    "buffer_load_dwordx4 v254, s[4:7], s47 offen offset:2048 sc1 lds\n"
    "buffer_load_dwordx4 v254, s[4:7], s47 offen offset:3072 sc1 lds\n"
    "s_add_u32 m0, s12, 8192\n"
    "s_add_u32 s47, s16, 8192\n"
    "buffer_load_dwordx4 v254, s[4:7], s47 offen offset:0 sc1 lds\n"
    "buffer_load_dwordx4 v254, s[4:7], s47 offen offset:1024 sc1 lds\n"
    "s_add_u32 m0, s13, 0\n"
    "s_add_u32 s47, s17, 0\n"
    "buffer_load_dwordx4 v254, s[4:7], s47 offen offset:0 sc1 lds\n"
    "buffer_load_dwordx4 v254, s[4:7], s47 offen offset:1024 sc1 lds\n"
    "buffer_load_dwordx4 v254, s[4:7], s47 offen offset:2048 sc1 lds\n"
    "buffer_load_dwordx4 v254, s[4:7], s47 offen offset:3072 sc1 lds\n"
    "s_add_u32 m0, s13, 4096\n"
    "s_add_u32 s47, s17, 4096\n"
    "buffer_load_dwordx4 v254, s[4:7], s47 offen offset:0 sc1 lds\n"
    "buffer_load_dwordx4 v254, s[4:7], s47 offen offset:1024 sc1 lds\n"
    "buffer_load_dwordx4 v254, s[4:7], s47 offen offset:2048 sc1 lds\n"
    "buffer_load_dwordx4 v254, s[4:7], s47 offen offset:3072 sc1 lds\n"
    "s_add_u32 m0, s13, 8192\n"
    "s_add_u32 s47, s17, 8192\n"
    "buffer_load_dwordx4 v254, s[4:7], s47 offen offset:0 sc1 lds\n"
    "buffer_load_dwordx4 v254, s[4:7], s47 offen offset:1024 sc1 lds\n"
    "s_add_u32 m0, s14, 0\n"
    "s_add_u32 s47, s18, 0\n"
    "buffer_load_dwordx4 v254, s[4:7], s47 offen sc1 lds\n"
    "s_add_u32 m0, s14, 4096\n"
    "s_add_u32 s47, s18, 4096\n"
    "buffer_load_dwordx4 v254, s[4:7], s47 offen sc1 lds\n"
    "s_add_u32 m0, s14, 8192\n"
    "s_add_u32 s47, s18, 8192\n"
    "buffer_load_dwordx4 v254, s[4:7], s47 offen sc1 lds\n"
    "s_add_u32 m0, s14, 12288\n"
    "s_add_u32 s47, s18, 12288\n"
    "buffer_load_dwordx4 v254, s[4:7], s47 offen sc1 lds\n"
    "s_add_u32 m0, s15, 0\n"
    "s_add_u32 s47, s19, 0\n"
    "buffer_load_dwordx4 v254, s[4:7], s47 offen sc1 lds\n"
    "s_add_u32 m0, s15, 4096\n"
    "s_add_u32 s47, s19, 4096\n"
    "buffer_load_dwordx4 v254, s[4:7], s47 offen sc1 lds\n"
    "s_add_u32 s0, s40, 65536\n"
    "v_min_u32 v220, 4992, v248\n"
    "v_lshl_or_b32 v220, s3, 28, v220\n"
    "buffer_load_dwordx4 v[216:219], v220, s[4:7], s0 offen sc1\n"
    "s_waitcnt vmcnt(0)\n"
    "s_nop 4\n"
    "s_branch .Lgemm1\n"
    ".Lrole_b:\n"
    "s_mov_b32 s10, 2211840\n"
    "s_mov_b32 s11, 0x20000\n"
    "s_lshl_b32 s26, s29, 2\n"
    "s_add_u32 s26, s26, s3\n"
    "s_mul_i32 s26, s26, 14080\n"
    "s_and_b32 s27, s28, 1\n"
    "s_lshl_b32 s27, s27, 9\n"
    "s_add_u32 s26, s26, s27\n"
    "v_lshrrev_b32 v242, 2, v255\n"
    "v_min_u32 v242, 10, v242\n"
    "v_mul_u32_u24 v242, 0x500, v242\n"
    "v_and_b32 v243, 3, v255\n"
    "v_lshl_add_u32 v159, v243, 7, v242\n"
    "v_mov_b32 v13, 0x7fffffff\n"
    "v_cmp_gt_u32 vcc, 2, v255\n"
    "s_nop 1\n"
    "v_cndmask_b32 v13, v13, v254, vcc\n"
    "s_mov_b32 s10, 2211840\n"
    "s_mov_b32 s11, 0x20000\n"
    "s_mov_b32 s22, 2621440\n"
    "s_mov_b32 s23, 0x20000\n"
    "s_lshr_b32 s34, s28, 1\n"
    "s_lshl_b32 s31, s34, 2\n"
    "s_lshl_b32 s33, s34, 3\n"
    "s_and_b32 s34, s28, 1\n"
    "s_lshl_b32 s32, s34, 3\n"
    "s_lshl_b32 s30, s34, 2\n"
    "s_and_b32 s34, s29, 3\n"
    "s_add_u32 s31, s31, s34\n"
    "s_add_u32 s30, s30, s34\n"
    "s_lshr_b32 s50, s29, 2\n"
    "s_add_u32 s32, s32, s50\n"
    "s_add_u32 s33, s33, s50\n"
    "s_add_u32 s50, s50, 7\n"
    "s_cmp_ge_u32 s50, 8\n"
    "s_cselect_b32 s34, 8, 0\n"
    "s_sub_u32 s50, s50, s34\n"
    "s_nop 0\n"
    "s_nop 0\n"
    "s_nop 0\n"
    "s_nop 0\n"
    "v_lshrrev_b32 v246, 3, v255\n"
    "v_and_b32 v247, 6, v246\n"
    "v_xor_b32 v247, v247, v255\n"
    "v_and_b32 v247, 7, v247\n"
    "v_mul_u32_u24 v246, 0x500, v246\n"
    "v_lshl_add_u32 v249, v247, 4, v246\n"
    "v_mul_u32_u24 v248, 128, v255\n"
    "s_mul_i32 s52, s3, 40960\n"
    "s_lshl_b32 s54, s30, 7\n"
    "s_add_u32 s52, s52, s54\n"
    "s_mul_i32 s53, s3, 4096\n"
    "v_lshrrev_b32 v244, 2, v251\n"
    "v_lshl_add_u32 v244, v250, 2, v244\n"
    "v_bfe_u32 v245, v255, 1, 1\n"
    "v_and_b32 v246, 6, v244\n"
    "v_or_b32 v245, v245, v246\n"
    "v_and_b32 v246, 1, v255\n"
    "v_lshlrev_b32 v246, 3, v246\n"
    "v_lshl_add_u32 v246, v244, 7, v246\n"
    "v_add_u32 v246, s53, v246\n"
    "v_xor_b32 v247, 0, v245\n"
    "v_lshl_add_u32 v240, v247, 4, v246\n"
    "v_xor_b32 v247, 2, v245\n"
    "v_lshl_add_u32 v241, v247, 4, v246\n"
    "v_xor_b32 v247, 4, v245\n"
    "v_lshl_add_u32 v242, v247, 4, v246\n"
    "v_xor_b32 v247, 6, v245\n"
    "v_lshl_add_u32 v243, v247, 4, v246\n"
    "s_lshr_b32 s55, s2, 2\n"
    "s_mul_i32 s55, s55, 128\n"
    "s_and_b32 s35, s2, 3\n"
    "s_lshl_b32 s35, s35, 2\n"
    "s_add_u32 s55, s55, s35\n"
    "s_mul_i32 s35, s3, 16384\n"
    "s_add_u32 s55, s55, s35\n"
    "s_add_u32 s55, s55, 20267008\n"
    "s_cmp_lt_u32 s3, 8\n"
    "s_cselect_b32 s35, 2, 0\n"
    "v_mul_u32_u24 v246, 8192, v255\n"
    "v_mov_b32 v247, 0x7fffffff\n"
    "v_cmp_gt_u32 vcc, s35, v255\n"
    "s_nop 1\n"
    "v_cndmask_b32 v252, v247, v246, vcc\n"
    "s_add_u32 m0, s42, 122880\n"
    "s_add_u32 s45, s41, 8192\n"
    "buffer_load_dwordx4 v254, s[12:15], s45 offen offset:0  lds\n"
    "buffer_load_dwordx4 v254, s[12:15], s45 offen offset:1024  lds\n"
    "buffer_load_dwordx4 v13, s[12:15], s45 offen offset:2048  lds\n"
    "s_mov_b32 s6, 21544960\n"
    "s_mov_b32 s7, 0x20000\n"
    "s_mov_b32 s34, 20201472\n"
    "v_mov_b32 v5, 0\n"
    "buffer_load_dword v253, v5, s[4:7], s34 offen sc1\n"
    "s_lshl_b32 s34, s2, 2\n"
    "s_add_u32 s34, s34, s3\n"
    "s_cmp_lt_u32 s34, 1024\n"
    "s_cselect_b32 s35, 2, 0\n"
    "s_lshl_b32 s34, s34, 6\n"
    "s_add_u32 s34, s34, 20267040\n"
    "v_cmp_gt_u32 vcc, s35, v255\n"
    "v_mul_u32_u24 v6, 65536, v255\n"
    "v_mov_b32 v7, 0x7fffffff\n"
    "v_cndmask_b32 v6, v7, v6, vcc\n"
    "buffer_store_dword v5, v6, s[4:7], s34 offen sc1\n"
    "s_cmp_ge_u32 s29, 20\n"
    "s_cbranch_scc1 .Lb_nc\n"
    "s_lshl_b32 s34, s3, 4\n"
    "v_add_u32 v0, s34, v251\n"
    "v_cvt_f32_u32 v0, v0\n"
    "v_lshlrev_b32 v1, 2, v250\n"
    "v_cvt_f32_u32 v2, v1\n"
    "v_cvt_f32_u32 v3, v251\n"
    "v_add_u32 v1, s34, v1\n"
    "v_cvt_f32_u32 v1, v1\n"
    "v_mul_f32 v4, 0x3c800000, v0\n"
    "v_mul_f32 v5, v4, v2\n"
    "v_and_b32 v12, 1, v255\n"
    "v_mul_u32_u24 v12, 0x8000, v12\n"
    "v_lshl_or_b32 v12, v12, 16, v12\n"
    "v_cos_f32 v8, v5\n"
    "v_sin_f32 v10, v5\n"
    "v_fmamk_f32 v6, v4, 0x3f800000, v5\n"
    "v_cos_f32 v9, v6\n"
    "v_sin_f32 v11, v6\n"
    "v_cvt_pk_bf16_f32 v162, v8, v9\n"
    "v_cvt_pk_bf16_f32 v164, v10, v11\n"
    "v_xor_b32 v160, 0x80008000, v164\n"
    "v_fmamk_f32 v6, v4, 0x40000000, v5\n"
    "v_cos_f32 v8, v6\n"
    "v_sin_f32 v10, v6\n"
    "v_fmamk_f32 v6, v4, 0x40400000, v5\n"
    "v_cos_f32 v9, v6\n"
    "v_sin_f32 v11, v6\n"
    "v_cvt_pk_bf16_f32 v163, v8, v9\n"
    "v_cvt_pk_bf16_f32 v165, v10, v11\n"
    "v_xor_b32 v161, 0x80008000, v165\n"
    "v_fmamk_f32 v6, v4, 0x41800000, v5\n"
    "v_cos_f32 v8, v6\n"
    "v_sin_f32 v10, v6\n"
    "v_fmamk_f32 v6, v4, 0x41880000, v5\n"
    "v_cos_f32 v9, v6\n"
    "v_sin_f32 v11, v6\n"
    "v_cvt_pk_bf16_f32 v168, v8, v9\n"
    "v_cvt_pk_bf16_f32 v170, v10, v11\n"
    "v_xor_b32 v166, 0x80008000, v170\n"
    "v_fmamk_f32 v6, v4, 0x41900000, v5\n"
    "v_cos_f32 v8, v6\n"
    "v_sin_f32 v10, v6\n"
    "v_fmamk_f32 v6, v4, 0x41980000, v5\n"
    "v_cos_f32 v9, v6\n"
    "v_sin_f32 v11, v6\n"
    "v_cvt_pk_bf16_f32 v169, v8, v9\n"
    "v_cvt_pk_bf16_f32 v171, v10, v11\n"
    "v_xor_b32 v167, 0x80008000, v171\n"
    "v_xor_b32 v172, v12, v160\n"
    "v_xor_b32 v173, v12, v161\n"
    "v_xor_b32 v174, v12, v162\n"
    "v_xor_b32 v175, v12, v163\n"
    "v_xor_b32 v176, v12, v164\n"
    "v_xor_b32 v177, v12, v165\n"
    "v_xor_b32 v178, v12, v166\n"
    "v_xor_b32 v179, v12, v167\n"
    "v_xor_b32 v180, v12, v168\n"
    "v_xor_b32 v181, v12, v169\n"
    "v_xor_b32 v182, v12, v170\n"
    "v_xor_b32 v183, v12, v171\n"
    ".Lb_nc:\n"
    "s_lshr_b32 s34, s3, 1\n"
    "s_lshl_b32 s34, s34, 1\n"
    "s_mul_i32 s35, s44, 320\n"
    "s_add_u32 s34, s34, s35\n"
    "v_mul_u32_u24 v0, 80, v250\n"
    "v_add_u32 v1, s34, v0\n"
    "v_sub_u32 v2, 640, v1\n"
    "v_min_u32 v1, v1, v2\n"
    "v_mul_u32_u24 v2, 0x282, v251\n"
    "v_add_u32 v2, 114688, v2\n"
    "v_lshl_add_u32 v1, v1, 1, v2\n"
    "s_cmp_eq_u32 s44, 0\n"
    "s_cselect_b32 s35, 2, -2\n"
    "s_mul_i32 s34, s35, 0\n"
    "v_add_u32 v96, s34, v1\n"
    "s_mul_i32 s34, s35, 40\n"
    "v_add_u32 v97, s34, v1\n"
    "s_mul_i32 s34, s35, 4\n"
    "v_add_u32 v98, s34, v1\n"
    "s_mul_i32 s34, s35, 44\n"
    "v_add_u32 v99, s34, v1\n"
    "s_mul_i32 s34, s35, 8\n"
    "v_add_u32 v100, s34, v1\n"
    "s_mul_i32 s34, s35, 48\n"
    "v_add_u32 v101, s34, v1\n"
    "s_mul_i32 s34, s35, 12\n"
    "v_add_u32 v102, s34, v1\n"
    "s_mul_i32 s34, s35, 52\n"
    "v_add_u32 v103, s34, v1\n"
    "s_mul_i32 s34, s35, 16\n"
    "v_add_u32 v104, s34, v1\n"
    "s_mul_i32 s34, s35, 56\n"
    "v_add_u32 v105, s34, v1\n"
    "s_mul_i32 s34, s35, 1\n"
    "v_add_u32 v106, s34, v1\n"
    "s_mul_i32 s34, s35, 41\n"
    "v_add_u32 v107, s34, v1\n"
    "s_mul_i32 s34, s35, 5\n"
    "v_add_u32 v108, s34, v1\n"
    "s_mul_i32 s34, s35, 45\n"
    "v_add_u32 v109, s34, v1\n"
    "s_mul_i32 s34, s35, 9\n"
    "v_add_u32 v110, s34, v1\n"
    "s_mul_i32 s34, s35, 49\n"
    "v_add_u32 v111, s34, v1\n"
    "s_mul_i32 s34, s35, 13\n"
    "v_add_u32 v112, s34, v1\n"
    "s_mul_i32 s34, s35, 53\n"
    "v_add_u32 v113, s34, v1\n"
    "s_mul_i32 s34, s35, 17\n"
    "v_add_u32 v114, s34, v1\n"
    "s_mul_i32 s34, s35, 57\n"
    "v_add_u32 v115, s34, v1\n"
    "s_cmp_ge_u32 s3, 2\n"
    "s_cbranch_scc1 .Lb_s0\n"
    "s_mul_i32 s34, s44, 20\n"
    "v_add_u32 v0, s34, v0\n"
    "v_lshl_add_u32 v97, v0, 1, v2\n"
    "v_add_u32 v97, 80, v97\n"
    "v_cmp_eq_u32 vcc, 0, v0\n"
    "v_mov_b32 v3, 320\n"
    "s_nop 0\n"
    "v_cndmask_b32 v0, v0, v3, vcc\n"
    "v_lshl_add_u32 v96, v0, 1, v2\n"
    ".Lb_s0:\n"
    "s_lshl_b32 s34, s43, 1\n"
    "s_lshr_b32 s35, s40, 3\n"
    "s_add_u32 s34, s34, s35\n"
    "s_lshl_b32 s34, s34, 1\n"
    "s_and_b32 s35, s40, 1\n"
    "s_add_u32 s34, s34, s35\n"
    "s_lshl_b32 s34, s34, 2\n"
    "s_bfe_u32 s35, s40, 0x20001\n"
    "s_add_u32 s34, s34, s35\n"
    "s_lshl_b32 s34, s34, 8\n"
    "s_lshl_b32 s35, s3, 16\n"
    "s_add_u32 s34, s34, s35\n"
    "s_add_u32 s34, s34, 2621440\n"
    "v_lshlrev_b32 v0, 14, v250\n"
    "v_lshl_add_u32 v0, v251, 4, v0\n"
    "s_mov_b32 s46, s34\n"
    "s_nop 0\n"
    "s_call_b64 s[0:1], .Lwarm\n"
    "s_waitcnt vmcnt(2)\n"
    "buffer_load_dword a252, v159, s[8:11], s26 offen\n"
    "s_barrier\n"
    "ds_read_u16 v16, v96 offset:0\n"
    "ds_read_u16 v17, v96 offset:22528\n"
    "ds_read_u16 v18, v96 offset:11264\n"
    "ds_read_u16 v19, v96 offset:33792\n"
    "ds_read_u16 v20, v97 offset:0\n"
    "ds_read_u16 v21, v97 offset:22528\n"
    "ds_read_u16 v22, v97 offset:11264\n"
    "ds_read_u16 v23, v97 offset:33792\n"
    "ds_read_u16 v24, v98 offset:0\n"
    "ds_read_u16 v25, v98 offset:22528\n"
    "ds_read_u16 v26, v98 offset:11264\n"
    "ds_read_u16 v27, v98 offset:33792\n"
    "ds_read_u16 v28, v99 offset:0\n"
    "ds_read_u16 v29, v99 offset:22528\n"
    "ds_read_u16 v30, v99 offset:11264\n"
    "ds_read_u16 v31, v99 offset:33792\n"
    "s_waitcnt lgkmcnt(8)\n"
    "v_lshl_or_b32 v4, v17, 16, v16\n"
    "v_lshl_or_b32 v5, v19, 16, v18\n"
    "v_lshl_or_b32 v6, v21, 16, v20\n"
    "v_lshl_or_b32 v7, v23, 16, v22\n"
    "ds_read_u16 v32, v100 offset:0\n"
    "ds_read_u16 v33, v100 offset:22528\n"
    "ds_read_u16 v34, v100 offset:11264\n"
    "ds_read_u16 v35, v100 offset:33792\n"
    "ds_read_u16 v36, v101 offset:0\n"
    "ds_read_u16 v37, v101 offset:22528\n"
    "ds_read_u16 v38, v101 offset:11264\n"
    "ds_read_u16 v39, v101 offset:33792\n"
    "buffer_store_dwordx4 v[4:7], v0, s[4:7], s46 offen sc1\n"
    "s_waitcnt lgkmcnt(8)\n"
    "v_lshl_or_b32 v8, v25, 16, v24\n"
    "v_lshl_or_b32 v9, v27, 16, v26\n"
    "v_lshl_or_b32 v10, v29, 16, v28\n"
    "v_lshl_or_b32 v11, v31, 16, v30\n"
    "s_add_u32 s46, s46, 262144\n"
    "ds_read_u16 v40, v102 offset:0\n"
    "ds_read_u16 v41, v102 offset:22528\n"
    "ds_read_u16 v42, v102 offset:11264\n"
    "ds_read_u16 v43, v102 offset:33792\n"
    "ds_read_u16 v44, v103 offset:0\n"
    "ds_read_u16 v45, v103 offset:22528\n"
    "ds_read_u16 v46, v103 offset:11264\n"
    "ds_read_u16 v47, v103 offset:33792\n"
    "buffer_store_dwordx4 v[8:11], v0, s[4:7], s46 offen sc1\n"
    "s_waitcnt lgkmcnt(8)\n"
    "v_lshl_or_b32 v12, v33, 16, v32\n"
    "v_lshl_or_b32 v13, v35, 16, v34\n"
    "v_lshl_or_b32 v14, v37, 16, v36\n"
    "v_lshl_or_b32 v15, v39, 16, v38\n"
    "s_add_u32 s46, s46, 262144\n"
    "ds_read_u16 v48, v104 offset:0\n"
    "ds_read_u16 v49, v104 offset:22528\n"
    "ds_read_u16 v50, v104 offset:11264\n"
    "ds_read_u16 v51, v104 offset:33792\n"
    "ds_read_u16 v52, v105 offset:0\n"
    "ds_read_u16 v53, v105 offset:22528\n"
    "ds_read_u16 v54, v105 offset:11264\n"
    "ds_read_u16 v55, v105 offset:33792\n"
    "buffer_store_dwordx4 v[12:15], v0, s[4:7], s46 offen sc1\n"
    "s_waitcnt lgkmcnt(8)\n"
    "v_lshl_or_b32 v4, v41, 16, v40\n"
    "v_lshl_or_b32 v5, v43, 16, v42\n"
    "v_lshl_or_b32 v6, v45, 16, v44\n"
    "v_lshl_or_b32 v7, v47, 16, v46\n"
    "s_add_u32 s46, s46, 262144\n"
    "ds_read_u16 v56, v106 offset:0\n"
    "ds_read_u16 v57, v106 offset:22528\n"
    "ds_read_u16 v58, v106 offset:11264\n"
    "ds_read_u16 v59, v106 offset:33792\n"
    "ds_read_u16 v60, v107 offset:0\n"
    "ds_read_u16 v61, v107 offset:22528\n"
    "ds_read_u16 v62, v107 offset:11264\n"
    "ds_read_u16 v63, v107 offset:33792\n"
    "buffer_store_dwordx4 v[4:7], v0, s[4:7], s46 offen sc1\n"
    "s_waitcnt lgkmcnt(8)\n"
    "v_lshl_or_b32 v8, v49, 16, v48\n"
    "v_lshl_or_b32 v9, v51, 16, v50\n"
    "v_lshl_or_b32 v10, v53, 16, v52\n"
    "v_lshl_or_b32 v11, v55, 16, v54\n"
    "s_add_u32 s46, s46, 262144\n"
    "ds_read_u16 v64, v108 offset:0\n"
    "ds_read_u16 v65, v108 offset:22528\n"
    "ds_read_u16 v66, v108 offset:11264\n"
    "ds_read_u16 v67, v108 offset:33792\n"
    "ds_read_u16 v68, v109 offset:0\n"
    "ds_read_u16 v69, v109 offset:22528\n"
    "ds_read_u16 v70, v109 offset:11264\n"
    "ds_read_u16 v71, v109 offset:33792\n"
    "buffer_store_dwordx4 v[8:11], v0, s[4:7], s46 offen sc1\n"
    "s_waitcnt lgkmcnt(8)\n"
    "v_lshl_or_b32 v12, v57, 16, v56\n"
    "v_lshl_or_b32 v13, v59, 16, v58\n"
    "v_lshl_or_b32 v14, v61, 16, v60\n"
    "v_lshl_or_b32 v15, v63, 16, v62\n"
    "s_add_u32 s46, s46, 262144\n"
    "ds_read_u16 v72, v110 offset:0\n"
    "ds_read_u16 v73, v110 offset:22528\n"
    "ds_read_u16 v74, v110 offset:11264\n"
    "ds_read_u16 v75, v110 offset:33792\n"
    "ds_read_u16 v76, v111 offset:0\n"
    "ds_read_u16 v77, v111 offset:22528\n"
    "ds_read_u16 v78, v111 offset:11264\n"
    "ds_read_u16 v79, v111 offset:33792\n"
    "buffer_store_dwordx4 v[12:15], v0, s[4:7], s46 offen sc1\n"
    "s_waitcnt lgkmcnt(8)\n"
    "v_lshl_or_b32 v4, v65, 16, v64\n"
    "v_lshl_or_b32 v5, v67, 16, v66\n"
    "v_lshl_or_b32 v6, v69, 16, v68\n"
    "v_lshl_or_b32 v7, v71, 16, v70\n"
    "s_add_u32 s46, s46, 262144\n"
    "ds_read_u16 v80, v112 offset:0\n"
    "ds_read_u16 v81, v112 offset:22528\n"
    "ds_read_u16 v82, v112 offset:11264\n"
    "ds_read_u16 v83, v112 offset:33792\n"
    "ds_read_u16 v84, v113 offset:0\n"
    "ds_read_u16 v85, v113 offset:22528\n"
    "ds_read_u16 v86, v113 offset:11264\n"
    "ds_read_u16 v87, v113 offset:33792\n"
    "buffer_store_dwordx4 v[4:7], v0, s[4:7], s46 offen sc1\n"
    "s_waitcnt lgkmcnt(8)\n"
    "v_lshl_or_b32 v8, v73, 16, v72\n"
    "v_lshl_or_b32 v9, v75, 16, v74\n"
    "v_lshl_or_b32 v10, v77, 16, v76\n"
    "v_lshl_or_b32 v11, v79, 16, v78\n"
    "s_add_u32 s46, s46, 262144\n"
    "ds_read_u16 v88, v114 offset:0\n"
    "ds_read_u16 v89, v114 offset:22528\n"
    "ds_read_u16 v90, v114 offset:11264\n"
    "ds_read_u16 v91, v114 offset:33792\n"
    "ds_read_u16 v92, v115 offset:0\n"
    "ds_read_u16 v93, v115 offset:22528\n"
    "ds_read_u16 v94, v115 offset:11264\n"
    "ds_read_u16 v95, v115 offset:33792\n"
    "buffer_store_dwordx4 v[8:11], v0, s[4:7], s46 offen sc1\n"
    "s_waitcnt lgkmcnt(8)\n"
    "v_lshl_or_b32 v12, v81, 16, v80\n"
    "v_lshl_or_b32 v13, v83, 16, v82\n"
    "v_lshl_or_b32 v14, v85, 16, v84\n"
    "v_lshl_or_b32 v15, v87, 16, v86\n"
    "s_add_u32 s46, s46, 262144\n"
    "buffer_store_dwordx4 v[12:15], v0, s[4:7], s46 offen sc1\n"
    "s_waitcnt lgkmcnt(0)\n"
    "v_lshl_or_b32 v4, v89, 16, v88\n"
    "v_lshl_or_b32 v5, v91, 16, v90\n"
    "v_lshl_or_b32 v6, v93, 16, v92\n"
    "v_lshl_or_b32 v7, v95, 16, v94\n"
    "s_add_u32 s46, s46, 262144\n"
    "buffer_store_dwordx4 v[4:7], v0, s[4:7], s46 offen sc1\n"
    "s_waitcnt vmcnt(12)\n"
    "v_add_u32 v253, 0x9e3779b9, v253\n"
    "s_add_u32 m0, s53, 0\n"
    "s_add_u32 s54, s52, 0\n"
    "buffer_load_dwordx4 v249, s[8:11], s54 offen lds\n"
    "s_add_u32 m0, s53, 1024\n"
    "s_add_u32 s54, s52, 10240\n"
    "buffer_load_dwordx4 v249, s[8:11], s54 offen lds\n"
    "s_add_u32 m0, s53, 2048\n"
    "s_add_u32 s54, s52, 20480\n"
    "buffer_load_dwordx4 v249, s[8:11], s54 offen lds\n"
    "s_add_u32 m0, s53, 3072\n"
    ".Lpad11:\n"
    "s_cbranch_execz .Lpad12\n"
    "s_add_u32 s54, s52, 30720\n"
    "buffer_load_dwordx4 v249, s[8:11], s54 offen lds\n"
    "s_waitcnt vmcnt(4)\n"
    "s_barrier\n"
    "buffer_store_dword v253, v252, s[4:7], s55 offen sc1\n"
    "s_nop 0\n"
    "s_nop 0\n"
    "s_branch .Larr4\n"
    ".Lrole_c:\n"
    "s_mov_b32 s6, 21544960\n"
    "s_mov_b32 s7, 0x20000\n"
    "s_mov_b32 s34, 20201472\n"
    "v_mov_b32 v5, 0\n"
    "buffer_load_dword v253, v5, s[4:7], s34 offen sc1\n"
    "s_lshl_b32 s34, s2, 2\n"
    "s_add_u32 s34, s34, s3\n"
    "s_cmp_lt_u32 s34, 1024\n"
    "s_cselect_b32 s35, 2, 0\n"
    "s_lshl_b32 s34, s34, 6\n"
    "s_add_u32 s34, s34, 20267040\n"
    "v_cmp_gt_u32 vcc, s35, v255\n"
    "v_mul_u32_u24 v6, 65536, v255\n"
    "v_mov_b32 v7, 0x7fffffff\n"
    "v_cndmask_b32 v6, v7, v6, vcc\n"
    "buffer_store_dword v5, v6, s[4:7], s34 offen sc1\n"
    "s_mov_b32 s10, 2211840\n"
    "s_mov_b32 s11, 0x20000\n"
    "s_mov_b32 s22, 2621440\n"
    "s_mov_b32 s23, 0x20000\n"
    "s_lshr_b32 s34, s28, 1\n"
    "s_lshl_b32 s31, s34, 2\n"
    "s_lshl_b32 s33, s34, 3\n"
    "s_and_b32 s34, s28, 1\n"
    "s_lshl_b32 s32, s34, 3\n"
    "s_lshl_b32 s30, s34, 2\n"
    "s_and_b32 s34, s29, 3\n"
    "s_add_u32 s31, s31, s34\n"
    "s_add_u32 s30, s30, s34\n"
    "s_lshr_b32 s50, s29, 2\n"
    "s_add_u32 s32, s32, s50\n"
    "s_add_u32 s33, s33, s50\n"
    "s_add_u32 s50, s50, 7\n"
    "s_cmp_ge_u32 s50, 8\n"
    "s_cselect_b32 s34, 8, 0\n"
    "s_sub_u32 s50, s50, s34\n"
    "s_nop 0\n"
    "s_nop 0\n"
    "s_nop 0\n"
    "s_nop 0\n"
    "v_lshrrev_b32 v246, 3, v255\n"
    "v_and_b32 v247, 6, v246\n"
    "v_xor_b32 v247, v247, v255\n"
    "v_and_b32 v247, 7, v247\n"
    "v_mul_u32_u24 v246, 0x500, v246\n"
    "v_lshl_add_u32 v249, v247, 4, v246\n"
    "v_mul_u32_u24 v248, 128, v255\n"
    "s_mul_i32 s52, s3, 40960\n"
    "s_lshl_b32 s54, s30, 7\n"
    "s_add_u32 s52, s52, s54\n"
    "s_mul_i32 s53, s3, 4096\n"
    "s_and_b32 s46, s3, 1\n"
    "s_lshr_b32 s47, s3, 1\n"
    "s_mul_i32 s34, s46, 5\n"
    "v_bfe_u32 v0, v255, 3, 2\n"
    "v_and_b32 v1, 3, v255\n"
    "v_lshl_add_u32 v0, v0, 2, v1\n"
    "v_add_u32 v1, s34, v0\n"
    "v_subrev_u32 v2, s34, v0\n"
    "v_cmp_gt_u32 vcc, 5, v0\n"
    "s_nop 1\n"
    "v_cndmask_b32 v0, v2, v1, vcc\n"
    "v_lshl_add_u32 v0, v0, 1, s47\n"
    "v_bfe_u32 v1, v255, 2, 1\n"
    "v_cvt_f32_u32 v2, v1\n"
    "v_mul_f32 v2, 0x3e800000, v2\n"
    "v_lshrrev_b32 v3, 5, v255\n"
    "v_cvt_f32_u32 v7, v3\n"
    "v_mul_f32 v4, 4.0, v7\n"
    "v_cvt_f32_u32 v5, v0\n"
    "v_mul_f32 v5, 0x3ccccccd, v5\n"
    "v_add_f32 v6, v2, v2\n"
    "v_cmp_eq_u32 vcc, 0, v0\n"
    "s_nop 1\n"
    "v_cndmask_b32 v5, v5, v6, vcc\n"
    "v_cndmask_b32_e64 v2, v2, 0, vcc\n"
    "v_fma_f32 v6, v5, v4, v2\n"
    "v_mul_f32 v142, 0x41000000, v5\n"
    "v_fmamk_f32 v123, v5, 0x3f800000, v6\n"
    "v_fmamk_f32 v124, v5, 0x40000000, v6\n"
    "v_fmamk_f32 v125, v5, 0x40400000, v6\n"
    "v_fmamk_f32 v126, v5, 0x41000000, v6\n"
    "v_fmamk_f32 v127, v5, 0x41100000, v6\n"
    "v_fmamk_f32 v128, v5, 0x41200000, v6\n"
    "v_fmamk_f32 v129, v5, 0x41300000, v6\n"
    "v_cos_f32 v142, v142\n"
    "v_cos_f32 v122, v6\n"
    "v_cos_f32 v123, v123\n"
    "v_cos_f32 v124, v124\n"
    "v_cos_f32 v125, v125\n"
    "v_cos_f32 v126, v126\n"
    "v_cos_f32 v127, v127\n"
    "v_cos_f32 v128, v128\n"
    "v_cos_f32 v129, v129\n"
    "v_add_f32 v142, v142, v142\n"
    "v_pk_fma_f32 v[130:131], v[126:127], v[142:143], v[122:123] op_sel_hi:[1,0,1] neg_lo:[0,0,1] neg_hi:[0,0,1]\n"
    "v_pk_fma_f32 v[132:133], v[128:129], v[142:143], v[124:125] op_sel_hi:[1,0,1] neg_lo:[0,0,1] neg_hi:[0,0,1]\n"
    "v_pk_fma_f32 v[134:135], v[130:131], v[142:143], v[126:127] op_sel_hi:[1,0,1] neg_lo:[0,0,1] neg_hi:[0,0,1]\n"
    "v_pk_fma_f32 v[136:137], v[132:133], v[142:143], v[128:129] op_sel_hi:[1,0,1] neg_lo:[0,0,1] neg_hi:[0,0,1]\n"
    "v_pk_fma_f32 v[138:139], v[134:135], v[142:143], v[130:131] op_sel_hi:[1,0,1] neg_lo:[0,0,1] neg_hi:[0,0,1]\n"
    "v_pk_fma_f32 v[140:141], v[136:137], v[142:143], v[132:133] op_sel_hi:[1,0,1] neg_lo:[0,0,1] neg_hi:[0,0,1]\n"
    "v_cvt_pk_bf16_f32 v72, v122, v123\n"
    "v_cvt_pk_bf16_f32 v73, v124, v125\n"
    "v_cvt_pk_bf16_f32 v74, v126, v127\n"
    "v_cvt_pk_bf16_f32 v75, v128, v129\n"
    "v_cvt_pk_bf16_f32 v76, v130, v131\n"
    "v_cvt_pk_bf16_f32 v77, v132, v133\n"
    "v_cvt_pk_bf16_f32 v78, v134, v135\n"
    "v_cvt_pk_bf16_f32 v79, v136, v137\n"
    "v_cvt_pk_bf16_f32 v80, v138, v139\n"
    "v_cvt_pk_bf16_f32 v81, v140, v141\n"
    "v_bfe_u32 v0, v255, 1, 1\n"
    "v_bfe_u32 v1, v255, 3, 1\n"
    "v_lshl_add_u32 v0, v1, 1, v0\n"
    "v_bfe_u32 v1, v255, 2, 1\n"
    "v_lshl_add_u32 v0, v1, 2, v0\n"
    "v_bfe_u32 v8, v255, 4, 1\n"
    "v_lshl_add_u32 v0, v8, 3, v0\n"
    "v_mul_u32_u24 v1, 40, v0\n"
    "s_add_u32 m0, s45, 98304\n"
    "s_add_u32 s56, s44, 884736\n"
    "buffer_load_dwordx4 v11, s[16:19], s56 offen  lds\n"
    "v_mov_b32 v2, 0x3acccccd\n"
    "v_cmp_eq_u32 vcc, 0, v8\n"
    "s_nop 1\n"
    "v_cndmask_b32_e64 v2, -v2, v2, vcc\n"
    "s_mul_i32 s34, s46, 10\n"
    "s_add_u32 s34, s34, s47\n"
    "v_add_u32 v4, s34, v1\n"
    "v_cvt_f32_u32 v4, v4\n"
    "v_mul_f32 v4, v2, v4\n"
    "v_and_b32 v5, 1, v255\n"
    "v_cvt_f32_u32 v5, v5\n"
    "v_mul_f32 v5, 0x3e800000, v5\n"
    "v_mul_f32 v6, 0xc3200000, v7\n"
    "v_fma_f32 v6, v6, v2, v5\n"
    "v_add_f32 v2, v2, v2\n"
    "s_lshl_b32 s34, s46, 3\n"
    "v_cvt_f32_u32 v7, s34\n"
    "v_sub_f32 v9, 0x41000000, v7\n"
    "v_subrev_u32 v236, 300, v1\n"
    "v_cndmask_b32 v236, v236, v1, vcc\n"
    "v_cvt_f32_u32 v236, v236\n"
    "v_mul_f32 v236, 0x3acccccd, v236\n"
    "s_cmp_eq_u32 s3, 0\n"
    "s_cselect_b64 s[48:49], -1, 0\n"
    "v_cndmask_b32_e64 v236, v4, v236, s[48:49]\n"
    "v_cndmask_b32_e64 v237, v6, v5, s[48:49]\n"
    "v_fma_f32 v238, v236, v7, v237\n"
    "v_fma_f32 v239, v236, v9, v237\n"
    "v_cos_f32 v138, v236\n"
    "v_add_f32 v124, v238, v236\n"
    "v_add_f32 v125, v239, v236\n"
    "v_cos_f32 v125, v125\n"
    "v_cos_f32 v124, v124\n"
    "v_cos_f32 v122, v238\n"
    "v_cos_f32 v123, v239\n"
    "s_add_u32 m0, s45, 102400\n"
    "s_add_u32 s56, s44, 1105920\n"
    "buffer_load_dwordx4 v11, s[16:19], s56 offen  lds\n"
    "v_add_f32 v138, v138, v138\n"
    "v_pk_fma_f32 v[126:127], v[124:125], v[138:139], v[122:123] op_sel_hi:[1,0,1] neg_lo:[0,0,1] neg_hi:[0,0,1]\n"
    "v_cvt_pk_bf16_f32 v82, v122, v124\n"
    "v_pk_fma_f32 v[128:129], v[126:127], v[138:139], v[124:125] op_sel_hi:[1,0,1] neg_lo:[0,0,1] neg_hi:[0,0,1]\n"
    "v_cvt_pk_bf16_f32 v86, v123, v125\n"
    "v_pk_fma_f32 v[130:131], v[128:129], v[138:139], v[126:127] op_sel_hi:[1,0,1] neg_lo:[0,0,1] neg_hi:[0,0,1]\n"
    "v_cvt_pk_bf16_f32 v83, v126, v128\n"
    "v_pk_fma_f32 v[132:133], v[130:131], v[138:139], v[128:129] op_sel_hi:[1,0,1] neg_lo:[0,0,1] neg_hi:[0,0,1]\n"
    "v_cvt_pk_bf16_f32 v87, v127, v129\n"
    "v_pk_fma_f32 v[134:135], v[132:133], v[138:139], v[130:131] op_sel_hi:[1,0,1] neg_lo:[0,0,1] neg_hi:[0,0,1]\n"
    "v_cvt_pk_bf16_f32 v84, v130, v132\n"
    "v_pk_fma_f32 v[136:137], v[134:135], v[138:139], v[132:133] op_sel_hi:[1,0,1] neg_lo:[0,0,1] neg_hi:[0,0,1]\n"
    "v_cvt_pk_bf16_f32 v88, v131, v133\n"
    "v_cvt_pk_bf16_f32 v85, v134, v136\n"
    "v_cvt_pk_bf16_f32 v89, v135, v137\n"
    "v_cmp_ne_u32 vcc, v8, v3\n"
    "v_cmp_eq_u32 s[58:59], 1, v255\n"
    "s_nop 0\n"
    "s_or_b64 vcc, vcc, s[58:59]\n"
    "s_and_b64 vcc, vcc, s[48:49]\n"
    "v_cndmask_b32_e64 v82, v82, 0, vcc\n"
    "v_cndmask_b32_e64 v83, v83, 0, vcc\n"
    "v_cndmask_b32_e64 v84, v84, 0, vcc\n"
    "v_cndmask_b32_e64 v85, v85, 0, vcc\n"
    "v_cndmask_b32_e64 v86, v86, 0, vcc\n"
    "v_cndmask_b32_e64 v87, v87, 0, vcc\n"
    "v_cndmask_b32_e64 v88, v88, 0, vcc\n"
    "v_cndmask_b32_e64 v89, v89, 0, vcc\n"
    "v_cmp_eq_u32 vcc, 0, v255\n"
    "s_nop 0\n"
    "s_and_b64 vcc, vcc, s[48:49]\n"
    "v_mov_b32 v238, 0xbf353f35\n"
    "v_cndmask_b32 v82, v82, v238, vcc\n"
    "v_cndmask_b32 v83, v83, v238, vcc\n"
    "v_cndmask_b32 v84, v84, v238, vcc\n"
    "v_cndmask_b32 v85, v85, v238, vcc\n"
    "v_cndmask_b32 v86, v86, v238, vcc\n"
    "v_cndmask_b32 v87, v87, v238, vcc\n"
    "s_add_u32 m0, s45, 106496\n"
    "s_add_u32 s56, s44, 1327104\n"
    "buffer_load_dwordx4 v11, s[16:19], s56 offen  lds\n"
    "v_cndmask_b32 v88, v88, v238, vcc\n"
    "v_cndmask_b32 v89, v89, v238, vcc\n"
    "v_add_f32 v4, v4, v2\n"
    "v_fma_f32 v238, v4, v7, v6\n"
    "v_fma_f32 v239, v4, v9, v6\n"
    "v_cos_f32 v138, v4\n"
    "v_add_f32 v124, v238, v4\n"
    "v_add_f32 v125, v239, v4\n"
    "v_cos_f32 v125, v125\n"
    "v_cos_f32 v124, v124\n"
    "v_cos_f32 v122, v238\n"
    "v_cos_f32 v123, v239\n"
    "v_add_f32 v138, v138, v138\n"
    "v_pk_fma_f32 v[126:127], v[124:125], v[138:139], v[122:123] op_sel_hi:[1,0,1] neg_lo:[0,0,1] neg_hi:[0,0,1]\n"
    "v_cvt_pk_bf16_f32 v90, v122, v124\n"
    "v_pk_fma_f32 v[128:129], v[126:127], v[138:139], v[124:125] op_sel_hi:[1,0,1] neg_lo:[0,0,1] neg_hi:[0,0,1]\n"
    "v_cvt_pk_bf16_f32 v94, v123, v125\n"
    "v_pk_fma_f32 v[130:131], v[128:129], v[138:139], v[126:127] op_sel_hi:[1,0,1] neg_lo:[0,0,1] neg_hi:[0,0,1]\n"
    "v_cvt_pk_bf16_f32 v91, v126, v128\n"
    "v_pk_fma_f32 v[132:133], v[130:131], v[138:139], v[128:129] op_sel_hi:[1,0,1] neg_lo:[0,0,1] neg_hi:[0,0,1]\n"
    "v_cvt_pk_bf16_f32 v95, v127, v129\n"
    "v_pk_fma_f32 v[134:135], v[132:133], v[138:139], v[130:131] op_sel_hi:[1,0,1] neg_lo:[0,0,1] neg_hi:[0,0,1]\n"
    "v_cvt_pk_bf16_f32 v92, v130, v132\n"
    "v_pk_fma_f32 v[136:137], v[134:135], v[138:139], v[132:133] op_sel_hi:[1,0,1] neg_lo:[0,0,1] neg_hi:[0,0,1]\n"
    "v_cvt_pk_bf16_f32 v96, v131, v133\n"
    "v_cvt_pk_bf16_f32 v93, v134, v136\n"
    "v_cvt_pk_bf16_f32 v97, v135, v137\n"
    "v_add_f32 v4, v4, v2\n"
    "v_fma_f32 v238, v4, v7, v6\n"
    "v_fma_f32 v239, v4, v9, v6\n"
    "v_cos_f32 v138, v4\n"
    "v_add_f32 v124, v238, v4\n"
    "s_add_u32 m0, s45, 110592\n"
    "s_add_u32 s56, s44, 1548288\n"
    "buffer_load_dwordx4 v11, s[16:19], s56 offen  lds\n"
    "v_add_f32 v125, v239, v4\n"
    "v_cos_f32 v125, v125\n"
    "v_cos_f32 v124, v124\n"
    "v_cos_f32 v122, v238\n"
    "v_cos_f32 v123, v239\n"
    "v_add_f32 v138, v138, v138\n"
    "v_pk_fma_f32 v[126:127], v[124:125], v[138:139], v[122:123] op_sel_hi:[1,0,1] neg_lo:[0,0,1] neg_hi:[0,0,1]\n"
    "v_cvt_pk_bf16_f32 v98, v122, v124\n"
    "v_pk_fma_f32 v[128:129], v[126:127], v[138:139], v[124:125] op_sel_hi:[1,0,1] neg_lo:[0,0,1] neg_hi:[0,0,1]\n"
    "v_cvt_pk_bf16_f32 v102, v123, v125\n"
    "v_pk_fma_f32 v[130:131], v[128:129], v[138:139], v[126:127] op_sel_hi:[1,0,1] neg_lo:[0,0,1] neg_hi:[0,0,1]\n"
    "v_cvt_pk_bf16_f32 v99, v126, v128\n"
    "v_pk_fma_f32 v[132:133], v[130:131], v[138:139], v[128:129] op_sel_hi:[1,0,1] neg_lo:[0,0,1] neg_hi:[0,0,1]\n"
    "v_cvt_pk_bf16_f32 v103, v127, v129\n"
    "v_pk_fma_f32 v[134:135], v[132:133], v[138:139], v[130:131] op_sel_hi:[1,0,1] neg_lo:[0,0,1] neg_hi:[0,0,1]\n"
    "v_cvt_pk_bf16_f32 v100, v130, v132\n"
    "v_pk_fma_f32 v[136:137], v[134:135], v[138:139], v[132:133] op_sel_hi:[1,0,1] neg_lo:[0,0,1] neg_hi:[0,0,1]\n"
    "v_cvt_pk_bf16_f32 v104, v131, v133\n"
    "v_cvt_pk_bf16_f32 v101, v134, v136\n"
    "v_cvt_pk_bf16_f32 v105, v135, v137\n"
    "v_add_f32 v4, v4, v2\n"
    "v_fma_f32 v238, v4, v7, v6\n"
    "v_fma_f32 v239, v4, v9, v6\n"
    "v_cos_f32 v138, v4\n"
    "v_add_f32 v124, v238, v4\n"
    "v_add_f32 v125, v239, v4\n"
    "v_cos_f32 v125, v125\n"
    "v_cos_f32 v124, v124\n"
    "v_cos_f32 v122, v238\n"
    "s_add_u32 m0, s45, 114688\n"
    "s_add_u32 s56, s44, 1769472\n"
    "buffer_load_dwordx4 v11, s[16:19], s56 offen  lds\n"
    "v_cos_f32 v123, v239\n"
    "v_add_f32 v138, v138, v138\n"
    "v_pk_fma_f32 v[126:127], v[124:125], v[138:139], v[122:123] op_sel_hi:[1,0,1] neg_lo:[0,0,1] neg_hi:[0,0,1]\n"
    "v_cvt_pk_bf16_f32 v106, v122, v124\n"
    "v_pk_fma_f32 v[128:129], v[126:127], v[138:139], v[124:125] op_sel_hi:[1,0,1] neg_lo:[0,0,1] neg_hi:[0,0,1]\n"
    "v_cvt_pk_bf16_f32 v110, v123, v125\n"
    "v_pk_fma_f32 v[130:131], v[128:129], v[138:139], v[126:127] op_sel_hi:[1,0,1] neg_lo:[0,0,1] neg_hi:[0,0,1]\n"
    "v_cvt_pk_bf16_f32 v107, v126, v128\n"
    "v_pk_fma_f32 v[132:133], v[130:131], v[138:139], v[128:129] op_sel_hi:[1,0,1] neg_lo:[0,0,1] neg_hi:[0,0,1]\n"
    "v_cvt_pk_bf16_f32 v111, v127, v129\n"
    "v_pk_fma_f32 v[134:135], v[132:133], v[138:139], v[130:131] op_sel_hi:[1,0,1] neg_lo:[0,0,1] neg_hi:[0,0,1]\n"
    "v_cvt_pk_bf16_f32 v108, v130, v132\n"
    "v_pk_fma_f32 v[136:137], v[134:135], v[138:139], v[132:133] op_sel_hi:[1,0,1] neg_lo:[0,0,1] neg_hi:[0,0,1]\n"
    "v_cvt_pk_bf16_f32 v112, v131, v133\n"
    "v_cvt_pk_bf16_f32 v109, v134, v136\n"
    "v_cvt_pk_bf16_f32 v113, v135, v137\n"
    "v_add_f32 v4, v4, v2\n"
    "v_fma_f32 v238, v4, v7, v6\n"
    "v_fma_f32 v239, v4, v9, v6\n"
    "v_cos_f32 v138, v4\n"
    "v_add_f32 v124, v238, v4\n"
    "v_add_f32 v125, v239, v4\n"
    "v_cos_f32 v125, v125\n"
    "v_cos_f32 v124, v124\n"
    "v_cos_f32 v122, v238\n"
    "v_cos_f32 v123, v239\n"
    "v_add_f32 v138, v138, v138\n"
    "v_pk_fma_f32 v[126:127], v[124:125], v[138:139], v[122:123] op_sel_hi:[1,0,1] neg_lo:[0,0,1] neg_hi:[0,0,1]\n"
    "v_cvt_pk_bf16_f32 v114, v122, v124\n"
    "v_pk_fma_f32 v[128:129], v[126:127], v[138:139], v[124:125] op_sel_hi:[1,0,1] neg_lo:[0,0,1] neg_hi:[0,0,1]\n"
    "v_cvt_pk_bf16_f32 v118, v123, v125\n"
    "v_pk_fma_f32 v[130:131], v[128:129], v[138:139], v[126:127] op_sel_hi:[1,0,1] neg_lo:[0,0,1] neg_hi:[0,0,1]\n"
    "s_add_u32 m0, s45, 118784\n"
    "s_add_u32 s56, s44, 1990656\n"
    "buffer_load_dwordx4 v11, s[16:19], s56 offen  lds\n"
    "v_cvt_pk_bf16_f32 v115, v126, v128\n"
    "v_pk_fma_f32 v[132:133], v[130:131], v[138:139], v[128:129] op_sel_hi:[1,0,1] neg_lo:[0,0,1] neg_hi:[0,0,1]\n"
    "v_cvt_pk_bf16_f32 v119, v127, v129\n"
    "v_pk_fma_f32 v[134:135], v[132:133], v[138:139], v[130:131] op_sel_hi:[1,0,1] neg_lo:[0,0,1] neg_hi:[0,0,1]\n"
    "v_cvt_pk_bf16_f32 v116, v130, v132\n"
    "v_pk_fma_f32 v[136:137], v[134:135], v[138:139], v[132:133] op_sel_hi:[1,0,1] neg_lo:[0,0,1] neg_hi:[0,0,1]\n"
    "v_cvt_pk_bf16_f32 v120, v131, v133\n"
    "v_cvt_pk_bf16_f32 v117, v134, v136\n"
    "v_cvt_pk_bf16_f32 v121, v135, v137\n"
    "v_lshrrev_b32 v244, 2, v251\n"
    "v_lshl_add_u32 v244, v250, 2, v244\n"
    "v_bfe_u32 v245, v255, 1, 1\n"
    "v_and_b32 v246, 6, v244\n"
    "v_or_b32 v245, v245, v246\n"
    "v_and_b32 v246, 1, v255\n"
    "v_lshlrev_b32 v246, 3, v246\n"
    "v_lshl_add_u32 v246, v244, 7, v246\n"
    "v_add_u32 v246, s53, v246\n"
    "v_xor_b32 v247, 0, v245\n"
    "v_lshl_add_u32 v240, v247, 4, v246\n"
    "v_xor_b32 v247, 2, v245\n"
    "v_lshl_add_u32 v241, v247, 4, v246\n"
    "v_xor_b32 v247, 4, v245\n"
    "v_lshl_add_u32 v242, v247, 4, v246\n"
    "v_xor_b32 v247, 6, v245\n"
    "v_lshl_add_u32 v243, v247, 4, v246\n"
    "s_lshr_b32 s55, s2, 2\n"
    "s_mul_i32 s55, s55, 128\n"
    "s_and_b32 s35, s2, 3\n"
    "s_lshl_b32 s35, s35, 2\n"
    "s_add_u32 s55, s55, s35\n"
    "s_mul_i32 s35, s3, 16384\n"
    "s_add_u32 s55, s55, s35\n"
    "s_add_u32 s55, s55, 20267008\n"
    "s_cmp_lt_u32 s3, 8\n"
    "s_cselect_b32 s35, 2, 0\n"
    "v_mul_u32_u24 v246, 8192, v255\n"
    "v_mov_b32 v247, 0x7fffffff\n"
    "v_cmp_gt_u32 vcc, s35, v255\n"
    "s_nop 1\n"
    "v_cndmask_b32 v252, v247, v246, vcc\n"
    "v_bfe_u32 v4, v255, 2, 2\n"
    "v_lshrrev_b32 v5, 5, v255\n"
    "v_lshl_add_u32 v5, v5, 2, v4\n"
    "v_and_b32 v6, 1, v250\n"
    "v_and_b32 v7, 3, v255\n"
    "v_lshlrev_b32 v7, 3, v7\n"
    "v_lshl_add_u32 v7, v6, 5, v7\n"
    "v_lshl_add_u32 v7, v5, 10, v7\n"
    "s_lshl_b32 s34, s46, 9\n"
    "s_add_u32 s34, s34, 81920\n"
    "v_add_u32 v7, s34, v7\n"
    "v_xor_b32 v8, 0, v4\n"
    "v_lshl_add_u32 v0, v8, 6, v7\n"
    "v_xor_b32 v8, 1, v4\n"
    "v_lshl_add_u32 v1, v8, 6, v7\n"
    "v_xor_b32 v8, 2, v4\n"
    "v_lshl_add_u32 v2, v8, 6, v7\n"
    "v_xor_b32 v8, 3, v4\n"
    "v_lshl_add_u32 v3, v8, 6, v7\n"
    "s_mul_i32 s34, s3, 5120\n"
    "s_add_u32 s34, s34, 122880\n"
    "v_add_u32 v4, s34, v254\n"
    "s_xor_b32 s35, s3, 1\n"
    "s_mul_i32 s34, s35, 5120\n"
    "s_add_u32 s34, s34, 122880\n"
    "v_add_u32 v5, s34, v254\n"
    "v_lshrrev_b32 v7, 5, v255\n"
    "v_lshlrev_b32 v7, 8, v7\n"
    "v_lshl_add_u32 v7, v251, 4, v7\n"
    "v_lshl_add_u32 v6, v6, 10, v7\n"
    "s_mul_i32 s34, s43, 40960\n"
    "s_mul_i32 s35, s46, 10240\n"
    "s_add_u32 s34, s34, s35\n"
    "s_mul_i32 s35, s47, 20480\n"
    "s_add_u32 s34, s34, s35\n"
    "s_add_u32 s34, s34, 5242880\n"
    "s_call_b64 s[0:1], .Lwarm\n"
    "s_waitcnt vmcnt(0)\n"
    "s_barrier\n"
    "ds_read_b64_tr_b16 a[160:161], v0 offset:0\n"
    "ds_read_b64_tr_b16 a[162:163], v0 offset:8192\n"
    "ds_read_b64_tr_b16 a[164:165], v0 offset:16384\n"
    "ds_read_b64_tr_b16 a[166:167], v0 offset:24576\n"
    "ds_read_b64_tr_b16 a[168:169], v0 offset:32768\n"
    "ds_read_b64_tr_b16 a[170:171], v1 offset:0\n"
    "ds_read_b64_tr_b16 a[172:173], v1 offset:8192\n"
    "ds_read_b64_tr_b16 a[174:175], v1 offset:16384\n"
    "ds_read_b64_tr_b16 a[176:177], v1 offset:24576\n"
    "ds_read_b64_tr_b16 a[178:179], v1 offset:32768\n"
    "ds_read_b64_tr_b16 a[180:181], v2 offset:0\n"
    "ds_read_b64_tr_b16 a[182:183], v2 offset:8192\n"
    "ds_read_b64_tr_b16 a[184:185], v2 offset:16384\n"
    "ds_read_b64_tr_b16 a[186:187], v2 offset:24576\n"
    "ds_read_b64_tr_b16 a[188:189], v2 offset:32768\n"
    "s_waitcnt lgkmcnt(13)\n"
    "v_mfma_f32_32x32x16_bf16 v[122:137], v[72:75], a[160:163], 0\n"
    "s_add_u32 m0, s53, 0\n"
    "s_add_u32 s54, s52, 0\n"
    "buffer_load_dwordx4 v249, s[8:11], s54 offen lds\n"
    "ds_read_b64_tr_b16 a[190:191], v3 offset:0\n"
    "ds_read_b64_tr_b16 a[192:193], v3 offset:8192\n"
    "ds_read_b64_tr_b16 a[194:195], v3 offset:16384\n"
    "s_waitcnt lgkmcnt(14)\n"
    "v_mfma_f32_32x32x16_bf16 v[122:137], v[76:79], a[164:167], v[122:137]\n"
    "s_waitcnt lgkmcnt(13)\n"
    "ds_read_b64_tr_b16 a[196:197], v3 offset:24576\n"
    "ds_read_b64_tr_b16 a[198:199], v3 offset:32768\n"
    "ds_read_b64_tr_b16 a[200:201], v0 offset:256\n"
    "v_mfma_f32_32x32x8_bf16 v[122:137], v[80:81], a[168:169], v[122:137]\n"
    "s_add_u32 m0, s53, 1024\n"
    "s_add_u32 s54, s52, 10240\n"
    "buffer_load_dwordx4 v249, s[8:11], s54 offen lds\n"
    "ds_read_b64_tr_b16 a[202:203], v0 offset:8448\n"
    "ds_read_b64_tr_b16 a[204:205], v0 offset:16640\n"
    "ds_read_b64_tr_b16 a[206:207], v0 offset:24832\n"
    "s_waitcnt lgkmcnt(14)\n"
    "v_mfma_f32_32x32x16_bf16 v[138:153], v[72:75], a[170:173], 0\n"
    "s_add_u32 s54, s52, 20480\n"
    "ds_read_b64_tr_b16 a[208:209], v0 offset:33024\n"
    "ds_read_b64_tr_b16 a[210:211], v1 offset:256\n"
    "ds_read_b64_tr_b16 a[212:213], v1 offset:8448\n"
    "v_mfma_f32_32x32x16_bf16 v[138:153], v[76:79], a[174:177], v[138:153]\n"
    "s_add_u32 m0, s53, 2048\n"
    "s_nop 0\n"
    "buffer_load_dwordx4 v249, s[8:11], s54 offen lds\n"
    "ds_read_b64_tr_b16 a[214:215], v1 offset:16640\n"
    "ds_read_b64_tr_b16 a[216:217], v1 offset:24832\n"
    "ds_read_b64_tr_b16 a[218:219], v1 offset:33024\n"
    "v_mfma_f32_32x32x8_bf16 v[138:153], v[80:81], a[178:179], v[138:153]\n"
    "s_waitcnt lgkmcnt(14)\n"
    "ds_read_b64_tr_b16 a[220:221], v2 offset:256\n"
    "ds_read_b64_tr_b16 a[222:223], v2 offset:8448\n"
    "ds_read_b64_tr_b16 a[224:225], v2 offset:16640\n"
    "v_mfma_f32_32x32x16_bf16 v[154:169], v[72:75], a[180:183], 0\n"
    "s_add_u32 m0, s53, 3072\n"
    "s_add_u32 s54, s52, 30720\n"
    "buffer_load_dwordx4 v249, s[8:11], s54 offen lds\n"
    "ds_read_b64_tr_b16 a[226:227], v2 offset:24832\n"
    "ds_read_b64_tr_b16 a[228:229], v2 offset:33024\n"
    "ds_read_b64_tr_b16 a[230:231], v3 offset:256\n"
    "s_waitcnt lgkmcnt(14)\n"
    "v_mfma_f32_32x32x16_bf16 v[154:169], v[76:79], a[184:187], v[154:169]\n"
    "s_add_u32 s54, s52, 163840\n"
    "ds_read_b64_tr_b16 a[232:233], v3 offset:8448\n"
    "ds_read_b64_tr_b16 a[234:235], v3 offset:16640\n"
    "ds_read_b64_tr_b16 a[236:237], v3 offset:24832\n"
    "v_mfma_f32_32x32x8_bf16 v[154:169], v[80:81], a[188:189], v[154:169]\n"
    "s_add_u32 m0, s53, 16384\n"
    "s_nop 0\n"
    "buffer_load_dwordx4 v249, s[8:11], s54 offen lds\n"
    "ds_read_b64_tr_b16 a[238:239], v3 offset:33024\n"
    "v_mfma_f32_32x32x16_bf16 v[170:185], v[72:75], a[190:193], 0\n"
    "s_add_u32 s54, s52, 174080\n"
    "v_cvt_pk_bf16_f32 v12, v122, v138\n"
    "v_cvt_pk_bf16_f32 v16, v123, v139\n"
    "v_cvt_pk_bf16_f32 v20, v124, v140\n"
    "v_cvt_pk_bf16_f32 v24, v125, v141\n"
    "v_cvt_pk_bf16_f32 v28, v126, v142\n"
    "v_cvt_pk_bf16_f32 v32, v127, v143\n"
    "v_mfma_f32_32x32x16_bf16 v[170:185], v[76:79], a[194:197], v[170:185]\n"
    "s_add_u32 m0, s53, 17408\n"
    "s_nop 0\n"
    "buffer_load_dwordx4 v249, s[8:11], s54 offen lds\n"
    "v_cvt_pk_bf16_f32 v36, v128, v144\n"
    "v_cvt_pk_bf16_f32 v40, v129, v145\n"
    "v_cvt_pk_bf16_f32 v44, v130, v146\n"
    "v_cvt_pk_bf16_f32 v48, v131, v147\n"
    "v_mfma_f32_32x32x8_bf16 v[170:185], v[80:81], a[198:199], v[170:185]\n"
    "s_add_u32 s54, s52, 184320\n"
    "v_mfma_f32_32x32x16_bf16 v[122:137], v[72:75], a[200:203], 0\n"
    "s_add_u32 m0, s53, 18432\n"
    "s_nop 0\n"
    "buffer_load_dwordx4 v249, s[8:11], s54 offen lds\n"
    "s_nop 0\n"
    "s_nop 0\n"
    "s_nop 0\n"
    "s_waitcnt lgkmcnt(14)\n"
    "v_mfma_f32_32x32x16_bf16 v[122:137], v[76:79], a[204:207], v[122:137]\n"
    "s_nop 0\n"
    "s_nop 0\n"
    "s_nop 0\n"
    "v_mfma_f32_32x32x8_bf16 v[122:137], v[80:81], a[208:209], v[122:137]\n"
    "s_add_u32 m0, s53, 19456\n"
    "s_add_u32 s54, s52, 194560\n"
    "buffer_load_dwordx4 v249, s[8:11], s54 offen lds\n"
    "v_cvt_pk_bf16_f32 v13, v154, v170\n"
    "v_cvt_pk_bf16_f32 v17, v155, v171\n"
    "v_cvt_pk_bf16_f32 v21, v156, v172\n"
    "s_waitcnt lgkmcnt(13)\n"
    "v_mfma_f32_32x32x16_bf16 v[138:153], v[72:75], a[210:213], 0\n"
    "s_waitcnt lgkmcnt(11)\n"
    "v_cvt_pk_bf16_f32 v25, v157, v173\n"
    "v_cvt_pk_bf16_f32 v29, v158, v174\n"
    "v_cvt_pk_bf16_f32 v33, v159, v175\n"
    "v_mfma_f32_32x32x16_bf16 v[138:153], v[76:79], a[214:217], v[138:153]\n"
    "s_add_u32 m0, s53, 32768\n"
    "s_add_u32 s54, s52, 327680\n"
    "buffer_load_dwordx4 v249, s[8:11], s54 offen lds\n"
    "v_cvt_pk_bf16_f32 v37, v160, v176\n"
    "v_cvt_pk_bf16_f32 v41, v161, v177\n"
    "v_cvt_pk_bf16_f32 v45, v162, v178\n"
    "s_waitcnt lgkmcnt(10)\n"
    "v_mfma_f32_32x32x8_bf16 v[138:153], v[80:81], a[218:219], v[138:153]\n"
    "s_waitcnt lgkmcnt(8)\n"
    "v_cvt_pk_bf16_f32 v49, v163, v179\n"
    "v_mfma_f32_32x32x16_bf16 v[154:169], v[72:75], a[220:223], 0\n"
    "s_add_u32 m0, s53, 33792\n"
    "s_add_u32 s54, s52, 337920\n"
    "buffer_load_dwordx4 v249, s[8:11], s54 offen lds\n"
    "s_nop 0\n"
    "s_nop 0\n"
    "s_nop 0\n"
    "s_waitcnt lgkmcnt(6)\n"
    "v_mfma_f32_32x32x16_bf16 v[154:169], v[76:79], a[224:227], v[154:169]\n"
    "s_nop 0\n"
    "s_nop 0\n"
    "s_nop 0\n"
    "s_waitcnt lgkmcnt(5)\n"
    "v_mfma_f32_32x32x8_bf16 v[154:169], v[80:81], a[228:229], v[154:169]\n"
    "s_add_u32 m0, s53, 34816\n"
    "s_add_u32 s54, s52, 348160\n"
    "buffer_load_dwordx4 v249, s[8:11], s54 offen lds\n"
    "v_cvt_pk_bf16_f32 v14, v122, v138\n"
    "v_cvt_pk_bf16_f32 v18, v123, v139\n"
    "v_cvt_pk_bf16_f32 v22, v124, v140\n"
    "s_waitcnt lgkmcnt(3)\n"
    "v_mfma_f32_32x32x16_bf16 v[170:185], v[72:75], a[230:233], 0\n"
    "s_waitcnt lgkmcnt(1)\n"
    "v_cvt_pk_bf16_f32 v26, v125, v141\n"
    "v_cvt_pk_bf16_f32 v30, v126, v142\n"
    "v_cvt_pk_bf16_f32 v34, v127, v143\n"
    "v_mfma_f32_32x32x16_bf16 v[170:185], v[76:79], a[234:237], v[170:185]\n"
    "s_add_u32 m0, s53, 35840\n"
    "s_add_u32 s54, s52, 358400\n"
    "buffer_load_dwordx4 v249, s[8:11], s54 offen lds\n"
    "v_cvt_pk_bf16_f32 v38, v128, v144\n"
    "v_cvt_pk_bf16_f32 v42, v129, v145\n"
    "v_cvt_pk_bf16_f32 v46, v130, v146\n"
    "s_waitcnt lgkmcnt(0)\n"
    "v_mfma_f32_32x32x8_bf16 v[170:185], v[80:81], a[238:239], v[170:185]\n"
    "v_cvt_pk_bf16_f32 v50, v131, v147\n"
    "s_nop 10\n"
    "v_cvt_pk_bf16_f32 v35, v159, v175\n"
    "v_cvt_pk_bf16_f32 v39, v160, v176\n"
    "v_cvt_pk_bf16_f32 v43, v161, v177\n"
    "v_cvt_pk_bf16_f32 v47, v162, v178\n"
    "v_cvt_pk_bf16_f32 v51, v163, v179\n"
    "ds_write_b128 v4, v[32:35] offset:0\n"
    "ds_write_b128 v4, v[36:39] offset:1024\n"
    "ds_write_b128 v4, v[40:43] offset:2048\n"
    "ds_write_b128 v4, v[44:47] offset:3072\n"
    "ds_write_b128 v4, v[48:51] offset:4096\n"
    "v_cvt_pk_bf16_f32 v15, v154, v170\n"
    "v_cvt_pk_bf16_f32 v19, v155, v171\n"
    "v_cvt_pk_bf16_f32 v23, v156, v172\n"
    "v_cvt_pk_bf16_f32 v27, v157, v173\n"
    "v_cvt_pk_bf16_f32 v31, v158, v174\n"
    "v_mfma_f32_32x32x16_bf16 v[122:137], v[82:85], v[12:15], 0\n"
    "v_mfma_f32_32x32x16_bf16 v[138:153], v[90:93], v[16:19], 0\n"
    "v_mfma_f32_32x32x16_bf16 v[154:169], v[98:101], v[20:23], 0\n"
    "v_mfma_f32_32x32x16_bf16 v[170:185], v[106:109], v[24:27], 0\n"
    "s_waitcnt lgkmcnt(0)\n"
    "s_barrier\n"
    "ds_read_b128 v[52:55], v5 offset:0\n"
    "ds_read_b128 v[56:59], v5 offset:1024\n"
    "ds_read_b128 v[60:63], v5 offset:2048\n"
    "ds_read_b128 v[64:67], v5 offset:3072\n"
    "ds_read_b128 v[68:71], v5 offset:4096\n"
    "v_mfma_f32_32x32x16_bf16 v[186:201], v[114:117], v[28:31], 0\n"
    "s_waitcnt lgkmcnt(4)\n"
    "v_mfma_f32_32x32x16_bf16 v[122:137], v[86:89], v[52:55], v[122:137]\n"
    "s_nop 5\n"
    "s_waitcnt lgkmcnt(3)\n"
    "v_mfma_f32_32x32x16_bf16 v[138:153], v[94:97], v[56:59], v[138:153]\n"
    "s_nop 3\n"
    "s_waitcnt lgkmcnt(2)\n"
    "v_mfma_f32_32x32x16_bf16 v[154:169], v[102:105], v[60:63], v[154:169]\n"
    "s_waitcnt lgkmcnt(1)\n"
    "v_cvt_pk_bf16_f32 v208, v122, v123\n"
    "v_cvt_pk_bf16_f32 v209, v124, v125\n"
    "v_cvt_pk_bf16_f32 v210, v126, v127\n"
    "v_cvt_pk_bf16_f32 v211, v128, v129\n"
    "buffer_store_dwordx4 v[208:211], v6, s[4:7], s34 offen offset:0 sc1\n"
    "v_mfma_f32_32x32x16_bf16 v[170:185], v[110:113], v[64:67], v[170:185]\n"
    "s_waitcnt lgkmcnt(0)\n"
    "v_cvt_pk_bf16_f32 v212, v130, v131\n"
    "v_cvt_pk_bf16_f32 v213, v132, v133\n"
    "v_cvt_pk_bf16_f32 v214, v134, v135\n"
    "v_cvt_pk_bf16_f32 v215, v136, v137\n"
    "buffer_store_dwordx4 v[212:215], v6, s[4:7], s34 offen offset:512 sc1\n"
    "v_mfma_f32_32x32x16_bf16 v[186:201], v[118:121], v[68:71], v[186:201]\n"
    "v_cvt_pk_bf16_f32 v216, v138, v139\n"
    "v_cvt_pk_bf16_f32 v217, v140, v141\n"
    "v_cvt_pk_bf16_f32 v218, v142, v143\n"
    "v_cvt_pk_bf16_f32 v219, v144, v145\n"
    "buffer_store_dwordx4 v[216:219], v6, s[4:7], s34 offen offset:2048 sc1\n"
    "v_cvt_pk_bf16_f32 v220, v146, v147\n"
    "v_cvt_pk_bf16_f32 v221, v148, v149\n"
    "v_cvt_pk_bf16_f32 v222, v150, v151\n"
    "v_cvt_pk_bf16_f32 v223, v152, v153\n"
    "buffer_store_dwordx4 v[220:223], v6, s[4:7], s34 offen offset:2560 sc1\n"
    "s_add_u32 s34, s34, 4096\n"
    "v_cvt_pk_bf16_f32 v208, v154, v155\n"
    "v_cvt_pk_bf16_f32 v209, v156, v157\n"
    "v_cvt_pk_bf16_f32 v210, v158, v159\n"
    "v_cvt_pk_bf16_f32 v211, v160, v161\n"
    "buffer_store_dwordx4 v[208:211], v6, s[4:7], s34 offen offset:0 sc1\n"
    "v_cvt_pk_bf16_f32 v212, v162, v163\n"
    "v_cvt_pk_bf16_f32 v213, v164, v165\n"
    "v_cvt_pk_bf16_f32 v214, v166, v167\n"
    "v_cvt_pk_bf16_f32 v215, v168, v169\n"
    "buffer_store_dwordx4 v[212:215], v6, s[4:7], s34 offen offset:512 sc1\n"
    "v_cvt_pk_bf16_f32 v216, v170, v171\n"
    ".Lpad12:\n"
    "s_cbranch_execz .Lpad13\n"
    "v_cvt_pk_bf16_f32 v217, v172, v173\n"
    "v_cvt_pk_bf16_f32 v218, v174, v175\n"
    "v_cvt_pk_bf16_f32 v219, v176, v177\n"
    "buffer_store_dwordx4 v[216:219], v6, s[4:7], s34 offen offset:2048 sc1\n"
    "v_cvt_pk_bf16_f32 v220, v178, v179\n"
    "v_cvt_pk_bf16_f32 v221, v180, v181\n"
    "v_cvt_pk_bf16_f32 v222, v182, v183\n"
    "v_cvt_pk_bf16_f32 v223, v184, v185\n"
    "buffer_store_dwordx4 v[220:223], v6, s[4:7], s34 offen offset:2560 sc1\n"
    "s_add_u32 s34, s34, 4096\n"
    "v_cvt_pk_bf16_f32 v208, v186, v187\n"
    "v_cvt_pk_bf16_f32 v209, v188, v189\n"
    "v_cvt_pk_bf16_f32 v210, v190, v191\n"
    "v_cvt_pk_bf16_f32 v211, v192, v193\n"
    "buffer_store_dwordx4 v[208:211], v6, s[4:7], s34 offen offset:0 sc1\n"
    "v_cvt_pk_bf16_f32 v212, v194, v195\n"
    "v_cvt_pk_bf16_f32 v213, v196, v197\n"
    "v_cvt_pk_bf16_f32 v214, v198, v199\n"
    "v_cvt_pk_bf16_f32 v215, v200, v201\n"
    "buffer_store_dwordx4 v[212:215], v6, s[4:7], s34 offen offset:512 sc1\n"
    "v_add_u32 v253, 0x9e3779b9, v253\n"
    "s_add_u32 m0, s53, 49152\n"
    "s_add_u32 s54, s52, 491520\n"
    "buffer_load_dwordx4 v249, s[8:11], s54 offen lds\n"
    "s_add_u32 m0, s53, 50176\n"
    "s_add_u32 s54, s52, 501760\n"
    "buffer_load_dwordx4 v249, s[8:11], s54 offen lds\n"
    "s_add_u32 m0, s53, 51200\n"
    "s_add_u32 s54, s52, 512000\n"
    "buffer_load_dwordx4 v249, s[8:11], s54 offen lds\n"
    "s_add_u32 m0, s53, 52224\n"
    "s_add_u32 s54, s52, 522240\n"
    "buffer_load_dwordx4 v249, s[8:11], s54 offen lds\n"
    "s_waitcnt vmcnt(4)\n"
    "s_barrier\n"
    "buffer_store_dword v253, v252, s[4:7], s55 offen sc1\n"
    "s_nop 0\n"
    "s_nop 0\n"
    "s_branch .Lidle2\n"
    ".Lrole_d:\n"
    "s_mov_b32 s10, 2211840\n"
    "s_mov_b32 s11, 0x20000\n"
    "s_mov_b32 s22, 2621440\n"
    "s_mov_b32 s23, 0x20000\n"
    "s_lshr_b32 s34, s28, 1\n"
    "s_lshl_b32 s31, s34, 2\n"
    "s_lshl_b32 s33, s34, 3\n"
    "s_and_b32 s34, s28, 1\n"
    "s_lshl_b32 s32, s34, 3\n"
    "s_lshl_b32 s30, s34, 2\n"
    "s_and_b32 s34, s29, 3\n"
    "s_add_u32 s31, s31, s34\n"
    "s_add_u32 s30, s30, s34\n"
    "s_lshr_b32 s50, s29, 2\n"
    "s_add_u32 s32, s32, s50\n"
    "s_add_u32 s33, s33, s50\n"
    "s_add_u32 s50, s50, 7\n"
    "s_cmp_ge_u32 s50, 8\n"
    "s_cselect_b32 s34, 8, 0\n"
    "s_sub_u32 s50, s50, s34\n"
    "s_nop 0\n"
    "s_nop 0\n"
    "s_nop 0\n"
    "s_nop 0\n"
    "v_lshrrev_b32 v246, 3, v255\n"
    "v_and_b32 v247, 6, v246\n"
    "v_xor_b32 v247, v247, v255\n"
    "v_and_b32 v247, 7, v247\n"
    "v_mul_u32_u24 v246, 0x500, v246\n"
    "v_lshl_add_u32 v249, v247, 4, v246\n"
    "v_mul_u32_u24 v248, 128, v255\n"
    "s_mul_i32 s52, s3, 40960\n"
    "s_lshl_b32 s54, s30, 7\n"
    "s_add_u32 s52, s52, s54\n"
    "s_mul_i32 s53, s3, 4096\n"
    "s_sub_u32 s43, s43, 54\n"
    "s_lshl_b32 s43, s43, 2\n"
    "s_add_u32 s43, s43, s3\n"
    "s_mul_i32 s43, s43, 3\n"
    "s_mul_i32 s45, s3, 12288\n"
    "s_add_u32 s45, s45, 81920\n"
    "s_add_u32 s34, s43, 0\n"
    "s_lshr_b32 s35, s34, 1\n"
    "s_and_b32 s34, s34, 1\n"
    "s_lshl_b32 s46, s34, 18\n"
    "s_waitcnt lgkmcnt(0)\n"
    "s_lshl_b32 s36, s35, 10\n"
    "s_add_u32 s46, s46, s36\n"
    "s_add_u32 s46, s46, 20463616\n"
    "s_mul_i32 s35, s35, 40960\n"
    "s_lshl_b32 s34, s34, 7\n"
    "s_add_u32 s34, s34, s35\n"
    "s_add_u32 m0, s45, 0\n"
    "s_add_u32 s35, s34, 1024\n"
    "buffer_load_dwordx4 v249, s[8:11], s35 offen  lds\n"
    "s_add_u32 m0, s45, 1024\n"
    "s_add_u32 s35, s34, 11264\n"
    "buffer_load_dwordx4 v249, s[8:11], s35 offen  lds\n"
    "s_add_u32 m0, s45, 2048\n"
    "s_add_u32 s35, s34, 21504\n"
    "buffer_load_dwordx4 v249, s[8:11], s35 offen  lds\n"
    "s_add_u32 m0, s45, 3072\n"
    "s_add_u32 s35, s34, 31744\n"
    "buffer_load_dwordx4 v249, s[8:11], s35 offen  lds\n"
    "s_add_u32 s34, s43, 1\n"
    "s_lshr_b32 s35, s34, 1\n"
    "s_and_b32 s34, s34, 1\n"
    "s_lshl_b32 s47, s34, 18\n"
    "s_lshl_b32 s36, s35, 10\n"
    "s_add_u32 s47, s47, s36\n"
    "s_add_u32 s47, s47, 20463616\n"
    "s_mul_i32 s35, s35, 40960\n"
    "s_lshl_b32 s34, s34, 7\n"
    "s_add_u32 s34, s34, s35\n"
    "s_add_u32 m0, s45, 4096\n"
    "s_add_u32 s35, s34, 1024\n"
    "buffer_load_dwordx4 v249, s[8:11], s35 offen  lds\n"
    "s_add_u32 m0, s45, 5120\n"
    "s_add_u32 s35, s34, 11264\n"
    "buffer_load_dwordx4 v249, s[8:11], s35 offen  lds\n"
    "s_add_u32 m0, s45, 6144\n"
    "s_add_u32 s35, s34, 21504\n"
    "buffer_load_dwordx4 v249, s[8:11], s35 offen  lds\n"
    "s_add_u32 m0, s45, 7168\n"
    "s_add_u32 s35, s34, 31744\n"
    "buffer_load_dwordx4 v249, s[8:11], s35 offen  lds\n"
    "s_add_u32 s34, s43, 2\n"
    "s_lshr_b32 s35, s34, 1\n"
    "s_and_b32 s34, s34, 1\n"
    "s_lshl_b32 s48, s34, 18\n"
    "s_lshl_b32 s36, s35, 10\n"
    "s_add_u32 s48, s48, s36\n"
    "s_add_u32 s48, s48, 20463616\n"
    "s_mul_i32 s35, s35, 40960\n"
    "s_lshl_b32 s34, s34, 7\n"
    "s_add_u32 s34, s34, s35\n"
    "s_add_u32 m0, s45, 8192\n"
    "s_add_u32 s35, s34, 1024\n"
    "buffer_load_dwordx4 v249, s[8:11], s35 offen  lds\n"
    "s_add_u32 m0, s45, 9216\n"
    "s_add_u32 s35, s34, 11264\n"
    "buffer_load_dwordx4 v249, s[8:11], s35 offen  lds\n"
    "s_add_u32 m0, s45, 10240\n"
    "s_add_u32 s35, s34, 21504\n"
    "buffer_load_dwordx4 v249, s[8:11], s35 offen  lds\n"
    "s_add_u32 m0, s45, 11264\n"
    "s_add_u32 s35, s34, 31744\n"
    "buffer_load_dwordx4 v249, s[8:11], s35 offen  lds\n"
    "s_mov_b32 s6, 21544960\n"
    "s_mov_b32 s7, 0x20000\n"
    "s_mov_b32 s34, 20201472\n"
    "v_mov_b32 v5, 0\n"
    "buffer_load_dword v253, v5, s[4:7], s34 offen sc1\n"
    "s_lshl_b32 s34, s2, 2\n"
    "s_add_u32 s34, s34, s3\n"
    "s_cmp_lt_u32 s34, 1024\n"
    "s_cselect_b32 s35, 2, 0\n"
    "s_lshl_b32 s34, s34, 6\n"
    "s_add_u32 s34, s34, 20267040\n"
    "v_cmp_gt_u32 vcc, s35, v255\n"
    "v_mul_u32_u24 v6, 65536, v255\n"
    "v_mov_b32 v7, 0x7fffffff\n"
    "v_cndmask_b32 v6, v7, v6, vcc\n"
    "buffer_store_dword v5, v6, s[4:7], s34 offen sc1\n"
    "v_lshrrev_b32 v244, 2, v251\n"
    "v_lshl_add_u32 v244, v250, 2, v244\n"
    "v_bfe_u32 v245, v255, 1, 1\n"
    "v_and_b32 v246, 6, v244\n"
    "v_or_b32 v245, v245, v246\n"
    "v_and_b32 v246, 1, v255\n"
    "v_lshlrev_b32 v246, 3, v246\n"
    "v_lshl_add_u32 v246, v244, 7, v246\n"
    "v_add_u32 v246, s45, v246\n"
    "v_xor_b32 v247, 0, v245\n"
    "v_lshl_add_u32 v0, v247, 4, v246\n"
    "v_xor_b32 v247, 2, v245\n"
    "v_lshl_add_u32 v1, v247, 4, v246\n"
    "v_xor_b32 v247, 4, v245\n"
    "v_lshl_add_u32 v2, v247, 4, v246\n"
    "v_xor_b32 v247, 6, v245\n"
    "v_lshl_add_u32 v3, v247, 4, v246\n"
    "v_lshrrev_b32 v244, 2, v251\n"
    "v_lshl_add_u32 v244, v250, 2, v244\n"
    "v_bfe_u32 v245, v255, 1, 1\n"
    "v_and_b32 v246, 6, v244\n"
    "v_or_b32 v245, v245, v246\n"
    "v_and_b32 v246, 1, v255\n"
    "v_lshlrev_b32 v246, 3, v246\n"
    "v_lshl_add_u32 v246, v244, 7, v246\n"
    "v_add_u32 v246, s53, v246\n"
    "v_xor_b32 v247, 0, v245\n"
    "v_lshl_add_u32 v240, v247, 4, v246\n"
    "v_xor_b32 v247, 2, v245\n"
    "v_lshl_add_u32 v241, v247, 4, v246\n"
    "v_xor_b32 v247, 4, v245\n"
    "v_lshl_add_u32 v242, v247, 4, v246\n"
    "v_xor_b32 v247, 6, v245\n"
    "v_lshl_add_u32 v243, v247, 4, v246\n"
    "s_lshr_b32 s55, s2, 2\n"
    "s_mul_i32 s55, s55, 128\n"
    "s_and_b32 s35, s2, 3\n"
    "s_lshl_b32 s35, s35, 2\n"
    "s_add_u32 s55, s55, s35\n"
    "s_mul_i32 s35, s3, 16384\n"
    "s_add_u32 s55, s55, s35\n"
    "s_add_u32 s55, s55, 20267008\n"
    "s_cmp_lt_u32 s3, 8\n"
    "s_cselect_b32 s35, 2, 0\n"
    "v_mul_u32_u24 v246, 8192, v255\n"
    "v_mov_b32 v247, 0x7fffffff\n"
    "v_cmp_gt_u32 vcc, s35, v255\n"
    "s_nop 1\n"
    "v_cndmask_b32 v252, v247, v246, vcc\n"
    "s_call_b64 s[0:1], .Lwarm\n"
    "s_waitcnt vmcnt(10)\n"
    "ds_read_b64_tr_b16 v[12:13], v0 offset:0\n"
    "ds_read_b64_tr_b16 v[14:15], v0 offset:2048\n"
    "ds_read_b64_tr_b16 v[16:17], v1 offset:0\n"
    "ds_read_b64_tr_b16 v[18:19], v1 offset:2048\n"
    "ds_read_b64_tr_b16 v[20:21], v2 offset:0\n"
    "ds_read_b64_tr_b16 v[22:23], v2 offset:2048\n"
    "ds_read_b64_tr_b16 v[24:25], v3 offset:0\n"
    "ds_read_b64_tr_b16 v[26:27], v3 offset:2048\n"
    "s_waitcnt lgkmcnt(6)\n"
    "buffer_store_dwordx4 v[12:15], v254, s[4:7], s46 offen sc1\n"
    "s_add_u32 s46, s46, 65536\n"
    "s_waitcnt lgkmcnt(4)\n"
    "buffer_store_dwordx4 v[16:19], v254, s[4:7], s46 offen sc1\n"
    "s_add_u32 s46, s46, 65536\n"
    "s_waitcnt lgkmcnt(2)\n"
    "buffer_store_dwordx4 v[20:23], v254, s[4:7], s46 offen sc1\n"
    "s_add_u32 s46, s46, 65536\n"
    "s_waitcnt lgkmcnt(0)\n"
    "buffer_store_dwordx4 v[24:27], v254, s[4:7], s46 offen sc1\n"
    "s_add_u32 s46, s46, 65536\n"
    "s_waitcnt vmcnt(10)\n"
    "ds_read_b64_tr_b16 v[28:29], v0 offset:4096\n"
    "ds_read_b64_tr_b16 v[30:31], v0 offset:6144\n"
    "ds_read_b64_tr_b16 v[32:33], v1 offset:4096\n"
    "ds_read_b64_tr_b16 v[34:35], v1 offset:6144\n"
    "ds_read_b64_tr_b16 v[36:37], v2 offset:4096\n"
    "ds_read_b64_tr_b16 v[38:39], v2 offset:6144\n"
    "ds_read_b64_tr_b16 v[40:41], v3 offset:4096\n"
    "ds_read_b64_tr_b16 v[42:43], v3 offset:6144\n"
    "s_waitcnt lgkmcnt(6)\n"
    "buffer_store_dwordx4 v[28:31], v254, s[4:7], s47 offen sc1\n"
    "s_add_u32 s47, s47, 65536\n"
    "s_waitcnt lgkmcnt(4)\n"
    "buffer_store_dwordx4 v[32:35], v254, s[4:7], s47 offen sc1\n"
    "s_add_u32 s47, s47, 65536\n"
    "s_waitcnt lgkmcnt(2)\n"
    "buffer_store_dwordx4 v[36:39], v254, s[4:7], s47 offen sc1\n"
    "s_add_u32 s47, s47, 65536\n"
    "s_waitcnt lgkmcnt(0)\n"
    "buffer_store_dwordx4 v[40:43], v254, s[4:7], s47 offen sc1\n"
    "s_add_u32 s47, s47, 65536\n"
    "s_waitcnt vmcnt(10)\n"
    "ds_read_b64_tr_b16 v[44:45], v0 offset:8192\n"
    "ds_read_b64_tr_b16 v[46:47], v0 offset:10240\n"
    "ds_read_b64_tr_b16 v[48:49], v1 offset:8192\n"
    "ds_read_b64_tr_b16 v[50:51], v1 offset:10240\n"
    "ds_read_b64_tr_b16 v[52:53], v2 offset:8192\n"
    "ds_read_b64_tr_b16 v[54:55], v2 offset:10240\n"
    "ds_read_b64_tr_b16 v[56:57], v3 offset:8192\n"
    "ds_read_b64_tr_b16 v[58:59], v3 offset:10240\n"
    "s_waitcnt lgkmcnt(6)\n"
    "buffer_store_dwordx4 v[44:47], v254, s[4:7], s48 offen sc1\n"
    "s_add_u32 s48, s48, 65536\n"
    "s_waitcnt lgkmcnt(4)\n"
    "buffer_store_dwordx4 v[48:51], v254, s[4:7], s48 offen sc1\n"
    "s_add_u32 s48, s48, 65536\n"
    "s_waitcnt lgkmcnt(2)\n"
    "buffer_store_dwordx4 v[52:55], v254, s[4:7], s48 offen sc1\n"
    "s_add_u32 s48, s48, 65536\n"
    "s_waitcnt lgkmcnt(0)\n"
    "buffer_store_dwordx4 v[56:59], v254, s[4:7], s48 offen sc1\n"
    "s_add_u32 s48, s48, 65536\n"
    "s_waitcnt vmcnt(13)\n"
    "v_add_u32 v253, 0x9e3779b9, v253\n"
    "s_nop 0\n"
    "s_waitcnt vmcnt(0)\n"
    "s_barrier\n"
    "buffer_store_dword v253, v252, s[4:7], s55 offen sc1\n"
    "s_add_u32 m0, s53, 0\n"
    "s_add_u32 s54, s52, 0\n"
    "buffer_load_dwordx4 v249, s[8:11], s54 offen lds\n"
    "s_add_u32 m0, s53, 1024\n"
    "s_add_u32 s54, s52, 10240\n"
    "buffer_load_dwordx4 v249, s[8:11], s54 offen lds\n"
    "s_add_u32 m0, s53, 2048\n"
    "s_add_u32 s54, s52, 20480\n"
    "buffer_load_dwordx4 v249, s[8:11], s54 offen lds\n"
    "s_add_u32 m0, s53, 3072\n"
    "s_add_u32 s54, s52, 30720\n"
    "buffer_load_dwordx4 v249, s[8:11], s54 offen lds\n"
    "s_add_u32 m0, s53, 16384\n"
    "s_add_u32 s54, s52, 163840\n"
    "buffer_load_dwordx4 v249, s[8:11], s54 offen lds\n"
    "s_add_u32 m0, s53, 17408\n"
    "s_add_u32 s54, s52, 174080\n"
    "buffer_load_dwordx4 v249, s[8:11], s54 offen lds\n"
    "s_add_u32 m0, s53, 18432\n"
    "s_add_u32 s54, s52, 184320\n"
    "buffer_load_dwordx4 v249, s[8:11], s54 offen lds\n"
    "s_add_u32 m0, s53, 19456\n"
    "s_add_u32 s54, s52, 194560\n"
    "buffer_load_dwordx4 v249, s[8:11], s54 offen lds\n"
    "s_add_u32 m0, s53, 32768\n"
    "s_add_u32 s54, s52, 327680\n"
    "buffer_load_dwordx4 v249, s[8:11], s54 offen lds\n"
    "s_add_u32 m0, s53, 33792\n"
    "s_add_u32 s54, s52, 337920\n"
    "buffer_load_dwordx4 v249, s[8:11], s54 offen lds\n"
    "s_add_u32 m0, s53, 34816\n"
    "s_add_u32 s54, s52, 348160\n"
    "buffer_load_dwordx4 v249, s[8:11], s54 offen lds\n"
    "s_add_u32 m0, s53, 35840\n"
    "s_add_u32 s54, s52, 358400\n"
    "buffer_load_dwordx4 v249, s[8:11], s54 offen lds\n"
    "s_add_u32 m0, s53, 49152\n"
    "s_add_u32 s54, s52, 491520\n"
    "buffer_load_dwordx4 v249, s[8:11], s54 offen lds\n"
    "s_add_u32 m0, s53, 50176\n"
    "s_add_u32 s54, s52, 501760\n"
    "buffer_load_dwordx4 v249, s[8:11], s54 offen lds\n"
    "s_add_u32 m0, s53, 51200\n"
    "s_add_u32 s54, s52, 512000\n"
    "buffer_load_dwordx4 v249, s[8:11], s54 offen lds\n"
    "s_add_u32 m0, s53, 52224\n"
    "s_add_u32 s54, s52, 522240\n"
    "buffer_load_dwordx4 v249, s[8:11], s54 offen lds\n"
    "s_branch .Lidle2\n"
    ".Lg_ffn:\n"
    ".Lpad13:\n"
    "s_cbranch_execz .Lpad0\n"
    "s_mov_b32 s48, s42\n"
    "s_add_u32 s49, s42, 55296\n"
    "s_mov_b32 s50, 0x7f000000\n"
    "s_mov_b32 s51, 0x7f000000\n"
    "v_pk_add_f32 v[160:161], v[160:161], v[208:209]\n"
    "v_pk_add_f32 v[162:163], v[162:163], v[210:211]\n"
    "v_pk_add_f32 v[164:165], v[164:165], v[212:213]\n"
    "v_pk_add_f32 v[166:167], v[166:167], v[214:215]\n"
    "v_pk_add_f32 v[168:169], v[168:169], v[216:217]\n"
    "v_pk_add_f32 v[170:171], v[170:171], v[218:219]\n"
    "v_pk_add_f32 v[172:173], v[172:173], v[220:221]\n"
    "v_pk_add_f32 v[174:175], v[174:175], v[222:223]\n"
    "v_pk_mul_f32 v[228:229], v[160:161], v[230:231] op_sel:[0,0] op_sel_hi:[1,0]\n"
    "v_pk_mul_f32 v[234:235], v[162:163], v[230:231] op_sel:[0,0] op_sel_hi:[1,0]\n"
    "v_exp_f32 v228, v228\n"
    "v_exp_f32 v229, v229\n"
    "v_exp_f32 v234, v234\n"
    "v_exp_f32 v235, v235\n"
    "v_pk_fma_f32 v[228:229], v[228:229], v[232:233], v[232:233] op_sel:[0,0,0] op_sel_hi:[1,0,0]\n"
    "v_pk_fma_f32 v[234:235], v[234:235], v[232:233], v[232:233] op_sel:[0,0,0] op_sel_hi:[1,0,0]\n"
    "v_rcp_f32 v228, v228\n"
    "v_rcp_f32 v229, v229\n"
    "v_rcp_f32 v234, v234\n"
    "v_rcp_f32 v235, v235\n"
    "v_pk_mul_f32 v[228:229], v[160:161], v[228:229]\n"
    "v_pk_mul_f32 v[234:235], v[162:163], v[234:235]\n"
    "v_cvt_pk_bf16_f32 v128, v228, v229\n"
    "v_cvt_pk_bf16_f32 v129, v234, v235\n"
    "v_pk_mul_f32 v[228:229], v[168:169], v[230:231] op_sel:[0,0] op_sel_hi:[1,0]\n"
    "v_pk_mul_f32 v[234:235], v[170:171], v[230:231] op_sel:[0,0] op_sel_hi:[1,0]\n"
    "v_exp_f32 v228, v228\n"
    "v_exp_f32 v229, v229\n"
    "v_exp_f32 v234, v234\n"
    "v_exp_f32 v235, v235\n"
    "v_pk_fma_f32 v[228:229], v[228:229], v[232:233], v[232:233] op_sel:[0,0,0] op_sel_hi:[1,0,0]\n"
    "v_pk_fma_f32 v[234:235], v[234:235], v[232:233], v[232:233] op_sel:[0,0,0] op_sel_hi:[1,0,0]\n"
    "v_rcp_f32 v228, v228\n"
    "v_rcp_f32 v229, v229\n"
    "v_rcp_f32 v234, v234\n"
    "v_rcp_f32 v235, v235\n"
    "v_pk_mul_f32 v[228:229], v[168:169], v[228:229]\n"
    "v_pk_mul_f32 v[234:235], v[170:171], v[234:235]\n"
    "v_cvt_pk_bf16_f32 v130, v228, v229\n"
    "v_cvt_pk_bf16_f32 v131, v234, v235\n"
    "v_pk_mul_f32 v[228:229], v[164:165], v[230:231] op_sel:[0,1] op_sel_hi:[1,1]\n"
    "v_pk_mul_f32 v[234:235], v[166:167], v[230:231] op_sel:[0,1] op_sel_hi:[1,1]\n"
    "v_exp_f32 v228, v228\n"
    "v_exp_f32 v229, v229\n"
    "v_exp_f32 v234, v234\n"
    "v_exp_f32 v235, v235\n"
    "v_pk_fma_f32 v[228:229], v[228:229], v[232:233], v[232:233] op_sel:[0,1,1] op_sel_hi:[1,1,1]\n"
    "v_pk_fma_f32 v[234:235], v[234:235], v[232:233], v[232:233] op_sel:[0,1,1] op_sel_hi:[1,1,1]\n"
    "v_rcp_f32 v228, v228\n"
    "v_rcp_f32 v229, v229\n"
    "v_rcp_f32 v234, v234\n"
    "v_rcp_f32 v235, v235\n"
    "v_pk_mul_f32 v[228:229], v[164:165], v[228:229]\n"
    "v_pk_mul_f32 v[234:235], v[166:167], v[234:235]\n"
    "v_cvt_pk_bf16_f32 v132, v228, v229\n"
    "v_cvt_pk_bf16_f32 v133, v234, v235\n"
    "v_pk_mul_f32 v[228:229], v[172:173], v[230:231] op_sel:[0,1] op_sel_hi:[1,1]\n"
    "v_pk_mul_f32 v[234:235], v[174:175], v[230:231] op_sel:[0,1] op_sel_hi:[1,1]\n"
    "v_exp_f32 v228, v228\n"
    "v_exp_f32 v229, v229\n"
    "v_exp_f32 v234, v234\n"
    "v_exp_f32 v235, v235\n"
    "v_pk_fma_f32 v[228:229], v[228:229], v[232:233], v[232:233] op_sel:[0,1,1] op_sel_hi:[1,1,1]\n"
    "v_pk_fma_f32 v[234:235], v[234:235], v[232:233], v[232:233] op_sel:[0,1,1] op_sel_hi:[1,1,1]\n"
    "v_rcp_f32 v228, v228\n"
    "v_rcp_f32 v229, v229\n"
    "v_rcp_f32 v234, v234\n"
    "v_rcp_f32 v235, v235\n"
    "v_pk_mul_f32 v[228:229], v[172:173], v[228:229]\n"
    "v_pk_mul_f32 v[234:235], v[174:175], v[234:235]\n"
    "v_cvt_pk_bf16_f32 v134, v228, v229\n"
    "v_cvt_pk_bf16_f32 v135, v234, v235\n"
    "s_nop 1\n"
    "s_branch .Lg_join\n"
    ".Lcold:\n"
    ".Lp1:\n"
    "s_waitcnt vmcnt(0)\n"
    "s_mov_b32 s57, 65536\n"
    ".Lpl1:\n"
    "buffer_load_dwordx4 v[16:19], v244, s[4:7], s40 offen offset:0 sc1\n"
    "s_waitcnt vmcnt(0)\n"
    "v_cmp_ne_u32 vcc, v16, v253\n"
    "s_nop 0\n"
    "v_cmp_ne_u32 s[58:59], v17, v253\n"
    "s_nop 0\n"
    "s_or_b64 vcc, vcc, s[58:59]\n"
    "v_cmp_ne_u32 s[58:59], v18, v253\n"
    "s_nop 0\n"
    "s_or_b64 vcc, vcc, s[58:59]\n"
    "v_cmp_ne_u32 s[58:59], v19, v253\n"
    "s_nop 0\n"
    "s_or_b64 vcc, vcc, s[58:59]\n"
    "s_and_b32 s59, vcc_hi, 0xffff\n"
    "s_or_b32 s58, vcc_lo, s59\n"
    "s_cbranch_scc0 .Lpr1\n"
    "s_sub_u32 s57, s57, 1\n"
    "s_cmp_lg_u32 s57, 0\n"
    "s_cbranch_scc1 .Lpl1\n"
    "s_branch .Lpr1\n"
    ".Lp2:\n"
    "s_waitcnt vmcnt(0)\n"
    "s_mov_b32 s57, 65536\n"
    ".Lpl2:\n"
    "buffer_load_dword v158, v244, s[4:7], s40 offen offset:0 sc1\n"
    "s_waitcnt vmcnt(0)\n"
    "v_cmp_ne_u32 vcc, v158, v253\n"
    "s_nop 0\n"
    "s_and_b32 s58, vcc_lo, 0x7\n"
    "s_cbranch_scc0 .Lpr2\n"
    "s_sub_u32 s57, s57, 1\n"
    "s_cmp_lg_u32 s57, 0\n"
    "s_cbranch_scc1 .Lpl2\n"
    "s_branch .Lpr2\n"
    ".Lhr_f:\n"
    "s_sleep 4\n"
    "buffer_load_dwordx4 v[216:219], v220, s[4:7], s0 offen sc1\n"
    "s_waitcnt vmcnt(0)\n"
    "s_sub_u32 s57, s57, 1\n"
    "s_cbranch_scc0 .Lh_f\n"
    "s_branch .Lhd_f\n"
    ".Lp3:\n"
    "s_waitcnt vmcnt(0)\n"
    "s_mov_b32 s57, 65536\n"
    ".Lpl3:\n"
    "buffer_load_dwordx4 v[0:3], v244, s[4:7], s40 offen offset:0 sc1\n"
    "s_waitcnt vmcnt(0)\n"
    "v_cmp_ne_u32 vcc, v0, v253\n"
    "s_nop 0\n"
    "v_cmp_ne_u32 s[58:59], v1, v253\n"
    "s_nop 0\n"
    "s_or_b64 vcc, vcc, s[58:59]\n"
    "v_cmp_ne_u32 s[58:59], v2, v253\n"
    "s_nop 0\n"
    "s_or_b64 vcc, vcc, s[58:59]\n"
    "v_cmp_ne_u32 s[58:59], v3, v253\n"
    "s_nop 0\n"
    "s_or_b64 vcc, vcc, s[58:59]\n"
    "s_or_b32 s58, vcc_lo, vcc_hi\n"
    "s_cbranch_scc0 .Lpr3\n"
    "s_sub_u32 s57, s57, 1\n"
    "s_cmp_lg_u32 s57, 0\n"
    "s_cbranch_scc1 .Lpl3\n"
    "s_branch .Lpr3\n"
    ".Lre0:\n"
    "s_waitcnt vmcnt(0)\n"
    "v_and_b32 v228, 31, v255\n"
    "v_lshlrev_b32 v228, 2, v228\n"
    "buffer_store_dword v253, v228, s[4:7], s1 offen sc1\n"
    "v_swap_b32 v2, v4\n"
    "v_swap_b32 v3, v5\n"
    "s_mov_b32 s57, 65544\n"
    "s_mov_b32 s56, 20398080\n"
    "v_min_u32 v228, 39, v255\n"
    "v_lshlrev_b32 v228, 9, v228\n"
    ".Lrl0:\n"
    "v_mov_b32 v232, 0\n"
    "v_or3_b32 v232, v0, v1, v232\n"
    "v_or3_b32 v232, v2, v3, v232\n"
    "v_or3_b32 v232, v4, v5, v232\n"
    "v_or3_b32 v232, v6, v7, v232\n"
    "v_or3_b32 v232, v8, v9, v232\n"
    "v_or3_b32 v232, v10, v11, v232\n"
    "v_or3_b32 v232, v12, v13, v232\n"
    "v_or3_b32 v232, v14, v15, v232\n"
    "v_or3_b32 v232, v16, v17, v232\n"
    "v_or3_b32 v232, v18, v19, v232\n"
    "v_or3_b32 v232, v20, v21, v232\n"
    "v_or3_b32 v232, v22, v23, v232\n"
    "v_or3_b32 v232, v24, v25, v232\n"
    "v_or3_b32 v232, v26, v27, v232\n"
    "v_or3_b32 v232, v28, v29, v232\n"
    "v_or3_b32 v232, v30, v31, v232\n"
    "v_and_b32 v232, 0x4000, v232\n"
    "v_cmp_ne_u32 vcc, 0, v232\n"
    "s_nop 0\n"
    "s_cbranch_vccz .Lrd0\n"
    "s_and_b32 s58, s57, 63\n"
    "s_mov_b32 s58, 1\n"
    "s_cbranch_scc1 .Lrf0\n"
    "buffer_load_dword v224, v228, s[4:7], s56 offen offset:0 sc1\n"
    "buffer_load_dword v225, v228, s[4:7], s56 offen offset:128 sc1\n"
    "buffer_load_dword v226, v228, s[4:7], s56 offen offset:256 sc1\n"
    "buffer_load_dword v227, v228, s[4:7], s56 offen offset:384 sc1\n"
    "s_waitcnt vmcnt(0)\n"
    "v_cmp_ne_u32 vcc, v224, v253\n"
    "v_cmp_ne_u32 s[58:59], v225, v253\n"
    "s_nop 0\n"
    "s_or_b64 vcc, vcc, s[58:59]\n"
    "v_cmp_ne_u32 s[58:59], v226, v253\n"
    "s_nop 0\n"
    "s_or_b64 vcc, vcc, s[58:59]\n"
    "v_cmp_ne_u32 s[58:59], v227, v253\n"
    "s_nop 0\n"
    "s_or_b64 vcc, vcc, s[58:59]\n"
    "s_and_b32 s59, vcc_hi, 0xff\n"
    "s_or_b32 s58, vcc_lo, s59\n"
    ".Lrf0:\n"
    "s_add_u32 s41, s40, 0\n"
    "buffer_load_dwordx4 v[0:3], v244, s[4:7], s41 offen sc1\n"
    "s_add_u32 s41, s40, 16384\n"
    "buffer_load_dwordx4 v[4:7], v244, s[4:7], s41 offen sc1\n"
    "s_add_u32 s41, s40, 131072\n"
    "buffer_load_dwordx4 v[8:11], v244, s[4:7], s41 offen sc1\n"
    "s_add_u32 s41, s40, 147456\n"
    "buffer_load_dwordx4 v[12:15], v244, s[4:7], s41 offen sc1\n"
    "s_add_u32 s41, s40, 262144\n"
    "buffer_load_dwordx4 v[16:19], v244, s[4:7], s41 offen sc1\n"
    "s_add_u32 s41, s40, 278528\n"
    "buffer_load_dwordx4 v[20:23], v244, s[4:7], s41 offen sc1\n"
    "s_add_u32 s41, s40, 393216\n"
    "buffer_load_dwordx4 v[24:27], v244, s[4:7], s41 offen sc1\n"
    "s_add_u32 s41, s40, 409600\n"
    "buffer_load_dwordx4 v[28:31], v244, s[4:7], s41 offen sc1\n"
    "s_waitcnt vmcnt(0)\n"
    "s_cmp_eq_u32 s58, 0\n"
    "s_cbranch_scc1 .Lrd0\n"
    "s_sub_u32 s57, s57, 1\n"
    "s_cmp_lg_u32 s57, 1\n"
    "s_cbranch_scc1 .Lrl0\n"
    ".Lrd0:\n"
    "v_mov_b32 v238, v236\n"
    "v_mov_b32 v239, v237\n"
    "v_and_b32 v232, v235, v0\n"
    "v_and_b32 v233, v235, v2\n"
    "v_dot2c_f32_bf16 v238, v232, v232\n"
    "v_dot2c_f32_bf16 v239, v233, v233\n"
    "v_dot2c_f32_bf16 v238, v1, v1\n"
    "v_dot2c_f32_bf16 v239, v3, v3\n"
    "v_dot2c_f32_bf16 v238, v4, v4\n"
    "v_dot2c_f32_bf16 v239, v6, v6\n"
    "v_dot2c_f32_bf16 v238, v5, v5\n"
    "v_dot2c_f32_bf16 v239, v7, v7\n"
    "v_dot2c_f32_bf16 v238, v8, v8\n"
    "v_dot2c_f32_bf16 v239, v10, v10\n"
    "v_dot2c_f32_bf16 v238, v9, v9\n"
    "v_dot2c_f32_bf16 v239, v11, v11\n"
    "v_dot2c_f32_bf16 v238, v12, v12\n"
    "v_dot2c_f32_bf16 v239, v14, v14\n"
    "v_dot2c_f32_bf16 v238, v13, v13\n"
    "v_dot2c_f32_bf16 v239, v15, v15\n"
    "v_dot2c_f32_bf16 v238, v16, v16\n"
    "v_dot2c_f32_bf16 v239, v18, v18\n"
    "v_dot2c_f32_bf16 v238, v17, v17\n"
    "v_dot2c_f32_bf16 v239, v19, v19\n"
    "v_dot2c_f32_bf16 v238, v20, v20\n"
    "v_dot2c_f32_bf16 v239, v22, v22\n"
    "v_dot2c_f32_bf16 v238, v21, v21\n"
    "v_dot2c_f32_bf16 v239, v23, v23\n"
    "v_dot2c_f32_bf16 v238, v24, v24\n"
    "v_dot2c_f32_bf16 v239, v26, v26\n"
    "v_dot2c_f32_bf16 v238, v25, v25\n"
    "v_dot2c_f32_bf16 v239, v27, v27\n"
    "v_dot2c_f32_bf16 v238, v28, v28\n"
    "v_dot2c_f32_bf16 v239, v30, v30\n"
    "v_dot2c_f32_bf16 v238, v29, v29\n"
    "v_dot2c_f32_bf16 v239, v31, v31\n"
    "v_swap_b32 v2, v4\n"
    "v_swap_b32 v3, v5\n"
    "s_nop 3\n"
    "s_branch .Lrr0\n"
    ".Lre1:\n"
    "s_waitcnt vmcnt(0)\n"
    "v_and_b32 v228, 31, v255\n"
    "v_lshlrev_b32 v228, 2, v228\n"
    "buffer_store_dword v253, v228, s[4:7], s1 offen sc1\n"
    "v_swap_b32 v34, v36\n"
    "v_swap_b32 v35, v37\n"
    "s_mov_b32 s57, 65544\n"
    "s_mov_b32 s56, 20398080\n"
    "v_min_u32 v228, 39, v255\n"
    "v_lshlrev_b32 v228, 9, v228\n"
    ".Lrl1:\n"
    "v_mov_b32 v232, 0\n"
    "v_or3_b32 v232, v32, v33, v232\n"
    "v_or3_b32 v232, v34, v35, v232\n"
    "v_or3_b32 v232, v36, v37, v232\n"
    "v_or3_b32 v232, v38, v39, v232\n"
    "v_or3_b32 v232, v40, v41, v232\n"
    "v_or3_b32 v232, v42, v43, v232\n"
    "v_or3_b32 v232, v44, v45, v232\n"
    "v_or3_b32 v232, v46, v47, v232\n"
    "v_or3_b32 v232, v48, v49, v232\n"
    "v_or3_b32 v232, v50, v51, v232\n"
    "v_or3_b32 v232, v52, v53, v232\n"
    "v_or3_b32 v232, v54, v55, v232\n"
    "v_or3_b32 v232, v56, v57, v232\n"
    "v_or3_b32 v232, v58, v59, v232\n"
    "v_or3_b32 v232, v60, v61, v232\n"
    "v_or3_b32 v232, v62, v63, v232\n"
    "v_and_b32 v232, 0x4000, v232\n"
    "v_cmp_ne_u32 vcc, 0, v232\n"
    "s_nop 0\n"
    "s_cbranch_vccz .Lrd1\n"
    "s_and_b32 s58, s57, 63\n"
    "s_mov_b32 s58, 1\n"
    "s_cbranch_scc1 .Lrf1\n"
    "buffer_load_dword v224, v228, s[4:7], s56 offen offset:0 sc1\n"
    "buffer_load_dword v225, v228, s[4:7], s56 offen offset:128 sc1\n"
    "buffer_load_dword v226, v228, s[4:7], s56 offen offset:256 sc1\n"
    "buffer_load_dword v227, v228, s[4:7], s56 offen offset:384 sc1\n"
    "s_waitcnt vmcnt(0)\n"
    "v_cmp_ne_u32 vcc, v224, v253\n"
    "v_cmp_ne_u32 s[58:59], v225, v253\n"
    "s_nop 0\n"
    "s_or_b64 vcc, vcc, s[58:59]\n"
    "v_cmp_ne_u32 s[58:59], v226, v253\n"
    "s_nop 0\n"
    "s_or_b64 vcc, vcc, s[58:59]\n"
    "v_cmp_ne_u32 s[58:59], v227, v253\n"
    "s_nop 0\n"
    "s_or_b64 vcc, vcc, s[58:59]\n"
    "s_and_b32 s59, vcc_hi, 0xff\n"
    "s_or_b32 s58, vcc_lo, s59\n"
    ".Lrf1:\n"
    "s_add_u32 s41, s40, 524288\n"
    "buffer_load_dwordx4 v[32:35], v244, s[4:7], s41 offen sc1\n"
    "s_add_u32 s41, s40, 540672\n"
    "buffer_load_dwordx4 v[36:39], v244, s[4:7], s41 offen sc1\n"
    "s_add_u32 s41, s40, 655360\n"
    "buffer_load_dwordx4 v[40:43], v244, s[4:7], s41 offen sc1\n"
    "s_add_u32 s41, s40, 671744\n"
    "buffer_load_dwordx4 v[44:47], v244, s[4:7], s41 offen sc1\n"
    "s_add_u32 s41, s40, 786432\n"
    "buffer_load_dwordx4 v[48:51], v244, s[4:7], s41 offen sc1\n"
    "s_add_u32 s41, s40, 802816\n"
    "buffer_load_dwordx4 v[52:55], v244, s[4:7], s41 offen sc1\n"
    "s_add_u32 s41, s40, 917504\n"
    "buffer_load_dwordx4 v[56:59], v244, s[4:7], s41 offen sc1\n"
    "s_add_u32 s41, s40, 933888\n"
    "buffer_load_dwordx4 v[60:63], v244, s[4:7], s41 offen sc1\n"
    "s_waitcnt vmcnt(0)\n"
    "s_cmp_eq_u32 s58, 0\n"
    "s_cbranch_scc1 .Lrd1\n"
    "s_sub_u32 s57, s57, 1\n"
    "s_cmp_lg_u32 s57, 1\n"
    "s_cbranch_scc1 .Lrl1\n"
    ".Lrd1:\n"
    "v_mov_b32 v238, v236\n"
    "v_mov_b32 v239, v237\n"
    "v_dot2c_f32_bf16 v238, v32, v32\n"
    "v_dot2c_f32_bf16 v239, v34, v34\n"
    "v_dot2c_f32_bf16 v238, v33, v33\n"
    "v_dot2c_f32_bf16 v239, v35, v35\n"
    "v_dot2c_f32_bf16 v238, v36, v36\n"
    "v_dot2c_f32_bf16 v239, v38, v38\n"
    "v_dot2c_f32_bf16 v238, v37, v37\n"
    "v_dot2c_f32_bf16 v239, v39, v39\n"
    "v_dot2c_f32_bf16 v238, v40, v40\n"
    "v_dot2c_f32_bf16 v239, v42, v42\n"
    "v_dot2c_f32_bf16 v238, v41, v41\n"
    "v_dot2c_f32_bf16 v239, v43, v43\n"
    "v_dot2c_f32_bf16 v238, v44, v44\n"
    "v_dot2c_f32_bf16 v239, v46, v46\n"
    "v_dot2c_f32_bf16 v238, v45, v45\n"
    "v_dot2c_f32_bf16 v239, v47, v47\n"
    "v_dot2c_f32_bf16 v238, v48, v48\n"
    "v_dot2c_f32_bf16 v239, v50, v50\n"
    "v_dot2c_f32_bf16 v238, v49, v49\n"
    "v_dot2c_f32_bf16 v239, v51, v51\n"
    "v_dot2c_f32_bf16 v238, v52, v52\n"
    "v_dot2c_f32_bf16 v239, v54, v54\n"
    "v_dot2c_f32_bf16 v238, v53, v53\n"
    "v_dot2c_f32_bf16 v239, v55, v55\n"
    "v_dot2c_f32_bf16 v238, v56, v56\n"
    "v_dot2c_f32_bf16 v239, v58, v58\n"
    "v_dot2c_f32_bf16 v238, v57, v57\n"
    "v_dot2c_f32_bf16 v239, v59, v59\n"
    "v_dot2c_f32_bf16 v238, v60, v60\n"
    "v_dot2c_f32_bf16 v239, v62, v62\n"
    "v_dot2c_f32_bf16 v238, v61, v61\n"
    "v_dot2c_f32_bf16 v239, v63, v63\n"
    "v_swap_b32 v34, v36\n"
    "v_swap_b32 v35, v37\n"
    "s_nop 3\n"
    "s_branch .Lrr1\n"
    ".Lre2:\n"
    "s_waitcnt vmcnt(0)\n"
    "v_and_b32 v228, 31, v255\n"
    "v_lshlrev_b32 v228, 2, v228\n"
    "buffer_store_dword v253, v228, s[4:7], s1 offen sc1\n"
    "v_swap_b32 v66, v68\n"
    "v_swap_b32 v67, v69\n"
    "s_mov_b32 s57, 65544\n"
    "s_mov_b32 s56, 20398080\n"
    "v_min_u32 v228, 39, v255\n"
    "v_lshlrev_b32 v228, 9, v228\n"
    ".Lrl2:\n"
    "v_mov_b32 v232, 0\n"
    "v_or3_b32 v232, v64, v65, v232\n"
    "v_or3_b32 v232, v66, v67, v232\n"
    "v_or3_b32 v232, v68, v69, v232\n"
    "v_or3_b32 v232, v70, v71, v232\n"
    "v_or3_b32 v232, v72, v73, v232\n"
    "v_or3_b32 v232, v74, v75, v232\n"
    "v_or3_b32 v232, v76, v77, v232\n"
    "v_or3_b32 v232, v78, v79, v232\n"
    "v_or3_b32 v232, v80, v81, v232\n"
    "v_or3_b32 v232, v82, v83, v232\n"
    "v_or3_b32 v232, v84, v85, v232\n"
    "v_or3_b32 v232, v86, v87, v232\n"
    "v_or3_b32 v232, v88, v89, v232\n"
    "v_or3_b32 v232, v90, v91, v232\n"
    "v_or3_b32 v232, v92, v93, v232\n"
    "v_or3_b32 v232, v94, v95, v232\n"
    "v_and_b32 v232, 0x4000, v232\n"
    "v_cmp_ne_u32 vcc, 0, v232\n"
    "s_nop 0\n"
    "s_cbranch_vccz .Lrd2\n"
    "s_and_b32 s58, s57, 63\n"
    "s_mov_b32 s58, 1\n"
    "s_cbranch_scc1 .Lrf2\n"
    "buffer_load_dword v224, v228, s[4:7], s56 offen offset:0 sc1\n"
    "buffer_load_dword v225, v228, s[4:7], s56 offen offset:128 sc1\n"
    "buffer_load_dword v226, v228, s[4:7], s56 offen offset:256 sc1\n"
    "buffer_load_dword v227, v228, s[4:7], s56 offen offset:384 sc1\n"
    "s_waitcnt vmcnt(0)\n"
    "v_cmp_ne_u32 vcc, v224, v253\n"
    "v_cmp_ne_u32 s[58:59], v225, v253\n"
    "s_nop 0\n"
    "s_or_b64 vcc, vcc, s[58:59]\n"
    "v_cmp_ne_u32 s[58:59], v226, v253\n"
    "s_nop 0\n"
    "s_or_b64 vcc, vcc, s[58:59]\n"
    "v_cmp_ne_u32 s[58:59], v227, v253\n"
    "s_nop 0\n"
    "s_or_b64 vcc, vcc, s[58:59]\n"
    "s_and_b32 s59, vcc_hi, 0xff\n"
    "s_or_b32 s58, vcc_lo, s59\n"
    ".Lrf2:\n"
    "s_add_u32 s41, s40, 1048576\n"
    "buffer_load_dwordx4 v[64:67], v244, s[4:7], s41 offen sc1\n"
    "s_add_u32 s41, s40, 1064960\n"
    "buffer_load_dwordx4 v[68:71], v244, s[4:7], s41 offen sc1\n"
    "s_add_u32 s41, s40, 1179648\n"
    "buffer_load_dwordx4 v[72:75], v244, s[4:7], s41 offen sc1\n"
    "s_add_u32 s41, s40, 1196032\n"
    "buffer_load_dwordx4 v[76:79], v244, s[4:7], s41 offen sc1\n"
    "s_add_u32 s41, s40, 1310720\n"
    "buffer_load_dwordx4 v[80:83], v244, s[4:7], s41 offen sc1\n"
    "s_add_u32 s41, s40, 1327104\n"
    "buffer_load_dwordx4 v[84:87], v244, s[4:7], s41 offen sc1\n"
    "s_add_u32 s41, s40, 1441792\n"
    "buffer_load_dwordx4 v[88:91], v244, s[4:7], s41 offen sc1\n"
    "s_add_u32 s41, s40, 1458176\n"
    "buffer_load_dwordx4 v[92:95], v244, s[4:7], s41 offen sc1\n"
    "s_waitcnt vmcnt(0)\n"
    "s_cmp_eq_u32 s58, 0\n"
    "s_cbranch_scc1 .Lrd2\n"
    "s_sub_u32 s57, s57, 1\n"
    "s_cmp_lg_u32 s57, 1\n"
    "s_cbranch_scc1 .Lrl2\n"
    ".Lrd2:\n"
    "v_mov_b32 v238, v236\n"
    "v_mov_b32 v239, v237\n"
    "v_dot2c_f32_bf16 v238, v64, v64\n"
    "v_dot2c_f32_bf16 v239, v66, v66\n"
    "v_dot2c_f32_bf16 v238, v65, v65\n"
    "v_dot2c_f32_bf16 v239, v67, v67\n"
    "v_dot2c_f32_bf16 v238, v68, v68\n"
    "v_dot2c_f32_bf16 v239, v70, v70\n"
    "v_dot2c_f32_bf16 v238, v69, v69\n"
    "v_dot2c_f32_bf16 v239, v71, v71\n"
    "v_dot2c_f32_bf16 v238, v72, v72\n"
    "v_dot2c_f32_bf16 v239, v74, v74\n"
    "v_dot2c_f32_bf16 v238, v73, v73\n"
    "v_dot2c_f32_bf16 v239, v75, v75\n"
    "v_dot2c_f32_bf16 v238, v76, v76\n"
    "v_dot2c_f32_bf16 v239, v78, v78\n"
    "v_dot2c_f32_bf16 v238, v77, v77\n"
    "v_dot2c_f32_bf16 v239, v79, v79\n"
    "v_dot2c_f32_bf16 v238, v80, v80\n"
    "v_dot2c_f32_bf16 v239, v82, v82\n"
    "v_dot2c_f32_bf16 v238, v81, v81\n"
    "v_dot2c_f32_bf16 v239, v83, v83\n"
    "v_dot2c_f32_bf16 v238, v84, v84\n"
    "v_dot2c_f32_bf16 v239, v86, v86\n"
    "v_dot2c_f32_bf16 v238, v85, v85\n"
    "v_dot2c_f32_bf16 v239, v87, v87\n"
    "v_dot2c_f32_bf16 v238, v88, v88\n"
    "v_dot2c_f32_bf16 v239, v90, v90\n"
    "v_dot2c_f32_bf16 v238, v89, v89\n"
    "v_dot2c_f32_bf16 v239, v91, v91\n"
    "v_dot2c_f32_bf16 v238, v92, v92\n"
    "v_dot2c_f32_bf16 v239, v94, v94\n"
    "v_dot2c_f32_bf16 v238, v93, v93\n"
    "v_dot2c_f32_bf16 v239, v95, v95\n"
    "v_swap_b32 v66, v68\n"
    "v_swap_b32 v67, v69\n"
    "s_nop 3\n"
    "s_branch .Lrr2\n"
    ".Lp4:\n"
    "s_waitcnt vmcnt(0)\n"
    "s_mov_b32 s57, 65536\n"
    ".Lpl4:\n"
    "buffer_load_dwordx4 v[216:219], v231, s[4:7], s12 offen offset:0 sc1\n"
    "s_waitcnt vmcnt(0)\n"
    "v_cmp_ne_u32 vcc, v216, v253\n"
    "s_nop 0\n"
    "v_cmp_ne_u32 s[58:59], v217, v253\n"
    "s_nop 0\n"
    "s_or_b64 vcc, vcc, s[58:59]\n"
    "v_cmp_ne_u32 s[58:59], v218, v253\n"
    "s_nop 0\n"
    "s_or_b64 vcc, vcc, s[58:59]\n"
    "v_cmp_ne_u32 s[58:59], v219, v253\n"
    "s_nop 0\n"
    "s_or_b64 vcc, vcc, s[58:59]\n"
    "s_or_b32 s58, vcc_lo, vcc_hi\n"
    "s_cbranch_scc0 .Lpr4\n"
    "s_sub_u32 s57, s57, 1\n"
    "s_cmp_lg_u32 s57, 0\n"
    "s_cbranch_scc1 .Lpl4\n"
    "s_branch .Lpr4\n"
    ".Lre3:\n"
    "s_waitcnt vmcnt(0)\n"
    "v_and_b32 v228, 31, v255\n"
    "v_lshlrev_b32 v228, 2, v228\n"
    "buffer_store_dword v253, v228, s[4:7], s1 offen sc1\n"
    "v_swap_b32 v98, v100\n"
    "v_swap_b32 v99, v101\n"
    "s_mov_b32 s57, 65544\n"
    "s_mov_b32 s56, 20398080\n"
    "v_min_u32 v228, 39, v255\n"
    "v_lshlrev_b32 v228, 9, v228\n"
    ".Lrl3:\n"
    "v_mov_b32 v232, 0\n"
    "v_or3_b32 v232, v96, v97, v232\n"
    "v_or3_b32 v232, v98, v99, v232\n"
    "v_or3_b32 v232, v100, v101, v232\n"
    "v_or3_b32 v232, v102, v103, v232\n"
    "v_or3_b32 v232, v104, v105, v232\n"
    "v_or3_b32 v232, v106, v107, v232\n"
    "v_or3_b32 v232, v108, v109, v232\n"
    "v_or3_b32 v232, v110, v111, v232\n"
    "v_or3_b32 v232, v112, v113, v232\n"
    "v_or3_b32 v232, v114, v115, v232\n"
    "v_or3_b32 v232, v116, v117, v232\n"
    "v_or3_b32 v232, v118, v119, v232\n"
    "v_or3_b32 v232, v120, v121, v232\n"
    "v_or3_b32 v232, v122, v123, v232\n"
    "v_or3_b32 v232, v124, v125, v232\n"
    "v_or3_b32 v232, v126, v127, v232\n"
    "v_and_b32 v232, 0x4000, v232\n"
    "v_cmp_ne_u32 vcc, 0, v232\n"
    "s_nop 0\n"
    "s_cbranch_vccz .Lrd3\n"
    "s_and_b32 s58, s57, 63\n"
    "s_mov_b32 s58, 1\n"
    "s_cbranch_scc1 .Lrf3\n"
    "buffer_load_dword v224, v228, s[4:7], s56 offen offset:0 sc1\n"
    "buffer_load_dword v225, v228, s[4:7], s56 offen offset:128 sc1\n"
    "buffer_load_dword v226, v228, s[4:7], s56 offen offset:256 sc1\n"
    "buffer_load_dword v227, v228, s[4:7], s56 offen offset:384 sc1\n"
    "s_waitcnt vmcnt(0)\n"
    "v_cmp_ne_u32 vcc, v224, v253\n"
    "v_cmp_ne_u32 s[58:59], v225, v253\n"
    "s_nop 0\n"
    "s_or_b64 vcc, vcc, s[58:59]\n"
    "v_cmp_ne_u32 s[58:59], v226, v253\n"
    "s_nop 0\n"
    "s_or_b64 vcc, vcc, s[58:59]\n"
    "v_cmp_ne_u32 s[58:59], v227, v253\n"
    "s_nop 0\n"
    "s_or_b64 vcc, vcc, s[58:59]\n"
    "s_and_b32 s59, vcc_hi, 0xff\n"
    "s_or_b32 s58, vcc_lo, s59\n"
    ".Lrf3:\n"
    "s_add_u32 s41, s40, 1572864\n"
    "buffer_load_dwordx4 v[96:99], v244, s[4:7], s41 offen sc1\n"
    "s_add_u32 s41, s40, 1589248\n"
    "buffer_load_dwordx4 v[100:103], v244, s[4:7], s41 offen sc1\n"
    "s_add_u32 s41, s40, 1703936\n"
    "buffer_load_dwordx4 v[104:107], v244, s[4:7], s41 offen sc1\n"
    "s_add_u32 s41, s40, 1720320\n"
    "buffer_load_dwordx4 v[108:111], v244, s[4:7], s41 offen sc1\n"
    "s_add_u32 s41, s40, 1835008\n"
    "buffer_load_dwordx4 v[112:115], v244, s[4:7], s41 offen sc1\n"
    "s_add_u32 s41, s40, 1851392\n"
    "buffer_load_dwordx4 v[116:119], v244, s[4:7], s41 offen sc1\n"
    "s_add_u32 s41, s40, 1966080\n"
    "buffer_load_dwordx4 v[120:123], v244, s[4:7], s41 offen sc1\n"
    "s_add_u32 s41, s40, 1982464\n"
    "buffer_load_dwordx4 v[124:127], v244, s[4:7], s41 offen sc1\n"
    "s_waitcnt vmcnt(0)\n"
    "s_cmp_eq_u32 s58, 0\n"
    "s_cbranch_scc1 .Lrd3\n"
    "s_sub_u32 s57, s57, 1\n"
    "s_cmp_lg_u32 s57, 1\n"
    "s_cbranch_scc1 .Lrl3\n"
    ".Lrd3:\n"
    "v_mov_b32 v238, v236\n"
    "v_mov_b32 v239, v237\n"
    "v_dot2c_f32_bf16 v238, v96, v96\n"
    "v_dot2c_f32_bf16 v239, v98, v98\n"
    "v_dot2c_f32_bf16 v238, v97, v97\n"
    "v_dot2c_f32_bf16 v239, v99, v99\n"
    "v_dot2c_f32_bf16 v238, v100, v100\n"
    "v_dot2c_f32_bf16 v239, v102, v102\n"
    "v_dot2c_f32_bf16 v238, v101, v101\n"
    "v_dot2c_f32_bf16 v239, v103, v103\n"
    "v_dot2c_f32_bf16 v238, v104, v104\n"
    "v_dot2c_f32_bf16 v239, v106, v106\n"
    "v_dot2c_f32_bf16 v238, v105, v105\n"
    "v_dot2c_f32_bf16 v239, v107, v107\n"
    "v_dot2c_f32_bf16 v238, v108, v108\n"
    "v_dot2c_f32_bf16 v239, v110, v110\n"
    "v_dot2c_f32_bf16 v238, v109, v109\n"
    "v_dot2c_f32_bf16 v239, v111, v111\n"
    "v_dot2c_f32_bf16 v238, v112, v112\n"
    "v_dot2c_f32_bf16 v239, v114, v114\n"
    "v_dot2c_f32_bf16 v238, v113, v113\n"
    "v_dot2c_f32_bf16 v239, v115, v115\n"
    "v_dot2c_f32_bf16 v238, v116, v116\n"
    "v_dot2c_f32_bf16 v239, v118, v118\n"
    "v_dot2c_f32_bf16 v238, v117, v117\n"
    "v_dot2c_f32_bf16 v239, v119, v119\n"
    "v_dot2c_f32_bf16 v238, v120, v120\n"
    "v_dot2c_f32_bf16 v239, v122, v122\n"
    "v_dot2c_f32_bf16 v238, v121, v121\n"
    "v_dot2c_f32_bf16 v239, v123, v123\n"
    "v_dot2c_f32_bf16 v238, v124, v124\n"
    "v_dot2c_f32_bf16 v239, v126, v126\n"
    "v_dot2c_f32_bf16 v238, v125, v125\n"
    "v_dot2c_f32_bf16 v239, v127, v127\n"
    "v_swap_b32 v98, v100\n"
    "v_swap_b32 v99, v101\n"
    "s_nop 3\n"
    "s_branch .Lrr3\n"
    ".Lre4:\n"
    "s_waitcnt vmcnt(0)\n"
    "v_and_b32 v228, 31, v255\n"
    "v_lshlrev_b32 v228, 2, v228\n"
    "buffer_store_dword v253, v228, s[4:7], s1 offen sc1\n"
    "v_swap_b32 v130, v132\n"
    "v_swap_b32 v131, v133\n"
    "s_mov_b32 s57, 65544\n"
    "s_mov_b32 s56, 20398080\n"
    "v_min_u32 v228, 39, v255\n"
    "v_lshlrev_b32 v228, 9, v228\n"
    ".Lrl4:\n"
    "v_mov_b32 v232, 0\n"
    "v_or3_b32 v232, v128, v129, v232\n"
    "v_or3_b32 v232, v130, v131, v232\n"
    "v_or3_b32 v232, v132, v133, v232\n"
    "v_or3_b32 v232, v134, v135, v232\n"
    "v_or3_b32 v232, v136, v137, v232\n"
    "v_or3_b32 v232, v138, v139, v232\n"
    "v_or3_b32 v232, v140, v141, v232\n"
    "v_or3_b32 v232, v142, v143, v232\n"
    "v_or3_b32 v232, v144, v145, v232\n"
    "v_or3_b32 v232, v146, v147, v232\n"
    "v_or3_b32 v232, v148, v149, v232\n"
    "v_or3_b32 v232, v150, v151, v232\n"
    "v_or3_b32 v232, v152, v153, v232\n"
    "v_or3_b32 v232, v154, v155, v232\n"
    "v_or3_b32 v232, v156, v157, v232\n"
    "v_or3_b32 v232, v158, v159, v232\n"
    "v_and_b32 v232, 0x4000, v232\n"
    "v_cmp_ne_u32 vcc, 0, v232\n"
    "s_nop 0\n"
    "s_cbranch_vccz .Lrd4\n"
    "s_and_b32 s58, s57, 63\n"
    "s_mov_b32 s58, 1\n"
    "s_cbranch_scc1 .Lrf4\n"
    "buffer_load_dword v224, v228, s[4:7], s56 offen offset:0 sc1\n"
    "buffer_load_dword v225, v228, s[4:7], s56 offen offset:128 sc1\n"
    "buffer_load_dword v226, v228, s[4:7], s56 offen offset:256 sc1\n"
    "buffer_load_dword v227, v228, s[4:7], s56 offen offset:384 sc1\n"
    "s_waitcnt vmcnt(0)\n"
    "v_cmp_ne_u32 vcc, v224, v253\n"
    "v_cmp_ne_u32 s[58:59], v225, v253\n"
    "s_nop 0\n"
    "s_or_b64 vcc, vcc, s[58:59]\n"
    "v_cmp_ne_u32 s[58:59], v226, v253\n"
    "s_nop 0\n"
    "s_or_b64 vcc, vcc, s[58:59]\n"
    "v_cmp_ne_u32 s[58:59], v227, v253\n"
    "s_nop 0\n"
    "s_or_b64 vcc, vcc, s[58:59]\n"
    "s_and_b32 s59, vcc_hi, 0xff\n"
    "s_or_b32 s58, vcc_lo, s59\n"
    ".Lrf4:\n"
    "s_add_u32 s41, s40, 2097152\n"
    "buffer_load_dwordx4 v[128:131], v244, s[4:7], s41 offen sc1\n"
    "s_add_u32 s41, s40, 2113536\n"
    "buffer_load_dwordx4 v[132:135], v244, s[4:7], s41 offen sc1\n"
    "s_add_u32 s41, s40, 2228224\n"
    "buffer_load_dwordx4 v[136:139], v244, s[4:7], s41 offen sc1\n"
    "s_add_u32 s41, s40, 2244608\n"
    "buffer_load_dwordx4 v[140:143], v244, s[4:7], s41 offen sc1\n"
    "s_add_u32 s41, s40, 2359296\n"
    "buffer_load_dwordx4 v[144:147], v244, s[4:7], s41 offen sc1\n"
    "s_add_u32 s41, s40, 2375680\n"
    "buffer_load_dwordx4 v[148:151], v244, s[4:7], s41 offen sc1\n"
    "s_add_u32 s41, s40, 2490368\n"
    "buffer_load_dwordx4 v[152:155], v244, s[4:7], s41 offen sc1\n"
    "s_add_u32 s41, s40, 2506752\n"
    "buffer_load_dwordx4 v[156:159], v244, s[4:7], s41 offen sc1\n"
    "s_waitcnt vmcnt(0)\n"
    "s_cmp_eq_u32 s58, 0\n"
    "s_cbranch_scc1 .Lrd4\n"
    "s_sub_u32 s57, s57, 1\n"
    "s_cmp_lg_u32 s57, 1\n"
    "s_cbranch_scc1 .Lrl4\n"
    ".Lrd4:\n"
    "v_mov_b32 v238, v236\n"
    "v_mov_b32 v239, v237\n"
    "v_dot2c_f32_bf16 v238, v128, v128\n"
    "v_dot2c_f32_bf16 v239, v130, v130\n"
    "v_dot2c_f32_bf16 v238, v129, v129\n"
    "v_dot2c_f32_bf16 v239, v131, v131\n"
    "v_dot2c_f32_bf16 v238, v132, v132\n"
    "v_dot2c_f32_bf16 v239, v134, v134\n"
    "v_dot2c_f32_bf16 v238, v133, v133\n"
    "v_dot2c_f32_bf16 v239, v135, v135\n"
    "v_dot2c_f32_bf16 v238, v136, v136\n"
    "v_dot2c_f32_bf16 v239, v138, v138\n"
    "v_dot2c_f32_bf16 v238, v137, v137\n"
    "v_dot2c_f32_bf16 v239, v139, v139\n"
    "v_dot2c_f32_bf16 v238, v140, v140\n"
    "v_dot2c_f32_bf16 v239, v142, v142\n"
    "v_dot2c_f32_bf16 v238, v141, v141\n"
    "v_dot2c_f32_bf16 v239, v143, v143\n"
    "v_dot2c_f32_bf16 v238, v144, v144\n"
    "v_dot2c_f32_bf16 v239, v146, v146\n"
    "v_dot2c_f32_bf16 v238, v145, v145\n"
    "v_dot2c_f32_bf16 v239, v147, v147\n"
    "v_dot2c_f32_bf16 v238, v148, v148\n"
    "v_dot2c_f32_bf16 v239, v150, v150\n"
    "v_dot2c_f32_bf16 v238, v149, v149\n"
    "v_dot2c_f32_bf16 v239, v151, v151\n"
    "v_dot2c_f32_bf16 v238, v152, v152\n"
    "v_dot2c_f32_bf16 v239, v154, v154\n"
    "v_dot2c_f32_bf16 v238, v153, v153\n"
    "v_dot2c_f32_bf16 v239, v155, v155\n"
    "v_dot2c_f32_bf16 v238, v156, v156\n"
    "v_dot2c_f32_bf16 v239, v158, v158\n"
    "v_dot2c_f32_bf16 v238, v157, v157\n"
    "v_dot2c_f32_bf16 v239, v159, v159\n"
    "v_swap_b32 v130, v132\n"
    "v_swap_b32 v131, v133\n"
    "s_nop 3\n"
    "s_branch .Lrr4\n"
    ".Lp5:\n"
    "s_waitcnt vmcnt(0)\n"
    "v_and_b32 v246, 31, v255\n"
    "v_lshlrev_b32 v246, 2, v246\n"
    "buffer_store_dword v253, v246, s[4:7], s1 offen sc1\n"
    "s_mov_b32 s57, 65536\n"
    ".Lpl5:\n"
    "buffer_load_dword v246, v248, s[4:7], s38 offen offset:0 sc1\n"
    "s_waitcnt vmcnt(0)\n"
    "v_cmp_ne_u32 vcc, v246, v253\n"
    "s_nop 0\n"
    "s_and_b32 s58, vcc_lo, 0xffffffff\n"
    "s_cbranch_scc0 .Lpr5\n"
    "s_sub_u32 s57, s57, 1\n"
    "s_cmp_lg_u32 s57, 0\n"
    "s_cbranch_scc1 .Lpl5\n"
    "s_branch .Lpr5\n"
    ".Lp6:\n"
    "s_waitcnt vmcnt(0)\n"
    "s_add_u32 s47, s45, 12288\n"
    "buffer_store_dword v253, v244, s[4:7], s47 offen sc1\n"
    "v_and_b32 v252, 31, v255\n"
    "v_lshlrev_b32 v252, 2, v252\n"
    "buffer_store_dword v253, v252, s[4:7], s1 offen sc1\n"
    "s_mov_b32 s57, 65536\n"
    ".Lpl6:\n"
    "buffer_load_dword v252, v248, s[4:7], s19 offen offset:0 sc1\n"
    "s_waitcnt vmcnt(0)\n"
    "v_cmp_ne_u32 vcc, v252, v253\n"
    "s_nop 0\n"
    "s_and_b32 s58, vcc_lo, 0xffffffff\n"
    "s_cbranch_scc0 .Lpr6\n"
    "s_sub_u32 s57, s57, 1\n"
    "s_cmp_lg_u32 s57, 0\n"
    "s_cbranch_scc1 .Lpl6\n"
    "s_branch .Lpr6\n"
    ".Lp7:\n"
    "s_waitcnt vmcnt(0)\n"
    "v_and_b32 v246, 31, v255\n"
    "v_lshlrev_b32 v246, 2, v246\n"
    "buffer_store_dword v253, v246, s[4:7], s1 offen sc1\n"
    "s_mov_b32 s57, 65536\n"
    ".Lpl7:\n"
    "buffer_load_dword v246, v248, s[4:7], s38 offen offset:0 sc1\n"
    "s_waitcnt vmcnt(0)\n"
    "v_cmp_ne_u32 vcc, v246, v253\n"
    "s_nop 0\n"
    "s_and_b32 s58, vcc_lo, 0xffffffff\n"
    "s_cbranch_scc0 .Lpr7\n"
    "s_sub_u32 s57, s57, 1\n"
    "s_cmp_lg_u32 s57, 0\n"
    "s_cbranch_scc1 .Lpl7\n"
    "s_branch .Lpr7\n"
    ".Lp8:\n"
    "s_waitcnt vmcnt(0)\n"
    "v_and_b32 v159, 31, v255\n"
    "v_lshlrev_b32 v159, 2, v159\n"
    "buffer_store_dword v253, v159, s[4:7], s1 offen sc1\n"
    "s_mov_b32 s57, 65536\n"
    ".Lpl8:\n"
    "buffer_load_dword v159, v158, s[4:7], s39 offen offset:0 sc1\n"
    "s_waitcnt vmcnt(0)\n"
    "v_cmp_ne_u32 vcc, v159, v253\n"
    "s_nop 0\n"
    "s_and_b32 s58, vcc_lo, 0x1ff\n"
    "s_cbranch_scc0 .Lpr8\n"
    "s_sub_u32 s57, s57, 1\n"
    "s_cmp_lg_u32 s57, 0\n"
    "s_cbranch_scc1 .Lpl8\n"
    "s_branch .Lpr8\n"
    ".Lend:\n"
    : "+{v0}"(tid), "+{s[0:1]}"(karg) : "{s2}"(bid)
    : "v1", "v2", "v3", "v4", "v5", "v6", "v7", "v8", "v9", "v10", "v11", "v12", "v13", "v14", "v15", "v16", "v17", "v18", "v19", "v20", "v21", "v22", "v23", "v24", "v25", "v26", "v27", "v28", "v29", "v30", "v31", "v32", "v33", "v34", "v35", "v36", "v37", "v38", "v39", "v40", "v41", "v42", "v43", "v44", "v45", "v46", "v47", "v48", "v49", "v50", "v51", "v52", "v53", "v54", "v55", "v56", "v57", "v58", "v59", "v60", "v61", "v62", "v63", "v64", "v65", "v66", "v67", "v68", "v69", "v70", "v71", "v72", "v73", "v74", "v75", "v76", "v77", "v78", "v79", "v80", "v81", "v82", "v83", "v84", "v85", "v86", "v87", "v88", "v89", "v90", "v91", "v92", "v93", "v94", "v95", "v96", "v97", "v98", "v99", "v100", "v101", "v102", "v103", "v104", "v105", "v106", "v107", "v108", "v109", "v110", "v111", "v112", "v113", "v114", "v115", "v116", "v117", "v118", "v119", "v120", "v121", "v122", "v123", "v124", "v125", "v126", "v127", "v128", "v129", "v130", "v131", "v132", "v133", "v134", "v135", "v136", "v137", "v138", "v139", "v140", "v141", "v142", "v143", "v144", "v145", "v146", "v147", "v148", "v149", "v150", "v151", "v152", "v153", "v154", "v155", "v156", "v157", "v158", "v159", "v160", "v161", "v162", "v163", "v164", "v165", "v166", "v167", "v168", "v169", "v170", "v171", "v172", "v173", "v174", "v175", "v176", "v177", "v178", "v179", "v180", "v181", "v182", "v183", "v184", "v185", "v186", "v187", "v188", "v189", "v190", "v191", "v192", "v193", "v194", "v195", "v196", "v197", "v198", "v199", "v200", "v201", "v202", "v203", "v204", "v205", "v206", "v207", "v208", "v209", "v210", "v211", "v212", "v213", "v214", "v215", "v216", "v217", "v218", "v219", "v220", "v221", "v222", "v223", "v224", "v225", "v226", "v227", "v228", "v229", "v230", "v231", "v232", "v233", "v234", "v235", "v236", "v237", "v238", "v239", "v240", "v241", "v242", "v243", "v244", "v245", "v246", "v247", "v248", "v249", "v250", "v251", "v252", "v253", "v254", "v255", "a0", "a1", "a2", "a3", "a4", "a5", "a6", "a7", "a8", "a9", "a10", "a11", "a12", "a13", "a14", "a15", "a16", "a17", "a18", "a19", "a20", "a21", "a22", "a23", "a24", "a25", "a26", "a27", "a28", "a29", "a30", "a31", "a32", "a33", "a34", "a35", "a36", "a37", "a38", "a39", "a40", "a41", "a42", "a43", "a44", "a45", "a46", "a47", "a48", "a49", "a50", "a51", "a52", "a53", "a54", "a55", "a56", "a57", "a58", "a59", "a60", "a61", "a62", "a63", "a64", "a65", "a66", "a67", "a68", "a69", "a70", "a71", "a72", "a73", "a74", "a75", "a76", "a77", "a78", "a79", "a80", "a81", "a82", "a83", "a84", "a85", "a86", "a87", "a88", "a89", "a90", "a91", "a92", "a93", "a94", "a95", "a96", "a97", "a98", "a99", "a100", "a101", "a102", "a103", "a104", "a105", "a106", "a107", "a108", "a109", "a110", "a111", "a112", "a113", "a114", "a115", "a116", "a117", "a118", "a119", "a120", "a121", "a122", "a123", "a124", "a125", "a126", "a127", "a128", "a129", "a130", "a131", "a132", "a133", "a134", "a135", "a136", "a137", "a138", "a139", "a140", "a141", "a142", "a143", "a144", "a145", "a146", "a147", "a148", "a149", "a150", "a151", "a152", "a153", "a154", "a155", "a156", "a157", "a158", "a159", "a160", "a161", "a162", "a163", "a164", "a165", "a166", "a167", "a168", "a169", "a170", "a171", "a172", "a173", "a174", "a175", "a176", "a177", "a178", "a179", "a180", "a181", "a182", "a183", "a184", "a185", "a186", "a187", "a188", "a189", "a190", "a191", "a192", "a193", "a194", "a195", "a196", "a197", "a198", "a199", "a200", "a201", "a202", "a203", "a204", "a205", "a206", "a207", "a208", "a209", "a210", "a211", "a212", "a213", "a214", "a215", "a216", "a217", "a218", "a219", "a220", "a221", "a222", "a223", "a224", "a225", "a226", "a227", "a228", "a229", "a230", "a231", "a232", "a233", "a234", "a235", "a236", "a237", "a238", "a239", "a240", "a241", "a242", "a243", "a244", "a245", "a246", "a247", "a248", "a249", "a250", "a251", "a252", "a253", "a254", "a255", "s3", "s4", "s5", "s6", "s7", "s8", "s9", "s10", "s11", "s12", "s13", "s14", "s15", "s16", "s17", "s18", "s19", "s20", "s21", "s22", "s23", "s24", "s25", "s26", "s27", "s28", "s29", "s30", "s31", "s32", "s33", "s34", "s35", "s36", "s37", "s38", "s39", "s40", "s41", "s42", "s43", "s44", "s45", "s46", "s47", "s48", "s49", "s50", "s51", "s52", "s53", "s54", "s55", "s56", "s57", "s58", "s59", "s60", "s61", "s62", "s63", "s64", "s65", "s66", "s67", "s68", "s69", "s70", "s71", "s72", "s73", "s74", "s75", "s76", "s77", "s78", "s79", "s80", "s81", "s82", "s83", "s84", "s85", "s86", "s87", "s88", "s89", "s90", "s91", "s92", "s93", "s94", "s95", "s96", "s97", "s98", "s99", "s100", "s101", "vcc", "scc", "memory");
}

extern "C" void kernel_launch(void* const* d_in, const int* in_sizes, int n_in, void* d_out, int out_size, void* d_ws, size_t ws_size,
                              hipStream_t stream) {
  (void)hipFuncSetAttribute((const void*)fused, hipFuncAttributeMaxDynamicSharedMemorySize, 163840);
  fused<<<256, 256, 163840, stream>>>(d_in[0], d_in[1], d_in[2], d_in[4], d_in[5], (char*)d_ws + WS_OFF, d_out);
}
```
